```python
import torch

def _softmax(x, axis=-1):
    return torch.softmax(x, dim=axis)
def _gelu(x):
    return torch.nn.functional.gelu(x, approximate="tanh")
def _silu(x):
    return torch.nn.functional.silu(x)
def _sigmoid(x):
    return torch.sigmoid(x)
def _layernorm(x, axis=-1, eps=1e-5):
    m = x.mean(dim=axis, keepdim=True)
    v = x.var(dim=axis, keepdim=True, unbiased=False)
    return (x - m) / torch.sqrt(v + eps)
def _scatter_add(tgt, idx, src, axis=0):
    return tgt.clone().index_add_(axis, idx, src)
def _segment_sum(src, seg, n):
    out = torch.zeros((n,) + src.shape[1:], dtype=src.dtype)
    return out.index_add_(0, seg, src)
def _pad(x, pw):
    flat = tuple(v for pair in reversed(pw) for v in pair)
    return torch.nn.functional.pad(x, flat)

kernel_name = 'standalone__weight_only_int_quip__prompt'

def setup_inputs():

    _w = (lambda a: (lambda t: t.to(torch.bfloat16) if t.is_floating_point() else t)(torch.as_tensor(a)))
    B, S, D, F = 1, 2048, 640, 1536
    G = 32 if 32 > 0 else D
    D = (D // G) * G
    ng = D // G
    bits = 3 if 3 > 0 else 4
    qmax = 1 << bits
    qw = torch.randint(0, qmax, (D, F)).to(torch.int32)
    U, _ = (lambda *_a, **_k: torch.linalg.qr(*[torch.as_tensor(_x).float() for _x in _a], **_k))((lambda *s: torch.randn(*s, dtype=torch.bfloat16) * 0.05)(D, D))
    V, _ = (lambda *_a, **_k: torch.linalg.qr(*[torch.as_tensor(_x).float() for _x in _a], **_k))((lambda *s: torch.randn(*s, dtype=torch.bfloat16) * 0.05)(F, F))
    return {
        "x": _w((lambda *s: torch.randn(*s, dtype=torch.bfloat16) * 0.05)(B, S, D)),
        "qw": _w(qw),
        "scales": _w(torch.abs((lambda *s: torch.randn(*s, dtype=torch.bfloat16) * 0.05)(ng, F)) * 0.02 + 0.005),
        "zeros": _w(torch.full((ng, F), qmax / 2.0, dtype=float)),
        "U": _w(U.to(float)),
        "V": _w(V.to(float)),
    }

def reference(x, qw, scales, zeros, U, V):
    B, S, D = x.shape
    ng, F = scales.shape
    G = D // ng
    s = (lambda x, n, axis: torch.repeat_interleave(x, n, dim=axis))(scales, G, axis=0)
    z = (lambda x, n, axis: torch.repeat_interleave(x, n, dim=axis))(zeros, G, axis=0)
    wq = ((lambda x, dt: x.to(dt))(qw, x.dtype) - z) * s
    xr = torch.einsum("bsd,de->bse", x, U)
    h  = torch.einsum("bse,ef->bsf", xr, wq)
    y  = torch.einsum("bsf,fg->bsg", h, V)
    return y
```

```cpp
#include <hip/hip_runtime.h>
#include <hip/hip_bf16.h>

typedef __attribute__((ext_vector_type(8))) __bf16 bf16x8;
typedef __attribute__((ext_vector_type(2))) __bf16 bf16x2;
typedef __attribute__((ext_vector_type(4))) short s16x4;
typedef __attribute__((ext_vector_type(16))) float f32x16;
typedef __attribute__((ext_vector_type(4))) float f32x4;
typedef __attribute__((ext_vector_type(4))) int i32x4;
typedef __attribute__((ext_vector_type(4))) unsigned u32x4;
typedef __attribute__((ext_vector_type(2))) unsigned u32x2;
typedef __attribute__((address_space(3))) unsigned lds_u32;
typedef __attribute__((address_space(3))) s16x4 lds_s16x4;
typedef __attribute__((address_space(3))) char lds_char;

constexpr int S_ = 2048, D_ = 640, F_ = 1536;

#define WAIT_V(n) asm volatile("s_waitcnt vmcnt(" #n ")" ::: "memory")
#define WAIT_L(n) asm volatile("s_waitcnt lgkmcnt(" #n ")" ::: "memory")
#define BAR() __builtin_amdgcn_s_barrier()
__device__ __forceinline__ void glds(const void* sbase, unsigned voff, unsigned ldsaddr) {
  asm volatile("s_mov_b32 m0, %2\n\ts_nop 0\n\tglobal_load_lds_dwordx4 %0, %1" ::"v"(voff), "s"(sbase), "s"(ldsaddr) : "memory");
}

__device__ __forceinline__ unsigned pack2(float a, float b) {
  bf16x2 p; p[0] = (__bf16)a; p[1] = (__bf16)b;
  return __builtin_bit_cast(unsigned, p);
}
__device__ __forceinline__ float bflo(unsigned u) { return __builtin_bit_cast(float, u << 16); }
__device__ __forceinline__ float bfhi(unsigned u) { return __builtin_bit_cast(float, u & 0xffff0000u); }
__device__ __forceinline__ u32x2 trread(lds_char* p) {
  s16x4 v = __builtin_amdgcn_ds_read_tr16_b64_v4i16((lds_s16x4*)p);
  return __builtin_bit_cast(u32x2, v);
}
template <typename T> __device__ __forceinline__ T ldsr(lds_char* p) { return *(__attribute__((address_space(3))) T*)p; }
template <typename T> __device__ __forceinline__ void ldsw(lds_char* p, T v) { *(__attribute__((address_space(3))) T*)p = v; }
__device__ __forceinline__ f32x16 mfma32(u32x4 a, u32x4 b, f32x16 c) {
  return __builtin_amdgcn_mfma_f32_32x32x16_bf16(__builtin_bit_cast(bf16x8, a), __builtin_bit_cast(bf16x8, b), c, 0, 0, 0);
}
__device__ __forceinline__ u32x4 pack8(const f32x16& a, int s) {
  u32x4 r;
  r[0] = pack2(a[8 * s + 0], a[8 * s + 1]); r[1] = pack2(a[8 * s + 2], a[8 * s + 3]);
  r[2] = pack2(a[8 * s + 4], a[8 * s + 5]); r[3] = pack2(a[8 * s + 6], a[8 * s + 7]);
  return r;
}

constexpr int K1_LDS = 163840;
__global__ __launch_bounds__(512) void k1(const __hip_bfloat16* __restrict__ x, const int* __restrict__ qw,
                                          const __hip_bfloat16* __restrict__ scales, const __hip_bfloat16* __restrict__ zeros,
                                          const __hip_bfloat16* __restrict__ U, const __hip_bfloat16* __restrict__ V,
                                          char* __restrict__ TF, char* __restrict__ xrF) {
  extern __shared__ __attribute__((aligned(16))) char smem_[];
  lds_char* smem = (lds_char*)smem_;
  const int wid = __builtin_amdgcn_readfirstlane(threadIdx.x >> 6), lane = threadIdx.x & 63;
  const int xcd = blockIdx.x & 7, idx = blockIdx.x >> 3;
  const int h = lane >> 5, r = lane & 31;
  const int blk = (lane >> 4) & 1, q4 = (lane & 15) >> 2, p4 = lane & 3;

  if (idx < 20) {
    const int mt = (xcd & 1) * 5 + idx % 5, nb = (xcd >> 1) * 4 + idx / 5;
    const int m = wid & 1, kg = wid >> 1;
    const int grp = mt * 2 + m;
    lds_char* TAB = smem + wid * 3072;
    lds_char* AR = smem + 24576 + wid * 8192;
    lds_char* BR = smem + 24576 + 65536 + kg * 18432;
    const int lc = lane < 48 ? lane : 47;
    const unsigned lb = (unsigned)(size_t)smem;
    glds((const char*)scales + (size_t)(grp * F_ + kg * 384) * 2, lc * 16, lb + wid * 3072);
    glds((const char*)zeros + (size_t)(grp * F_ + kg * 384) * 2, lc * 16, lb + wid * 3072 + 1024);
    unsigned ag[4];
#pragma unroll
    for (int i = 0; i < 4; ++i) {
      int row = i * 8 + (lane >> 3);
      int ch = (lane & 7) ^ ((row >> 1) & 7);
      ag[i] = row * (F_ * 4) + ch * 16;
    }
    unsigned bg[3];
#pragma unroll
    for (int i = 0; i < 3; ++i) {
      int off = (m * 3 + i) * 1024 + lane * 16;
      bg[i] = (off / 192) * (F_ * 2) + off % 192;
    }
    const char* abase = (const char*)qw + (size_t)(grp * 32) * (F_ * 4) + kg * 384 * 4;
    const char* bbase = (const char*)V + (size_t)(kg * 384) * (F_ * 2) + nb * 192;
    auto loadA = [&](int rd, int slot) {
#pragma unroll
      for (int i = 0; i < 4; ++i) glds(abase + rd * 128, ag[i], lb + 24576 + wid * 8192 + slot * 4096 + i * 1024);
    };
    auto loadB = [&](int rd, int slot) {
#pragma unroll
      for (int i = 0; i < 3; ++i) glds(bbase + (size_t)rd * 32 * (F_ * 2), bg[i], lb + 24576 + 65536 + kg * 18432 + slot * 6144 + (m * 3 + i) * 1024);
    };
    loadB(0, 0); loadA(0, 0); loadB(1, 1); loadA(1, 1);
    WAIT_V(14);
    {
      u32x4 s8 = ldsr<u32x4>(TAB + lc * 16), z8 = ldsr<u32x4>(TAB + 1024 + lc * 16);
      f32x4 sa, sb, za, zb;
      sa[0] = bflo(s8[0]); sa[1] = bfhi(s8[0]); sa[2] = bflo(s8[1]); sa[3] = bfhi(s8[1]);
      sb[0] = bflo(s8[2]); sb[1] = bfhi(s8[2]); sb[2] = bflo(s8[3]); sb[3] = bfhi(s8[3]);
      za[0] = -bflo(z8[0]) * sa[0]; za[1] = -bfhi(z8[0]) * sa[1]; za[2] = -bflo(z8[1]) * sa[2]; za[3] = -bfhi(z8[1]) * sa[3];
      zb[0] = -bflo(z8[2]) * sb[0]; zb[1] = -bfhi(z8[2]) * sb[1]; zb[2] = -bflo(z8[3]) * sb[2]; zb[3] = -bfhi(z8[3]) * sb[3];
      ldsw<f32x4>(TAB + lc * 32, sa); ldsw<f32x4>(TAB + lc * 32 + 16, sb);
      ldsw<f32x4>(TAB + 1536 + lc * 32, za); ldsw<f32x4>(TAB + 1536 + lc * 32 + 16, zb);
    }
    int aoff[4];
#pragma unroll
    for (int c = 0; c < 4; ++c) {
      int ch = 4 * (c >> 1) + 2 * h + (c & 1);
      aoff[c] = r * 128 + ((ch ^ ((r >> 1) & 7)) * 16);
    }
    const int boff = (8 * h + q4) * 192 + 32 * (p4 & 1) + 16 * blk + 8 * (p4 >> 1);
    f32x16 acc[3] = {};
    int bs = 0;
#pragma unroll 1
    for (int rd = 0; rd < 12; ++rd) {
      if (rd < 11) WAIT_V(7); else WAIT_V(0);
      BAR();
      int bs2 = bs == 0 ? 2 : bs - 1;
      if (rd < 10) loadB(rd + 2, bs2);
      lds_char* As = AR + (rd & 1) * 4096;
      lds_char* Bs = BR + bs * 6144 + boff;
      lds_char* Ts = TAB + (rd * 32 + 8 * h) * 4;
      i32x4 a[4];
#pragma unroll
      for (int c = 0; c < 4; ++c) a[c] = ldsr<i32x4>(As + aoff[c]);
      f32x4 ts[4], tz[4];
#pragma unroll
      for (int c = 0; c < 4; ++c) {
        ts[c] = ldsr<f32x4>(Ts + (c >> 1) * 64 + (c & 1) * 16);
        tz[c] = ldsr<f32x4>(Ts + 1536 + (c >> 1) * 64 + (c & 1) * 16);
      }
      u32x4 b[2][3];
#pragma unroll
      for (int s = 0; s < 2; ++s)
#pragma unroll
        for (int c = 0; c < 3; ++c) {
          u32x2 lo = trread(Bs + (16 * s) * 192 + 64 * c), hi = trread(Bs + (16 * s + 4) * 192 + 64 * c);
          b[s][c] = u32x4{lo[0], lo[1], hi[0], hi[1]};
        }
      u32x4 af[2];
#pragma unroll
      for (int c = 0; c < 4; ++c) {
        float w0 = __builtin_fmaf((float)a[c][0], ts[c][0], tz[c][0]);
        float w1 = __builtin_fmaf((float)a[c][1], ts[c][1], tz[c][1]);
        float w2 = __builtin_fmaf((float)a[c][2], ts[c][2], tz[c][2]);
        float w3 = __builtin_fmaf((float)a[c][3], ts[c][3], tz[c][3]);
        af[c >> 1][(c & 1) * 2] = pack2(w0, w1);
        af[c >> 1][(c & 1) * 2 + 1] = pack2(w2, w3);
      }
      WAIT_L(0);
      if (rd < 10) loadA(rd + 2, rd & 1);
#pragma unroll
      for (int s = 0; s < 2; ++s)
#pragma unroll
        for (int c = 0; c < 3; ++c) acc[c] = mfma32(af[s], b[s][c], acc[c]);
      bs = bs == 2 ? 0 : bs + 1;
    }
    BAR();
    lds_char* RED = smem + m * 49152;
#pragma unroll
    for (int c = 0; c < 3; ++c) {
      if (c != kg) {
#pragma unroll
        for (int j = 0; j < 4; ++j)
          ldsw<f32x4>(RED + (kg * 3 + c) * 4096 + j * 1024 + lane * 16, f32x4{acc[c][4 * j], acc[c][4 * j + 1], acc[c][4 * j + 2], acc[c][4 * j + 3]});
      }
    }
    WAIT_L(0);
    BAR();
    if (kg < 3) {
      f32x16 o = kg == 0 ? acc[0] : (kg == 1 ? acc[1] : acc[2]);
#pragma unroll
      for (int i = 1; i < 4; ++i) {
        int src = (kg + i) & 3;
#pragma unroll
        for (int j = 0; j < 4; ++j) {
          f32x4 v = ldsr<f32x4>(RED + (src * 3 + kg) * 4096 + j * 1024 + lane * 16);
          o[4 * j] += v[0]; o[4 * j + 1] += v[1]; o[4 * j + 2] += v[2]; o[4 * j + 3] += v[3];
        }
      }
      char* dst = TF + ((size_t)(nb * 40 + grp * 2) * 3 + kg) * 1024 + lane * 16;
      *(u32x4*)dst = pack8(o, 0);
      *(u32x4*)(dst + 3072) = pack8(o, 1);
    }
  } else if (idx < 30) {
    const int j = idx - 20;
    const int mb = xcd * 2 + (j & 1), eb = j >> 1;
    const int we = wid & 1, wm = wid >> 1;
    unsigned xg[2], ug[2];
    const unsigned lb = (unsigned)(size_t)smem;
#pragma unroll
    for (int i = 0; i < 2; ++i) {
      int row = wid * 16 + i * 8 + (lane >> 3);
      int ch = (lane & 7) ^ ((row >> 1) & 7);
      xg[i] = row * (D_ * 2) + ch * 16;
      int krow = wid * 8 + i * 4 + (lane >> 4);
      int uc = (lane & 15) ^ ((krow & 3) << 2);
      ug[i] = krow * (D_ * 2) + uc * 16;
    }
    auto stage = [&](int st) {
      unsigned base = lb + (st & 3) * 32768 + wid * 2048;
#pragma unroll
      for (int i = 0; i < 2; ++i) {
        glds((const char*)x + (size_t)(mb * 128) * (D_ * 2) + st * 128, xg[i], base + i * 1024);
        glds((const char*)U + eb * 256 + (size_t)st * 64 * (D_ * 2), ug[i], base + 16384 + i * 1024);
      }
    };
    stage(0); stage(1); stage(2);
    int xoff[4];
#pragma unroll
    for (int s = 0; s < 4; ++s) {
      int row = wm * 32 + r;
      xoff[s] = row * 128 + (((2 * s + h) ^ ((row >> 1) & 7)) * 16);
    }
    int uoff[2];
#pragma unroll
    for (int c = 0; c < 2; ++c) {
      int seg = (2 * we + c) ^ q4;
      uoff[c] = 16384 + (8 * h + q4) * 256 + seg * 64 + blk * 32 + p4 * 8;
    }
    f32x16 acc[2] = {};
#pragma unroll 1
    for (int st = 0; st < 10; ++st) {
      if (st < 8) WAIT_V(8); else if (st == 8) WAIT_V(4); else WAIT_V(0);
      BAR();
      if (st < 7) stage(st + 3);
      lds_char* base = smem + (st & 3) * 32768;
#pragma unroll
      for (int s = 0; s < 4; ++s) {
        u32x4 xf = ldsr<u32x4>(base + xoff[s]);
#pragma unroll
        for (int c = 0; c < 2; ++c) {
          u32x2 lo = trread(base + uoff[c] + (16 * s) * 256), hi = trread(base + uoff[c] + (16 * s + 4) * 256);
          acc[c] = mfma32(u32x4{lo[0], lo[1], hi[0], hi[1]}, xf, acc[c]);
        }
      }
      WAIT_L(0);
    }
#pragma unroll
    for (int c = 0; c < 2; ++c) {
      int et = eb * 4 + we * 2 + c, mtile = mb * 4 + wm;
      char* dst = xrF + ((size_t)mtile * 40 + et * 2) * 1024 + lane * 16;
      *(u32x4*)dst = pack8(acc[c], 0);
      *(u32x4*)(dst + 1024) = pack8(acc[c], 1);
    }
  }
}

constexpr int K2_LDS = 122880;
__global__ __launch_bounds__(512) void k2(const char* __restrict__ TF, const char* __restrict__ xrF, __hip_bfloat16* __restrict__ y) {
  extern __shared__ __attribute__((aligned(16))) char smem_[];
  lds_char* smem = (lds_char*)smem_;
  const int wid = __builtin_amdgcn_readfirstlane(threadIdx.x >> 6), lane = threadIdx.x & 63;
  const int xcd = blockIdx.x & 7, idx = blockIdx.x >> 3;
  const int mb = (xcd >> 1) * 4 + (idx & 3), nb = (xcd & 1) * 8 + (idx >> 2);
  if (wid >= 4) {
    const char* src = TF + (size_t)nb * 122880 + (wid - 4) * 3072;
    unsigned dst = (unsigned)(size_t)smem + (wid - 4) * 3072;
#pragma unroll
    for (int ch = 0; ch < 10; ++ch)
#pragma unroll
      for (int i = 0; i < 3; ++i) glds(src + ch * 12288 + i * 1024, lane * 16, dst + ch * 12288 + i * 1024);
    WAIT_V(27); BAR(); WAIT_V(24); BAR(); WAIT_V(21); BAR(); WAIT_V(18); BAR(); WAIT_V(15); BAR();
    WAIT_V(12); BAR(); WAIT_V(9); BAR(); WAIT_V(6); BAR(); WAIT_V(3); BAR(); WAIT_V(0); BAR();
  } else {
    const u32x4* xs = (const u32x4*)(xrF + (size_t)(mb * 4 + wid) * 40960) + lane;
    constexpr int W = 20;
    u32x4 xf[W];
#pragma unroll
    for (int i = 0; i < W; ++i) xf[i] = xs[i * 64];
    f32x16 acc[3] = {};
#pragma unroll
    for (int ks = 0; ks < 40; ++ks) {
      if ((ks & 3) == 0) { BAR(); asm volatile("" ::: "memory"); }
      u32x4 xv = xf[ks % W];
      if (ks + W < 40) xf[ks % W] = xs[(ks + W) * 64];
#pragma unroll
      for (int c = 0; c < 3; ++c) {
        u32x4 tf = ldsr<u32x4>(smem + (ks * 3 + c) * 1024 + lane * 16);
        acc[c] = mfma32(tf, xv, acc[c]);
      }
    }
    const int h = lane >> 5, r = lane & 31;
    char* dst = (char*)y + ((size_t)(mb * 128 + wid * 32 + r) * F_ + nb * 96 + 16 * h) * 2;
#pragma unroll
    for (int c = 0; c < 3; ++c) {
      *(u32x4*)(dst + c * 64) = pack8(acc[c], 0);
      *(u32x4*)(dst + c * 64 + 16) = pack8(acc[c], 1);
    }
  }
}

extern "C" void kernel_launch(void* const* d_in, const int* in_sizes, int n_in, void* d_out, int out_size, void* d_ws,
                              size_t ws_size, hipStream_t stream) {
  const __hip_bfloat16* x = (const __hip_bfloat16*)d_in[0];
  const int* qw = (const int*)d_in[1];
  const __hip_bfloat16* scales = (const __hip_bfloat16*)d_in[2];
  const __hip_bfloat16* zeros = (const __hip_bfloat16*)d_in[3];
  const __hip_bfloat16* U = (const __hip_bfloat16*)d_in[4];
  const __hip_bfloat16* V = (const __hip_bfloat16*)d_in[5];
  char* TF = (char*)d_ws;
  char* xrF = TF + 2 * 1024 * 1024;
  (void)hipFuncSetAttribute((const void*)k1, hipFuncAttributeMaxDynamicSharedMemorySize, K1_LDS);
  (void)hipFuncSetAttribute((const void*)k2, hipFuncAttributeMaxDynamicSharedMemorySize, K2_LDS);
  k1<<<256, 512, K1_LDS, stream>>>(x, qw, scales, zeros, U, V, TF, xrF);
  k2<<<256, 512, K2_LDS, stream>>>(TF, xrF, (__hip_bfloat16*)d_out);
}
```

```cpp
#include <hip/hip_runtime.h>
#include <hip/hip_bf16.h>

typedef __attribute__((ext_vector_type(8))) __bf16 bf16x8;
typedef __attribute__((ext_vector_type(2))) __bf16 bf16x2;
typedef __attribute__((ext_vector_type(4))) short s16x4;
typedef __attribute__((ext_vector_type(16))) float f32x16;
typedef __attribute__((ext_vector_type(4))) float f32x4;
typedef __attribute__((ext_vector_type(4))) int i32x4;
typedef __attribute__((ext_vector_type(4))) unsigned u32x4;
typedef __attribute__((ext_vector_type(2))) unsigned u32x2;
typedef __attribute__((address_space(3))) unsigned lds_u32;
typedef __attribute__((address_space(3))) s16x4 lds_s16x4;
typedef __attribute__((address_space(3))) char lds_char;

#define STAMP(base, i)
constexpr int S_ = 2048, D_ = 640, F_ = 1536;

#define WAIT_V(n) asm volatile("s_waitcnt vmcnt(" #n ")" ::: "memory")
#define WAIT_L(n) asm volatile("s_waitcnt lgkmcnt(" #n ")" ::: "memory")
#define BAR() __builtin_amdgcn_s_barrier()
__device__ __forceinline__ void glds(const void* sbase, unsigned voff, unsigned ldsaddr) {
  asm volatile("s_mov_b32 m0, %2\n\ts_nop 0\n\tglobal_load_lds_dwordx4 %0, %1" ::"v"(voff), "s"(sbase), "s"(ldsaddr) : "memory");
}

__device__ __forceinline__ unsigned pack2(float a, float b) {
  bf16x2 p; p[0] = (__bf16)a; p[1] = (__bf16)b;
  return __builtin_bit_cast(unsigned, p);
}
__device__ __forceinline__ float bflo(unsigned u) { return __builtin_bit_cast(float, u << 16); }
__device__ __forceinline__ float bfhi(unsigned u) { return __builtin_bit_cast(float, u & 0xffff0000u); }
__device__ __forceinline__ u32x2 trread(lds_char* p) {
  s16x4 v = __builtin_amdgcn_ds_read_tr16_b64_v4i16((lds_s16x4*)p);
  return __builtin_bit_cast(u32x2, v);
}
template <typename T> __device__ __forceinline__ T ldsr(lds_char* p) { return *(__attribute__((address_space(3))) T*)p; }
template <typename T> __device__ __forceinline__ void ldsw(lds_char* p, T v) { *(__attribute__((address_space(3))) T*)p = v; }
__device__ __forceinline__ f32x16 mfma32(u32x4 a, u32x4 b, f32x16 c) {
  return __builtin_amdgcn_mfma_f32_32x32x16_bf16(__builtin_bit_cast(bf16x8, a), __builtin_bit_cast(bf16x8, b), c, 0, 0, 0);
}
__device__ __forceinline__ u32x4 pack8(const f32x16& a, int s) {
  u32x4 r;
  r[0] = pack2(a[8 * s + 0], a[8 * s + 1]); r[1] = pack2(a[8 * s + 2], a[8 * s + 3]);
  r[2] = pack2(a[8 * s + 4], a[8 * s + 5]); r[3] = pack2(a[8 * s + 6], a[8 * s + 7]);
  return r;
}

constexpr int K1_LDS = 163840;
__global__ __launch_bounds__(512) void k1(const __hip_bfloat16* __restrict__ x, const int* __restrict__ qw,
                                          const __hip_bfloat16* __restrict__ scales, const __hip_bfloat16* __restrict__ zeros,
                                          const __hip_bfloat16* __restrict__ U, const __hip_bfloat16* __restrict__ V,
                                          char* __restrict__ TF, char* __restrict__ xrF) {
  extern __shared__ __attribute__((aligned(16))) char smem_[];
  lds_char* smem = (lds_char*)smem_;
  const int wid = __builtin_amdgcn_readfirstlane(threadIdx.x >> 6), lane = threadIdx.x & 63;
  const int xcd = blockIdx.x & 7, idx = blockIdx.x >> 3;
  const int h = lane >> 5, r = lane & 31;
  const int blk = (lane >> 4) & 1, q4 = (lane & 15) >> 2, p4 = lane & 3;

  STAMP(TF, 0);
  if (idx < 20) {
    const int mt = (xcd & 1) * 5 + idx % 5, nb = (xcd >> 1) * 4 + idx / 5;
    const int m = wid & 1, kg = wid >> 1;
    const int grp = mt * 2 + m;
    lds_char* TAB = smem + wid * 3072;
    lds_char* AR = smem + 24576 + wid * 8192;
    lds_char* BR = smem + 24576 + 65536 + kg * 18432;
    const int lc = lane < 48 ? lane : 47;
    const unsigned lb = (unsigned)(size_t)smem;
    glds((const char*)scales + (size_t)(grp * F_ + kg * 384) * 2, lc * 16, lb + wid * 3072);
    glds((const char*)zeros + (size_t)(grp * F_ + kg * 384) * 2, lc * 16, lb + wid * 3072 + 1024);
    unsigned ag[4];
#pragma unroll
    for (int i = 0; i < 4; ++i) {
      int row = i * 8 + (lane >> 3);
      int ch = (lane & 7) ^ ((row >> 1) & 7);
      ag[i] = row * (F_ * 4) + ch * 16;
    }
    unsigned bg[3];
#pragma unroll
    for (int i = 0; i < 3; ++i) {
      int off = (m * 3 + i) * 1024 + lane * 16;
      bg[i] = (off / 192) * (F_ * 2) + off % 192;
    }
    const char* abase = (const char*)qw + (size_t)(grp * 32) * (F_ * 4) + kg * 384 * 4;
    const char* bbase = (const char*)V + (size_t)(kg * 384) * (F_ * 2) + nb * 192;
    auto loadA = [&](int rd, int slot) {
#pragma unroll
      for (int i = 0; i < 4; ++i) glds(abase + rd * 128, ag[i], lb + 24576 + wid * 8192 + slot * 4096 + i * 1024);
    };
    auto loadB = [&](int rd, int slot) {
#pragma unroll
      for (int i = 0; i < 3; ++i) glds(bbase + (size_t)rd * 32 * (F_ * 2), bg[i], lb + 24576 + 65536 + kg * 18432 + slot * 6144 + (m * 3 + i) * 1024);
    };
    loadB(0, 0); loadA(0, 0); loadB(1, 1); loadA(1, 1);
    WAIT_V(14);
    {
      u32x4 s8 = ldsr<u32x4>(TAB + lc * 16), z8 = ldsr<u32x4>(TAB + 1024 + lc * 16);
      f32x4 sa, sb, za, zb;
      sa[0] = bflo(s8[0]); sa[1] = bfhi(s8[0]); sa[2] = bflo(s8[1]); sa[3] = bfhi(s8[1]);
      sb[0] = bflo(s8[2]); sb[1] = bfhi(s8[2]); sb[2] = bflo(s8[3]); sb[3] = bfhi(s8[3]);
      za[0] = -bflo(z8[0]) * sa[0]; za[1] = -bfhi(z8[0]) * sa[1]; za[2] = -bflo(z8[1]) * sa[2]; za[3] = -bfhi(z8[1]) * sa[3];
      zb[0] = -bflo(z8[2]) * sb[0]; zb[1] = -bfhi(z8[2]) * sb[1]; zb[2] = -bflo(z8[3]) * sb[2]; zb[3] = -bfhi(z8[3]) * sb[3];
      ldsw<f32x4>(TAB + lc * 32, sa); ldsw<f32x4>(TAB + lc * 32 + 16, sb);
      ldsw<f32x4>(TAB + 1536 + lc * 32, za); ldsw<f32x4>(TAB + 1536 + lc * 32 + 16, zb);
    }
    int aoff[4];
#pragma unroll
    for (int c = 0; c < 4; ++c) {
      int ch = 4 * (c >> 1) + 2 * h + (c & 1);
      aoff[c] = r * 128 + ((ch ^ ((r >> 1) & 7)) * 16);
    }
    const int boff = (8 * h + q4) * 192 + 32 * (p4 & 1) + 16 * blk + 8 * (p4 >> 1);
    f32x16 acc[3] = {};
    int bs = 0;
#pragma unroll 1
    for (int rd = 0; rd < 12; ++rd) {
      if (rd < 11) WAIT_V(7); else WAIT_V(0);
      BAR();
      int bs2 = bs == 0 ? 2 : bs - 1;
      if (rd < 10) loadB(rd + 2, bs2);
      lds_char* As = AR + (rd & 1) * 4096;
      lds_char* Bs = BR + bs * 6144 + boff;
      lds_char* Ts = TAB + (rd * 32 + 8 * h) * 4;
      i32x4 a[4];
#pragma unroll
      for (int c = 0; c < 4; ++c) a[c] = ldsr<i32x4>(As + aoff[c]);
      f32x4 ts[4], tz[4];
#pragma unroll
      for (int c = 0; c < 4; ++c) {
        ts[c] = ldsr<f32x4>(Ts + (c >> 1) * 64 + (c & 1) * 16);
        tz[c] = ldsr<f32x4>(Ts + 1536 + (c >> 1) * 64 + (c & 1) * 16);
      }
      u32x4 b[2][3];
#pragma unroll
      for (int s = 0; s < 2; ++s)
#pragma unroll
        for (int c = 0; c < 3; ++c) {
          u32x2 lo = trread(Bs + (16 * s) * 192 + 64 * c), hi = trread(Bs + (16 * s + 4) * 192 + 64 * c);
          b[s][c] = u32x4{lo[0], lo[1], hi[0], hi[1]};
        }
      u32x4 af[2];
#pragma unroll
      for (int c = 0; c < 4; ++c) {
        float w0 = __builtin_fmaf((float)a[c][0], ts[c][0], tz[c][0]);
        float w1 = __builtin_fmaf((float)a[c][1], ts[c][1], tz[c][1]);
        float w2 = __builtin_fmaf((float)a[c][2], ts[c][2], tz[c][2]);
        float w3 = __builtin_fmaf((float)a[c][3], ts[c][3], tz[c][3]);
        af[c >> 1][(c & 1) * 2] = pack2(w0, w1);
        af[c >> 1][(c & 1) * 2 + 1] = pack2(w2, w3);
      }
      WAIT_L(0);
      if (rd < 10) loadA(rd + 2, rd & 1);
#pragma unroll
      for (int s = 0; s < 2; ++s)
#pragma unroll
        for (int c = 0; c < 3; ++c) acc[c] = mfma32(af[s], b[s][c], acc[c]);
      bs = bs == 2 ? 0 : bs + 1;
    }
    STAMP(TF, 1);
    BAR();
    lds_char* RED = smem + m * 49152;
#pragma unroll
    for (int c = 0; c < 3; ++c) {
      if (c != kg) {
#pragma unroll
        for (int j = 0; j < 4; ++j)
          ldsw<f32x4>(RED + (kg * 3 + c) * 4096 + j * 1024 + lane * 16, f32x4{acc[c][4 * j], acc[c][4 * j + 1], acc[c][4 * j + 2], acc[c][4 * j + 3]});
      }
    }
    WAIT_L(0);
    BAR();
    if (kg < 3) {
      f32x16 o = kg == 0 ? acc[0] : (kg == 1 ? acc[1] : acc[2]);
#pragma unroll
      for (int i = 1; i < 4; ++i) {
        int src = (kg + i) & 3;
#pragma unroll
        for (int j = 0; j < 4; ++j) {
          f32x4 v = ldsr<f32x4>(RED + (src * 3 + kg) * 4096 + j * 1024 + lane * 16);
          o[4 * j] += v[0]; o[4 * j + 1] += v[1]; o[4 * j + 2] += v[2]; o[4 * j + 3] += v[3];
        }
      }
      char* dst = TF + ((size_t)(nb * 40 + grp * 2) * 3 + kg) * 1024 + lane * 16;
      *(u32x4*)dst = pack8(o, 0);
      *(u32x4*)(dst + 3072) = pack8(o, 1);
    }
    STAMP(TF, 2);
  } else if (idx < 30) {
    const int j = idx - 20;
    const int mb = xcd * 2 + (j & 1), eb = j >> 1;
    const int we = wid & 1, wm = wid >> 1;
    unsigned xg[2], ug[2];
    const unsigned lb = (unsigned)(size_t)smem;
#pragma unroll
    for (int i = 0; i < 2; ++i) {
      int row = wid * 16 + i * 8 + (lane >> 3);
      int ch = (lane & 7) ^ ((row >> 1) & 7);
      xg[i] = row * (D_ * 2) + ch * 16;
      int krow = wid * 8 + i * 4 + (lane >> 4);
      int uc = (lane & 15) ^ ((krow & 3) << 2);
      ug[i] = krow * (D_ * 2) + uc * 16;
    }
    auto stage = [&](int st) {
      unsigned base = lb + (st & 3) * 32768 + wid * 2048;
#pragma unroll
      for (int i = 0; i < 2; ++i) {
        glds((const char*)x + (size_t)(mb * 128) * (D_ * 2) + st * 128, xg[i], base + i * 1024);
        glds((const char*)U + eb * 256 + (size_t)st * 64 * (D_ * 2), ug[i], base + 16384 + i * 1024);
      }
    };
    stage(0); stage(1); stage(2);
    int xoff[4];
#pragma unroll
    for (int s = 0; s < 4; ++s) {
      int row = wm * 32 + r;
      xoff[s] = row * 128 + (((2 * s + h) ^ ((row >> 1) & 7)) * 16);
    }
    int uoff[2];
#pragma unroll
    for (int c = 0; c < 2; ++c) {
      int seg = (2 * we + c) ^ q4;
      uoff[c] = 16384 + (8 * h + q4) * 256 + seg * 64 + blk * 32 + p4 * 8;
    }
    f32x16 acc[2] = {};
#pragma unroll 1
    for (int st = 0; st < 10; ++st) {
      if (st < 8) WAIT_V(8); else if (st == 8) WAIT_V(4); else WAIT_V(0);
      BAR();
      if (st < 7) stage(st + 3);
      lds_char* base = smem + (st & 3) * 32768;
#pragma unroll
      for (int s = 0; s < 4; ++s) {
        u32x4 xf = ldsr<u32x4>(base + xoff[s]);
#pragma unroll
        for (int c = 0; c < 2; ++c) {
          u32x2 lo = trread(base + uoff[c] + (16 * s) * 256), hi = trread(base + uoff[c] + (16 * s + 4) * 256);
          acc[c] = mfma32(u32x4{lo[0], lo[1], hi[0], hi[1]}, xf, acc[c]);
        }
      }
      WAIT_L(0);
    }
#pragma unroll
    for (int c = 0; c < 2; ++c) {
      int et = eb * 4 + we * 2 + c, mtile = mb * 4 + wm;
      char* dst = xrF + ((size_t)mtile * 40 + et * 2) * 1024 + lane * 16;
      *(u32x4*)dst = pack8(acc[c], 0);
      *(u32x4*)(dst + 1024) = pack8(acc[c], 1);
    }
    STAMP(TF, 3);
  }
}

constexpr int K2_LDS = 122880;
__global__ __launch_bounds__(512) void k2(const char* __restrict__ TF, const char* __restrict__ xrF, __hip_bfloat16* __restrict__ y) {
  extern __shared__ __attribute__((aligned(16))) char smem_[];
  lds_char* smem = (lds_char*)smem_;
  const int wid = __builtin_amdgcn_readfirstlane(threadIdx.x >> 6), lane = threadIdx.x & 63;
  const int xcd = blockIdx.x & 7, idx = blockIdx.x >> 3;
  const int mb = (xcd >> 1) * 4 + (idx & 3), nb = (xcd & 1) * 8 + (idx >> 2);
  STAMP(TF, 4);
  if (wid >= 4) {
    const char* src = TF + (size_t)nb * 122880 + (wid - 4) * 3072;
    unsigned dst = (unsigned)(size_t)smem + (wid - 4) * 3072;
#pragma unroll
    for (int ch = 0; ch < 10; ++ch)
#pragma unroll
      for (int i = 0; i < 3; ++i) glds(src + ch * 12288 + i * 1024, lane * 16, dst + ch * 12288 + i * 1024);
    WAIT_V(27); BAR(); WAIT_V(24); BAR(); WAIT_V(21); BAR(); WAIT_V(18); BAR(); WAIT_V(15); BAR();
    WAIT_V(12); BAR(); WAIT_V(9); BAR(); WAIT_V(6); BAR(); WAIT_V(3); BAR(); WAIT_V(0); BAR();
  } else {
    const u32x4* xs = (const u32x4*)(xrF + (size_t)(mb * 4 + wid) * 40960) + lane;
    constexpr int W = 20;
    u32x4 xf[W];
#pragma unroll
    for (int i = 0; i < W; ++i) xf[i] = xs[i * 64];
    f32x16 acc[3] = {};
#pragma unroll
    for (int ks = 0; ks < 40; ++ks) {
      if ((ks & 3) == 0) { BAR(); asm volatile("" ::: "memory"); }
      u32x4 xv = xf[ks % W];
      if (ks + W < 40) xf[ks % W] = xs[(ks + W) * 64];
#pragma unroll
      for (int c = 0; c < 3; ++c) {
        u32x4 tf = ldsr<u32x4>(smem + (ks * 3 + c) * 1024 + lane * 16);
        acc[c] = mfma32(tf, xv, acc[c]);
      }
    }
    STAMP(TF, 5);
    const int h = lane >> 5, r = lane & 31;
    char* dst = (char*)y + ((size_t)(mb * 128 + wid * 32 + r) * F_ + nb * 96 + 16 * h) * 2;
#pragma unroll
    for (int c = 0; c < 3; ++c) {
      *(u32x4*)(dst + c * 64) = pack8(acc[c], 0);
      *(u32x4*)(dst + c * 64 + 16) = pack8(acc[c], 1);
    }
    WAIT_V(0);
    STAMP(TF, 6);
  }
}

extern "C" void kernel_launch(void* const* d_in, const int* in_sizes, int n_in, void* d_out, int out_size, void* d_ws,
                              size_t ws_size, hipStream_t stream) {
  const __hip_bfloat16* x = (const __hip_bfloat16*)d_in[0];
  const int* qw = (const int*)d_in[1];
  const __hip_bfloat16* scales = (const __hip_bfloat16*)d_in[2];
  const __hip_bfloat16* zeros = (const __hip_bfloat16*)d_in[3];
  const __hip_bfloat16* U = (const __hip_bfloat16*)d_in[4];
  const __hip_bfloat16* V = (const __hip_bfloat16*)d_in[5];
  char* TF = (char*)d_ws;
  char* xrF = TF + 2 * 1024 * 1024;
  (void)hipFuncSetAttribute((const void*)k1, hipFuncAttributeMaxDynamicSharedMemorySize, K1_LDS);
  (void)hipFuncSetAttribute((const void*)k2, hipFuncAttributeMaxDynamicSharedMemorySize, K2_LDS);
  k1<<<256, 512, K1_LDS, stream>>>(x, qw, scales, zeros, U, V, TF, xrF);
  k2<<<256, 512, K2_LDS, stream>>>(TF, xrF, (__hip_bfloat16*)d_out);
}
```

```cpp
#include <hip/hip_runtime.h>
#include <hip/hip_bf16.h>

typedef __attribute__((ext_vector_type(8))) __bf16 bf16x8;
typedef __attribute__((ext_vector_type(2))) __bf16 bf16x2;
typedef __attribute__((ext_vector_type(4))) short s16x4;
typedef __attribute__((ext_vector_type(16))) float f32x16;
typedef __attribute__((ext_vector_type(4))) float f32x4;
typedef __attribute__((ext_vector_type(4))) unsigned u32x4;
typedef __attribute__((ext_vector_type(2))) unsigned u32x2;
typedef __attribute__((address_space(3))) s16x4 lds_s16x4;
typedef __attribute__((address_space(3))) char lds_char;
typedef __attribute__((address_space(3))) unsigned lds_u32;
typedef __attribute__((address_space(1))) unsigned long long gu64;
typedef unsigned long long u64;

#define STAMP(i)
#define POLL_SLEEP 4
constexpr int D_ = 640, F_ = 1536;

#define WAIT_V(n) asm volatile("s_waitcnt vmcnt(" #n ")" ::: "memory")
#define WAIT_L(n) asm volatile("s_waitcnt lgkmcnt(" #n ")" ::: "memory")
#define BAR() __builtin_amdgcn_s_barrier()
#define SB() __builtin_amdgcn_sched_barrier(0)
#define RLX_AGENT __ATOMIC_RELAXED, __HIP_MEMORY_SCOPE_AGENT
#define RLX_WG __ATOMIC_RELAXED, __HIP_MEMORY_SCOPE_WORKGROUP

__device__ __forceinline__ void set_m0(unsigned ldsaddr) { asm volatile("s_mov_b32 m0, %0\n\ts_nop 0" ::"s"(ldsaddr) : "memory"); }
template <int OFF> __device__ __forceinline__ void glds(const void* sbase, unsigned voff) {
  asm volatile("global_load_lds_dwordx4 %0, %1 offset:%2" ::"v"(voff), "s"(sbase), "i"(OFF) : "memory");
}
__device__ __forceinline__ void touch(const void* sbase, unsigned voff) {
  asm volatile("global_load_lds_dword %0, %1" ::"v"(voff), "s"(sbase) : "memory");
}
__device__ __forceinline__ unsigned pack2(float a, float b) {
  bf16x2 p; p[0] = (__bf16)a; p[1] = (__bf16)b;
  return __builtin_bit_cast(unsigned, p);
}
__device__ __forceinline__ float bflo(unsigned u) { return __builtin_bit_cast(float, u << 16); }
__device__ __forceinline__ float bfhi(unsigned u) { return __builtin_bit_cast(float, u & 0xffff0000u); }
__device__ __forceinline__ u32x4 trread2(lds_char* p0, lds_char* p1) {
  u32x2 lo = __builtin_bit_cast(u32x2, __builtin_amdgcn_ds_read_tr16_b64_v4i16((lds_s16x4*)p0));
  u32x2 hi = __builtin_bit_cast(u32x2, __builtin_amdgcn_ds_read_tr16_b64_v4i16((lds_s16x4*)p1));
  return u32x4{lo[0], lo[1], hi[0], hi[1]};
}
template <typename T> __device__ __forceinline__ T ldsr(lds_char* p) { return *(__attribute__((address_space(3))) T*)p; }
template <typename T> __device__ __forceinline__ void ldsw(lds_char* p, T v) { *(__attribute__((address_space(3))) T*)p = v; }
__device__ __forceinline__ f32x16 mfma32(u32x4 a, u32x4 b, f32x16 c) {
  return __builtin_amdgcn_mfma_f32_32x32x16_bf16(__builtin_bit_cast(bf16x8, a), __builtin_bit_cast(bf16x8, b), c, 0, 0, 0);
}
__device__ __forceinline__ u32x4 pack8(const f32x16& a, int s) {
  u32x4 r;
  r[0] = pack2(a[8 * s + 0], a[8 * s + 1]); r[1] = pack2(a[8 * s + 2], a[8 * s + 3]);
  r[2] = pack2(a[8 * s + 4], a[8 * s + 5]); r[3] = pack2(a[8 * s + 6], a[8 * s + 7]);
  return r;
}
__device__ __forceinline__ u32x2 deq4(u32x4 q, f32x4 s, f32x4 z) {
  float w0, w1, w2, w3;
  asm("v_cvt_f32_i32 %0, %4\n\tv_cvt_f32_i32 %1, %5\n\tv_cvt_f32_i32 %2, %6\n\tv_cvt_f32_i32 %3, %7\n\t"
      "v_fma_f32 %0, %0, %8, %12\n\tv_fma_f32 %1, %1, %9, %13\n\tv_fma_f32 %2, %2, %10, %14\n\tv_fma_f32 %3, %3, %11, %15\n\t"
      "v_cvt_pk_bf16_f32 %0, %0, %1\n\tv_cvt_pk_bf16_f32 %1, %2, %3"
      : "=&v"(w0), "=&v"(w1), "=&v"(w2), "=&v"(w3)
      : "v"(q[0]), "v"(q[1]), "v"(q[2]), "v"(q[3]), "v"(s[0]), "v"(s[1]), "v"(s[2]), "v"(s[3]), "v"(z[0]), "v"(z[1]), "v"(z[2]), "v"(z[3]));
  return u32x2{__builtin_bit_cast(unsigned, w0), __builtin_bit_cast(unsigned, w1)};
}
__device__ __forceinline__ void lds_spin(lds_u32* w, unsigned want) {
  while ((unsigned)__builtin_amdgcn_readfirstlane(__hip_atomic_load(w, RLX_WG)) < want) __builtin_amdgcn_s_sleep(1);
  asm volatile("" ::: "memory");
}
__device__ __forceinline__ void poll_flags(gu64* f, u64 tok) {
#pragma unroll 1
  for (unsigned spins = 0; spins < (1u << 20); ++spins) {
    if (__all(__hip_atomic_load(f, RLX_AGENT) == tok)) break;
    __builtin_amdgcn_s_sleep(POLL_SLEEP);
  }
  __builtin_amdgcn_fence(__ATOMIC_ACQUIRE, "wavefront");
}

constexpr int XRF_OFF = 2 << 20, SYNC_OFF = 6 << 20;
constexpr int FT = 16, FX = 16 + 12 * 16, FP = FX + 16 * 16;
constexpr int CODE_TOUCH_MAX = 14848;
constexpr int L_SYNC = 156160, L_DUMP = 156416, LDS_BYTES = 156672;

__global__ __launch_bounds__(512, 2) void quip(const __hip_bfloat16* __restrict__ x, const int* __restrict__ qw,
                                               const __hip_bfloat16* __restrict__ scales, const __hip_bfloat16* __restrict__ zeros,
                                               const __hip_bfloat16* __restrict__ U, const __hip_bfloat16* __restrict__ V,
                                               char* ws, __hip_bfloat16* __restrict__ y) {
  extern __shared__ __attribute__((aligned(16))) char smem_[];
  lds_char* smem = (lds_char*)smem_;
  const unsigned lb = (unsigned)(size_t)smem;
  const int wid = __builtin_amdgcn_readfirstlane(threadIdx.x >> 6), lane = threadIdx.x & 63;
  const int xcd = blockIdx.x & 7, idx = blockIdx.x >> 3;
  const int h = lane >> 5, r = lane & 31;
  const int blk = (lane >> 4) & 1, q4 = (lane & 15) >> 2, p4 = lane & 3;
  lds_u32* sw = (lds_u32*)(smem + L_SYNC);
  gu64* sync = (gu64*)(ws + SYNC_OFF);
  const __amdgpu_buffer_rsrc_t wsr = __builtin_amdgcn_make_buffer_rsrc(ws, 0, SYNC_OFF, 0x00020000);
  const bool is_pf = xcd < 6 && idx < 12;
  if (wid == 0) STAMP(0);

  auto put_token = [&](u64 epoch) {
    if (wid == 3 && lane == 0) {
      const u64 e = epoch + 0x9E3779B97F4A7C15ull;
      sw[0] = (unsigned)e; sw[1] = (unsigned)(e >> 32);
      __hip_atomic_store(sw + 2, 1u, RLX_WG);
      if (is_pf) __hip_atomic_store(sync + FP + xcd * 12 + idx, e, RLX_AGENT);
    }
  };
  if (wid == 3) {
    if (lane < 8) sw[lane] = (lane == 3 && is_pf) ? 4u : 0u;
  } else if (wid == 1 && idx < 2) {
    u64 pc;
    asm volatile("s_getpc_b64 %0" : "=s"(pc));
    set_m0(lb + L_DUMP);
    touch((const char*)pc, min(idx * 8192 + lane * 128, CODE_TOUCH_MAX));
  }
  int myflag = 0;

  if (is_pf) {
    if (wid == 0) {
      const int a = xcd & 1, b = xcd >> 1;
      set_m0(lb + L_DUMP);
      const unsigned qo = (lane >> 1) * (F_ * 4) + (lane & 1) * 128, vo = (lane >> 3) * (F_ * 2) + (lane & 7) * 128;
      const char* qb = (const char*)qw + (size_t)(a * 320) * (F_ * 4);
      const char* vb = (const char*)V + b * 1024;
      int p = 0, j = idx;
#pragma unroll 1
      for (int n = 0; n < 36; ++n) {
        if (j < 10) touch(qb + (size_t)j * 32 * (F_ * 4) + p * 256, qo);
        else touch(vb + (size_t)(p * 64 + (j - 10) * 8) * (F_ * 2), vo);
        j += 12;
        if (j >= 18) { j -= 18; ++p; }
        if (n == 2) BAR();
      }
      STAMP(7);
    } else {
      WAIT_L(0);
      BAR();
      if (wid == 3) put_token(__hip_atomic_load(sync, RLX_AGENT));
    }
  } else if (xcd < 6) {
    const int t = idx - 12;
    const int mt = (xcd & 1) * 5 + t % 5, nt = (xcd >> 1) * 4 + t / 5;
    myflag = FT + nt * 16 + mt;
    if (wid >= 4) {
      const int w4 = wid - 4, rr = lane >> 4, c16 = lane & 15;
      unsigned ag[4], bg[4];
#pragma unroll
      for (int i = 0; i < 4; ++i) {
        int row = i * 4 + rr;
        ag[i] = row * (F_ * 4) + ((c16 ^ row) * 16) - i * 1024;
        bg[i] = row * (F_ * 2) + ((c16 ^ (rr << 2)) * 16) - i * 1024;
      }
      const char* ap = (const char*)qw + (size_t)(mt * 64 + w4 * 16) * (F_ * 4);
      const char* bp = (const char*)V + nt * 256 + (size_t)(w4 * 16) * (F_ * 2);
      unsigned so = lb + w4 * 4096;
      auto issue = [&](int p) {
        unsigned base = so + (p & 3) * 32768;
        set_m0(base);
        glds<0>(ap, ag[0]); glds<1024>(ap, ag[1]); glds<2048>(ap, ag[2]); glds<3072>(ap, ag[3]);
        set_m0(base + 16384);
        glds<0>(bp, bg[0]); glds<1024>(bp, bg[1]); glds<2048>(bp, bg[2]); glds<3072>(bp, bg[3]);
        ap += 256; bp += 64 * F_ * 2;
      };
      issue(0);
      issue(1);
      BAR();
#pragma unroll 1
      for (int p = 2; p < 24; ++p) {
        WAIT_V(8);
        BAR();
        issue(p);
      }
      WAIT_V(8);
      BAR();
      WAIT_V(0);
      BAR();
    } else {
      const int m = wid & 1, kl = wid >> 1;
      {
        const size_t o = (size_t)((mt * 2 + m) * F_ + kl * 768) * 2;
        lds_char* T = smem + 131072 + m * 12288 + kl * 3072;
        u32x4 s8[2], z8[2];
#pragma unroll
        for (int j = 0; j < 2; ++j) {
          int e = j ? 64 + (lane & 31) : lane;
          s8[j] = *(const u32x4*)((const char*)scales + o + e * 16);
          z8[j] = *(const u32x4*)((const char*)zeros + o + e * 16);
        }
        const u64 epoch = __hip_atomic_load(sync, RLX_AGENT);
#pragma unroll
        for (int j = 0; j < 2; ++j) {
          int e = j ? 64 + (lane & 31) : lane;
          f32x4 sa, sb, za, zb;
          sa[0] = bflo(s8[j][0]); sa[1] = bfhi(s8[j][0]); sa[2] = bflo(s8[j][1]); sa[3] = bfhi(s8[j][1]);
          sb[0] = bflo(s8[j][2]); sb[1] = bfhi(s8[j][2]); sb[2] = bflo(s8[j][3]); sb[3] = bfhi(s8[j][3]);
          za[0] = -bflo(z8[j][0]) * sa[0]; za[1] = -bfhi(z8[j][0]) * sa[1]; za[2] = -bflo(z8[j][1]) * sa[2]; za[3] = -bfhi(z8[j][1]) * sa[3];
          zb[0] = -bflo(z8[j][2]) * sb[0]; zb[1] = -bfhi(z8[j][2]) * sb[1]; zb[2] = -bflo(z8[j][3]) * sb[2]; zb[3] = -bfhi(z8[j][3]) * sb[3];
          ldsw<f32x4>(T + e * 32, sa); ldsw<f32x4>(T + e * 32 + 16, sb);
          ldsw<f32x4>(T + 6144 + e * 32, za); ldsw<f32x4>(T + 6144 + e * 32 + 16, zb);
        }
        put_token(epoch);
        WAIT_L(0);
        BAR();
      }
      int aoff[4], boff[4];
#pragma unroll
      for (int c = 0; c < 4; ++c) {
        int ch = 8 * kl + 4 * (c >> 1) + 2 * h + (c & 1);
        aoff[c] = (m * 32 + r) * 256 + ((ch ^ (r & 15)) * 16);
        boff[c] = 16384 + (32 * kl + 8 * h + q4) * 256 + ((c ^ (2 * kl) ^ q4) * 64) + 32 * (p4 & 1) + 16 * blk + 8 * (p4 >> 1);
      }
      lds_char* tp = smem + 131072 + m * 12288 + (32 * kl + 8 * h) * 4;
      f32x16 acc[4] = {};
      f32x4 ts[4], tz[4];
      u32x4 av[4];
      u32x4 af[2][2] = {}, bf[2][2][4] = {};
      auto rdtab = [&](int o, int c) {
        ts[c] = ldsr<f32x4>(tp + o + (c >> 1) * 64 + (c & 1) * 16);
        tz[c] = ldsr<f32x4>(tp + o + 6144 + (c >> 1) * 64 + (c & 1) * 16);
      };
      auto step = [&](int slot) {
        const int buf = slot & 1;
        lds_char* sl = smem + slot * 32768;
        BAR();
#pragma unroll
        for (int c = 0; c < 4; ++c) av[c] = ldsr<u32x4>(sl + aoff[c]);
        SB();
#pragma unroll
        for (int u = 0; u < 8; ++u) {
          acc[u & 3] = mfma32(af[buf ^ 1][u >> 2], bf[buf ^ 1][u >> 2][u & 3], acc[u & 3]);
          SB();
          if (u < 4) {
#pragma unroll
            for (int v = 2 * u; v < 2 * u + 2; ++v)
              bf[buf][v >> 2][v & 3] = trread2(sl + boff[v & 3] + (16 * (v >> 2)) * 256, sl + boff[v & 3] + (16 * (v >> 2) + 4) * 256);
          } else {
            const int c = u - 4;
            u32x2 o = deq4(av[c], ts[c], tz[c]);
            af[buf][c >> 1][(c & 1) * 2] = o[0]; af[buf][c >> 1][(c & 1) * 2 + 1] = o[1];
            SB();
            rdtab((slot + 1) * 256, c);
          }
          SB();
        }
        WAIT_L(8);
      };
#pragma unroll
      for (int c = 0; c < 4; ++c) rdtab(0, c);
#pragma unroll 1
      for (int p = 0; p < 24; p += 4) {
        step(0); step(1); step(2); step(3);
        tp += 1024;
      }
#pragma unroll
      for (int u = 0; u < 8; ++u) acc[u & 3] = mfma32(af[1][u >> 2], bf[1][u >> 2][u & 3], acc[u & 3]);
      if (wid == 0) STAMP(1);
      lds_char* RED = smem + m * 16384;
#pragma unroll
      for (int c = 0; c < 2; ++c)
#pragma unroll
        for (int j = 0; j < 4; ++j) {
          const f32x16& g = acc[2 + c];
          ldsw<f32x4>(RED + kl * 8192 + c * 4096 + j * 1024 + lane * 16, f32x4{g[4 * j], g[4 * j + 1], g[4 * j + 2], g[4 * j + 3]});
        }
      WAIT_L(0);
      if (lane == 0) __hip_atomic_fetch_add(sw + 6 + m, 1u, RLX_WG);
      lds_spin(sw + 6 + m, 2);
#pragma unroll
      for (int c = 0; c < 2; ++c) {
        f32x16 o = acc[c];
#pragma unroll
        for (int j = 0; j < 4; ++j) {
          f32x4 v = ldsr<f32x4>(RED + (kl ^ 1) * 8192 + c * 4096 + j * 1024 + lane * 16);
          o[4 * j] += v[0]; o[4 * j + 1] += v[1]; o[4 * j + 2] += v[2]; o[4 * j + 3] += v[3];
        }
        const int n32 = nt * 4 + kl * 2 + c;
        const unsigned oo = (((n32 / 3) * 40 + (mt * 2 + m) * 2) * 3 + n32 % 3) * 1024;
        __builtin_amdgcn_raw_buffer_store_b128(pack8(o, 0), wsr, lane * 16, oo, 16);
        __builtin_amdgcn_raw_buffer_store_b128(pack8(o, 1), wsr, lane * 16 + 3072, oo, 16);
      }
    }
  } else {
    const int mi = idx & 7, eb = idx >> 3;
    const int xmb = (xcd - 6) * 8 + mi;
    myflag = FX + xmb * 16 + eb;
    if (wid >= 4) {
      const int w4 = wid - 4;
      unsigned xg[4], ug[5];
#pragma unroll
      for (int i = 0; i < 4; ++i) {
        int row = i * 8 + (lane >> 3);
        xg[i] = row * (D_ * 2) + (((lane & 7) ^ ((row >> 1) & 7)) * 16) - i * 1024;
      }
#pragma unroll
      for (int i = 0; i < 5; ++i) {
        int off = i * 1024 + lane * 16;
        ug[i] = (off / 320) * (D_ * 2) + off % 320 - (i & 3) * 1024;
      }
      const char* xb = (const char*)x + (size_t)(xmb * 128 + w4 * 32) * (D_ * 2);
      const char* ub = (const char*)U + eb * 320 + (size_t)(w4 * 16) * (D_ * 2);
      int ks = mi + ((eb * 5 + 1) >> 1);
      if (ks >= 10) ks -= 10;
      auto issue = [&](int st) {
        unsigned base = lb + (st & 3) * 36864;
        const char* xp = xb + ks * 128;
        const char* up = ub + (size_t)ks * 64 * (D_ * 2);
        set_m0(base + w4 * 4096);
        glds<0>(xp, xg[0]); glds<1024>(xp, xg[1]); glds<2048>(xp, xg[2]); glds<3072>(xp, xg[3]);
        set_m0(base + 16384 + w4 * 5120);
        glds<0>(up, ug[0]); glds<1024>(up, ug[1]); glds<2048>(up, ug[2]); glds<3072>(up, ug[3]);
        set_m0(base + 16384 + w4 * 5120 + 4096);
        glds<0>(up, ug[4]);
        if (++ks == 10) ks = 0;
      };
      issue(0);
#pragma unroll 1
      for (int st = 1; st < 10; ++st) {
        issue(st);
        WAIT_V(9);
        BAR();
      }
      WAIT_V(0);
      BAR();
    } else {
      int xoff[4];
#pragma unroll
      for (int s = 0; s < 4; ++s) xoff[s] = (wid * 32 + r) * 128 + (((2 * s + h) ^ ((r >> 1) & 7)) * 16);
      const int uoff = 16384 + (8 * h + q4) * 320 + blk * 32 + p4 * 8;
      f32x16 acc[5] = {};
      u32x4 xf[2], uf[2][5];
      auto rdu = [&](lds_char* base, int s, int buf, int c) {
        uf[buf][c] = trread2(base + uoff + c * 64 + (16 * s) * 320, base + uoff + c * 64 + (16 * s + 4) * 320);
      };
      if (wid == 3) put_token(__hip_atomic_load(sync, RLX_AGENT));
      WAIT_L(0);
      BAR();
      xf[0] = ldsr<u32x4>(smem + xoff[0]);
#pragma unroll
      for (int c = 0; c < 5; ++c) rdu(smem, 0, 0, c);
#pragma unroll 1
      for (int st = 0; st < 10; ++st) {
#pragma unroll
        for (int s = 0; s < 4; ++s) {
          lds_char* base = smem + ((s < 3 ? st : st + 1) & 3) * 36864;
          const int nx = (s + 1) & 3, nb_ = (s + 1) & 1;
#pragma unroll
          for (int c = 0; c < 5; ++c) {
            acc[c] = mfma32(uf[s & 1][c], xf[s & 1], acc[c]);
            SB();
            if (c == 0) {
              if (s == 3) { WAIT_L(0); if (st < 9) BAR(); }
              xf[nb_] = ldsr<u32x4>(base + xoff[nx]);
              rdu(base, nx, nb_, 0);
            } else if (c < 4) {
              rdu(base, nx, nb_, c);
              if (c == 3) rdu(base, nx, nb_, 4);
            }
            SB();
          }
        }
      }
#pragma unroll
      for (int c = 0; c < 5; ++c) {
        const unsigned o = XRF_OFF + ((xmb * 4 + wid) * 40 + (eb * 5 + c) * 2) * 1024;
        __builtin_amdgcn_raw_buffer_store_b128(pack8(acc[c], 0), wsr, lane * 16, o, 16);
        __builtin_amdgcn_raw_buffer_store_b128(pack8(acc[c], 1), wsr, lane * 16 + 1024, o, 16);
      }
      if (wid == 0) STAMP(3);
    }
  }

  const int mb = (xcd >> 1) * 4 + (idx & 3), nb = (xcd & 1) * 8 + (idx >> 2);
  if (wid < 4) {
    lds_spin(sw + 2, 1);
    const u64 tok = (u64)sw[0] | ((u64)sw[1] << 32);
    if (!is_pf) {
      asm volatile("s_waitcnt vmcnt(0) lgkmcnt(0)" ::: "memory");
      if (lane == 0) {
        if (__hip_atomic_fetch_add(sw + 3, 1u, RLX_WG) == 3) __hip_atomic_store(sync + myflag, tok, RLX_AGENT);
      }
      if (wid == 0) STAMP(2);
    }
    if (wid == 0) {
      lds_spin(sw + 3, 4);
      lds_spin(sw + 4, 1);
      const int n0 = (nb * 3) >> 2, n1 = (nb * 3 + 2) >> 2;
      const int l20 = lane < 20 ? lane : 0;
      poll_flags(sync + FT + (l20 < 10 ? n0 * 16 + l20 : n1 * 16 + l20 - 10), tok);
      if (lane == 0) __hip_atomic_store(sw + 5, 1u, RLX_WG);
      STAMP(4);
    } else {
      lds_spin(sw + 5, 1);
    }
    u32x4 tv[30];
    const unsigned to = nb * 122880 + wid * 3072;
#pragma unroll
    for (int i = 0; i < 30; ++i) tv[i] = __builtin_amdgcn_raw_buffer_load_b128(wsr, lane * 16 + (i % 3) * 1024, to + (i / 3) * 12288, 16);
    SB();
#pragma unroll
    for (int ch = 0; ch < 10; ++ch) {
#pragma unroll
      for (int i = 0; i < 3; ++i) ldsw<u32x4>(smem + ch * 12288 + wid * 3072 + i * 1024 + lane * 16, tv[ch * 3 + i]);
      WAIT_L(0);
      BAR();
    }
    if (blockIdx.x == 0 && wid == 1) {
#pragma unroll 1
      for (int i = 0; i < 4; ++i) {
        const int f = i * 64 + lane;
        const int o = f < 120 ? FT + (f / 10) * 16 + f % 10 : (f < 184 ? FX + ((f - 120) >> 2) * 16 + (f & 3) : FP + f - 184);
        poll_flags(sync + o, tok);
      }
      if (lane == 0) __hip_atomic_store(sync, tok, RLX_AGENT);
    }
  } else {
    const int w4 = wid - 4;
    if (wid == 4) {
      lds_spin(sw + 2, 1);
      const u64 tok = (u64)sw[0] | ((u64)sw[1] << 32);
      poll_flags(sync + FX + mb * 16 + (lane & 3), tok);
      if (lane == 0) __hip_atomic_store(sw + 4, 1u, RLX_WG);
      STAMP(8);
    } else {
      lds_spin(sw + 4, 1);
    }
    u32x4 xf[40];
    const unsigned xo = XRF_OFF + (mb * 4 + w4) * 40960;
#pragma unroll
    for (int i = 0; i < 40; ++i) xf[i] = __builtin_amdgcn_raw_buffer_load_b128(wsr, lane * 16 + (i & 3) * 1024, xo + (i >> 2) * 4096, 16);
    SB();
    f32x16 acc[3] = {};
    u32x4 tf[3][3];
    lds_char* tb = smem + lane * 16;
    BAR();
    if (wid == 4) STAMP(9);
#pragma unroll
    for (int c = 0; c < 6; ++c) tf[c / 3][c % 3] = ldsr<u32x4>(tb + c * 1024);
#pragma unroll
    for (int ks = 0; ks < 40; ++ks) {
#pragma unroll
      for (int c = 0; c < 3; ++c) {
        acc[c] = mfma32(tf[ks % 3][c], xf[ks], acc[c]);
        SB();
        if (ks < 38) {
          if (c == 0 && (ks & 3) == 2) { BAR(); asm volatile("" ::: "memory"); }
          tf[(ks + 2) % 3][c] = ldsr<u32x4>(tb + ((ks + 2) * 3 + c) * 1024);
        }
        SB();
      }
    }
    if (wid == 4) STAMP(5);
    lds_char* eb = smem + 122880 + w4 * 6656;
#pragma unroll
    for (int c = 0; c < 3; ++c) {
      ldsw<u32x4>(eb + r * 208 + c * 64 + h * 32, pack8(acc[c], 0));
      ldsw<u32x4>(eb + r * 208 + c * 64 + h * 32 + 16, pack8(acc[c], 1));
    }
    char* dst = (char*)y + ((size_t)(mb * 128 + w4 * 32) * F_ + nb * 96) * 2;
#pragma unroll
    for (int i = 0; i < 6; ++i) {
      int q = i * 64 + lane, row = q / 12, chn = q % 12;
      *(u32x4*)(dst + (size_t)row * (F_ * 2) + chn * 16) = ldsr<u32x4>(eb + row * 208 + chn * 16);
    }
  }
}

extern "C" void kernel_launch(void* const* d_in, const int* in_sizes, int n_in, void* d_out, int out_size, void* d_ws,
                              size_t ws_size, hipStream_t stream) {
  (void)hipFuncSetAttribute((const void*)quip, hipFuncAttributeMaxDynamicSharedMemorySize, LDS_BYTES);
  quip<<<256, 512, LDS_BYTES, stream>>>((const __hip_bfloat16*)d_in[0], (const int*)d_in[1], (const __hip_bfloat16*)d_in[2],
                                        (const __hip_bfloat16*)d_in[3], (const __hip_bfloat16*)d_in[4], (const __hip_bfloat16*)d_in[5],
                                        (char*)d_ws, (__hip_bfloat16*)d_out);
}
```

```cpp
#include <hip/hip_runtime.h>
#include <hip/hip_bf16.h>
#include <type_traits>

typedef __attribute__((ext_vector_type(8))) __bf16 bf16x8;
typedef __attribute__((ext_vector_type(2))) __bf16 bf16x2;
typedef __attribute__((ext_vector_type(4))) short s16x4;
typedef __attribute__((ext_vector_type(16))) float f32x16;
typedef __attribute__((ext_vector_type(4))) float f32x4;
typedef __attribute__((ext_vector_type(4))) int i32x4;
typedef __attribute__((ext_vector_type(4))) unsigned u32x4;
typedef __attribute__((ext_vector_type(2))) unsigned u32x2;
typedef __attribute__((address_space(3))) unsigned lds_u32;
typedef __attribute__((address_space(3))) s16x4 lds_s16x4;
typedef __attribute__((address_space(3))) char lds_char;

#define STAMP(base, i)
constexpr int S_ = 2048, D_ = 640, F_ = 1536;

#define WAIT_V(n) asm volatile("s_waitcnt vmcnt(" #n ")" ::: "memory")
#define WAIT_L(n) asm volatile("s_waitcnt lgkmcnt(" #n ")" ::: "memory")
#define BAR() __builtin_amdgcn_s_barrier()
__device__ __forceinline__ void glds(const void* sbase, unsigned voff, unsigned ldsaddr) {
  asm volatile("s_mov_b32 m0, %2\n\ts_nop 0\n\tglobal_load_lds_dwordx4 %0, %1" ::"v"(voff), "s"(sbase), "s"(ldsaddr) : "memory");
}

__device__ __forceinline__ void glds4(const void* sbase, unsigned v0, unsigned v1, unsigned v2, unsigned v3, unsigned ldsaddr) {
  asm volatile("s_mov_b32 m0, %5\n\ts_nop 0\n\tglobal_load_lds_dwordx4 %0, %4\n\tglobal_load_lds_dwordx4 %1, %4 offset:1024\n\t"
               "global_load_lds_dwordx4 %2, %4 offset:2048\n\tglobal_load_lds_dwordx4 %3, %4 offset:3072"
               ::"v"(v0), "v"(v1), "v"(v2), "v"(v3), "s"(sbase), "s"(ldsaddr) : "memory");
}
__device__ __forceinline__ void glds_if(int cnt, int lim, const void* sbase, unsigned voff, unsigned ldsaddr) {
  asm volatile("s_cmp_ge_i32 %3, %4\n\ts_cbranch_scc1 1f\n\ts_mov_b32 m0, %2\n\ts_nop 0\n\tglobal_load_lds_dwordx4 %0, %1\n1:"
               ::"v"(voff), "s"(sbase), "s"(ldsaddr), "s"(cnt), "s"(lim) : "memory", "scc");
}
#define SB() __builtin_amdgcn_sched_barrier(0)
__device__ __forceinline__ unsigned pack2(float a, float b) {
  bf16x2 p; p[0] = (__bf16)a; p[1] = (__bf16)b;
  return __builtin_bit_cast(unsigned, p);
}
__device__ __forceinline__ float bflo(unsigned u) { return __builtin_bit_cast(float, u << 16); }
__device__ __forceinline__ float bfhi(unsigned u) { return __builtin_bit_cast(float, u & 0xffff0000u); }
__device__ __forceinline__ u32x2 trread(lds_char* p) {
  s16x4 v = __builtin_amdgcn_ds_read_tr16_b64_v4i16((lds_s16x4*)p);
  return __builtin_bit_cast(u32x2, v);
}
template <typename T> __device__ __forceinline__ T ldsr(lds_char* p) { return *(__attribute__((address_space(3))) T*)p; }
template <typename T> __device__ __forceinline__ void ldsw(lds_char* p, T v) { *(__attribute__((address_space(3))) T*)p = v; }
__device__ __forceinline__ f32x16 mfma32(u32x4 a, u32x4 b, f32x16 c) {
  return __builtin_amdgcn_mfma_f32_32x32x16_bf16(__builtin_bit_cast(bf16x8, a), __builtin_bit_cast(bf16x8, b), c, 0, 0, 0);
}
__device__ __forceinline__ u32x4 pack8(const f32x16& a, int s) {
  u32x4 r;
  r[0] = pack2(a[8 * s + 0], a[8 * s + 1]); r[1] = pack2(a[8 * s + 2], a[8 * s + 3]);
  r[2] = pack2(a[8 * s + 4], a[8 * s + 5]); r[3] = pack2(a[8 * s + 6], a[8 * s + 7]);
  return r;
}

constexpr int K1_LDS = 159744;
__device__ __forceinline__ void touch(const void* sbase, unsigned voff) {
  asm volatile("global_load_lds_dword %0, %1" ::"v"(voff), "s"(sbase) : "memory");
}
__global__ __launch_bounds__(512) void k1(const __hip_bfloat16* __restrict__ x, const int* __restrict__ qw,
                                          const __hip_bfloat16* __restrict__ scales, const __hip_bfloat16* __restrict__ zeros,
                                          const __hip_bfloat16* __restrict__ U, const __hip_bfloat16* __restrict__ V,
                                          char* __restrict__ TF, char* __restrict__ xrF) {
  extern __shared__ __attribute__((aligned(16))) char smem_[];
  lds_char* smem = (lds_char*)smem_;
  const unsigned lb = (unsigned)(size_t)smem;
  const int wid = __builtin_amdgcn_readfirstlane(threadIdx.x >> 6), lane = threadIdx.x & 63;
  const int xcd = blockIdx.x & 7, idx = blockIdx.x >> 3;
  const int h = lane >> 5, r = lane & 31;
  const int blk = (lane >> 4) & 1, q4 = (lane & 15) >> 2, p4 = lane & 3;
  STAMP(TF, 0);
  if (idx < 2 && wid == 1) {
    unsigned long long pc;
    asm volatile("s_getpc_b64 %0" : "=s"(pc));
    asm volatile("s_mov_b32 m0, %0\n\ts_nop 0" ::"s"(lb + 158720) : "memory");
    touch((const char*)pc, min(idx * 8192 + lane * 128, 9856));
  }
  if (xcd < 6) {
    const int a = xcd & 1, b = xcd >> 1;
    if (idx < 12) {
      if (wid == 0) {
        asm volatile("s_mov_b32 m0, %0\n\ts_nop 0" ::"s"(lb) : "memory");
        const unsigned qo = (lane >> 1) * (F_ * 4) + (lane & 1) * 128, vo = (lane >> 3) * (F_ * 2) + (lane & 7) * 128;
        const char* qb = (const char*)qw + (size_t)(a * 320) * (F_ * 4);
        const char* vb = (const char*)V + b * 1024;
        int p = 0, j = idx;
#pragma unroll 1
        for (int n = 0; n < 36; ++n) {
          if (j < 10) touch(qb + (size_t)j * 32 * (F_ * 4) + p * 256, qo);
          else touch(vb + (size_t)(p * 64 + (j - 10) * 8) * (F_ * 2), vo);
          j += 12;
          if (j >= 18) { j -= 18; ++p; }
        }
        WAIT_V(0);
        STAMP(TF, 7);
      }
    } else {
      const int t = idx - 12;
      const int mt = a * 5 + t % 5, nt = b * 4 + t / 5;
      if (wid >= 4) {
        const int w4 = wid - 4;
        unsigned ag[4], bg[4];
#pragma unroll
        for (int i = 0; i < 4; ++i) {
          int row = i * 4 + (lane >> 4);
          ag[i] = row * (F_ * 4) + (((lane & 15) ^ row) * 16) - i * 1024;
          bg[i] = row * (F_ * 2) + (((lane & 15) ^ ((row & 3) << 2)) * 16) - i * 1024;
        }
        const char* ap = (const char*)qw + (size_t)(mt * 64 + w4 * 16) * (F_ * 4);
        const char* bp = (const char*)V + nt * 256 + (size_t)(w4 * 16) * (F_ * 2);
        unsigned so = 0;
        auto issue = [&]() {
          unsigned base = lb + w4 * 4096 + so;
          glds4(ap, ag[0], ag[1], ag[2], ag[3], base);
          glds4(bp, bg[0], bg[1], bg[2], bg[3], base + 16384);
          ap += 256; bp += 64 * F_ * 2; so = (so + 32768) & 131071;
        };
        issue();
#pragma unroll 1
        for (int p = 1; p < 24; ++p) {
          issue();
          WAIT_V(8);
          BAR();
        }
        WAIT_V(0);
        BAR();
        return;
      }
      const int m = wid & 1, kl = wid >> 1;
      {
        const size_t o = (size_t)((mt * 2 + m) * F_ + kl * 768) * 2;
        lds_char* T = smem + 131072 + m * 12288 + kl * 3072;
        u32x4 s8[2], z8[2];
#pragma unroll
        for (int j = 0; j < 2; ++j) {
          int e = j ? 64 + (lane & 31) : lane;
          s8[j] = *(const u32x4*)((const char*)scales + o + e * 16);
          z8[j] = *(const u32x4*)((const char*)zeros + o + e * 16);
        }
#pragma unroll
        for (int j = 0; j < 2; ++j) {
          int e = j ? 64 + (lane & 31) : lane;
          f32x4 sa, sb, za, zb;
          sa[0] = bflo(s8[j][0]); sa[1] = bfhi(s8[j][0]); sa[2] = bflo(s8[j][1]); sa[3] = bfhi(s8[j][1]);
          sb[0] = bflo(s8[j][2]); sb[1] = bfhi(s8[j][2]); sb[2] = bflo(s8[j][3]); sb[3] = bfhi(s8[j][3]);
          za[0] = -bflo(z8[j][0]) * sa[0]; za[1] = -bfhi(z8[j][0]) * sa[1]; za[2] = -bflo(z8[j][1]) * sa[2]; za[3] = -bfhi(z8[j][1]) * sa[3];
          zb[0] = -bflo(z8[j][2]) * sb[0]; zb[1] = -bfhi(z8[j][2]) * sb[1]; zb[2] = -bflo(z8[j][3]) * sb[2]; zb[3] = -bfhi(z8[j][3]) * sb[3];
          ldsw<f32x4>(T + e * 32, sa); ldsw<f32x4>(T + e * 32 + 16, sb);
          ldsw<f32x4>(T + 6144 + e * 32, za); ldsw<f32x4>(T + 6144 + e * 32 + 16, zb);
        }
        WAIT_L(0);
      }
      int aoff[4];
#pragma unroll
      for (int c = 0; c < 4; ++c) {
        int ch = 8 * kl + 4 * (c >> 1) + 2 * h + (c & 1);
        aoff[c] = (m * 32 + r) * 256 + ((ch ^ (r & 15)) * 16);
      }
      int boff[4];
#pragma unroll
      for (int c = 0; c < 4; ++c)
        boff[c] = 16384 + (32 * kl + 8 * h + q4) * 256 + ((c ^ (2 * kl) ^ q4) * 64) + 32 * (p4 & 1) + 16 * blk + 8 * (p4 >> 1);
      const int toff = 131072 + m * 12288 + (32 * kl + 8 * h) * 4;
      f32x16 acc[4] = {};
      f32x4 ts[4], tz[4];
      i32x4 av[4];
      u32x4 af[2][2], bf[2][2][4];
      auto rdtab = [&](int p, int c) {
        lds_char* Ts = smem + toff + p * 256;
        ts[c] = ldsr<f32x4>(Ts + (c >> 1) * 64 + (c & 1) * 16);
        tz[c] = ldsr<f32x4>(Ts + 6144 + (c >> 1) * 64 + (c & 1) * 16);
      };
      auto rdb = [&](lds_char* sl, int buf, int u) {
        u32x2 lo = trread(sl + boff[u & 3] + (16 * (u >> 2)) * 256), hi = trread(sl + boff[u & 3] + (16 * (u >> 2) + 4) * 256);
        bf[buf][u >> 2][u & 3] = u32x4{lo[0], lo[1], hi[0], hi[1]};
      };
      auto deq = [&](int buf, int c) {
        float w0 = __builtin_fmaf((float)av[c][0], ts[c][0], tz[c][0]);
        float w1 = __builtin_fmaf((float)av[c][1], ts[c][1], tz[c][1]);
        float w2 = __builtin_fmaf((float)av[c][2], ts[c][2], tz[c][2]);
        float w3 = __builtin_fmaf((float)av[c][3], ts[c][3], tz[c][3]);
        af[buf][c >> 1][(c & 1) * 2] = pack2(w0, w1);
        af[buf][c >> 1][(c & 1) * 2 + 1] = pack2(w2, w3);
      };
      auto step = [&](int p, int buf, bool mm, bool ld) {
        lds_char* sl = smem + (p & 3) * 32768;
        if (ld) {
          BAR();
#pragma unroll
          for (int c = 0; c < 4; ++c) av[c] = ldsr<i32x4>(sl + aoff[c]);
        }
        SB();
#pragma unroll
        for (int u = 0; u < 8; ++u) {
          if (mm) acc[u & 3] = mfma32(af[buf ^ 1][u >> 2], bf[buf ^ 1][u >> 2][u & 3], acc[u & 3]);
          SB();
          if (ld) {
            if (u < 4) { rdb(sl, buf, 2 * u); rdb(sl, buf, 2 * u + 1); }
            else { deq(buf, u - 4); SB(); rdtab(p + 1, u - 4); }
          }
          SB();
        }
        if (ld) WAIT_L(8);
      };
      BAR();
#pragma unroll
      for (int c = 0; c < 4; ++c) { rdtab(0, c); av[c] = ldsr<i32x4>(smem + aoff[c]); }
#pragma unroll
      for (int u = 0; u < 8; ++u) rdb(smem, 0, u);
#pragma unroll
      for (int c = 0; c < 4; ++c) { deq(0, c); SB(); rdtab(1, c); }
      SB();
      WAIT_L(8);
#pragma unroll 1
      for (int p = 1; p < 23; p += 2) {
        step(p, 1, true, true);
        step(p + 1, 0, true, true);
      }
      step(23, 1, true, true);
      step(24, 0, true, false);
      STAMP(TF, 1);
      lds_char* RED = smem + m * 16384;
#pragma unroll
      for (int c = 0; c < 2; ++c)
#pragma unroll
        for (int j = 0; j < 4; ++j) {
          const f32x16& g = acc[2 + c];
          ldsw<f32x4>(RED + kl * 8192 + c * 4096 + j * 1024 + lane * 16, f32x4{g[4 * j], g[4 * j + 1], g[4 * j + 2], g[4 * j + 3]});
        }
      WAIT_L(0);
      BAR();
#pragma unroll
      for (int c = 0; c < 2; ++c) {
        f32x16 o = acc[c];
#pragma unroll
        for (int j = 0; j < 4; ++j) {
          f32x4 v = ldsr<f32x4>(RED + (kl ^ 1) * 8192 + c * 4096 + j * 1024 + lane * 16);
          o[4 * j] += v[0]; o[4 * j + 1] += v[1]; o[4 * j + 2] += v[2]; o[4 * j + 3] += v[3];
        }
        const int n32 = nt * 4 + kl * 2 + c;
        char* dst = TF + ((size_t)((n32 / 3) * 40 + (mt * 2 + m) * 2) * 3 + n32 % 3) * 1024 + lane * 16;
        *(u32x4*)dst = pack8(o, 0);
        *(u32x4*)(dst + 3072) = pack8(o, 1);
      }
      STAMP(TF, 2);
    }
  } else {
    const int mi = idx & 7, eb = idx >> 3;
    const int mb = (xcd - 6) * 8 + mi;
    if (wid >= 4) {
      const int w4 = wid - 4;
      unsigned xg[4], ug[5];
#pragma unroll
      for (int i = 0; i < 4; ++i) {
        int row = i * 8 + (lane >> 3);
        xg[i] = row * (D_ * 2) + (((lane & 7) ^ ((row >> 1) & 7)) * 16) - i * 1024;
      }
#pragma unroll
      for (int i = 0; i < 5; ++i) {
        int off = i * 1024 + lane * 16;
        ug[i] = (off / 320) * (D_ * 2) + off % 320 - (i & 3) * 1024;
      }
      const char* xb = (const char*)x + (size_t)(mb * 128 + w4 * 32) * (D_ * 2);
      const char* ub = (const char*)U + eb * 320 + (size_t)(w4 * 16) * (D_ * 2);
      int ks = mi + ((eb * 5 + 1) >> 1);
      if (ks >= 10) ks -= 10;
      auto issue = [&](int st) {
        unsigned base = lb + (st & 3) * 36864;
        const char* up = ub + (size_t)ks * 64 * (D_ * 2);
        glds4(xb + ks * 128, xg[0], xg[1], xg[2], xg[3], base + w4 * 4096);
        glds4(up, ug[0], ug[1], ug[2], ug[3], base + 16384 + w4 * 5120);
        glds(up, ug[4], base + 16384 + w4 * 5120 + 4096);
        if (++ks == 10) ks = 0;
      };
      issue(0);
#pragma unroll 1
      for (int st = 1; st < 10; ++st) {
        issue(st);
        WAIT_V(9);
        BAR();
      }
      WAIT_V(0);
      BAR();
      BAR();
    } else {
      int xoff[4];
#pragma unroll
      for (int sidx = 0; sidx < 4; ++sidx) {
        int row = wid * 32 + r;
        xoff[sidx] = row * 128 + (((2 * sidx + h) ^ ((row >> 1) & 7)) * 16);
      }
      const int uoff = 16384 + (8 * h + q4) * 320 + blk * 32 + p4 * 8;
      f32x16 acc[5] = {};
      u32x4 xf[2], uf[2][5];
      auto rdu = [&](lds_char* base, int sidx, int buf, int c) {
        u32x2 lo = trread(base + uoff + c * 64 + (16 * sidx) * 320), hi = trread(base + uoff + c * 64 + (16 * sidx + 4) * 320);
        uf[buf][c] = u32x4{lo[0], lo[1], hi[0], hi[1]};
      };
      BAR();
      xf[0] = ldsr<u32x4>(smem + xoff[0]);
#pragma unroll
      for (int c = 0; c < 5; ++c) rdu(smem, 0, 0, c);
#pragma unroll 1
      for (int st = 0; st < 10; ++st) {
#pragma unroll
        for (int sidx = 0; sidx < 4; ++sidx) {
          lds_char* base = smem + ((sidx < 3 ? st : st + 1) & 3) * 36864;
          const int nx = (sidx + 1) & 3, nb_ = (sidx + 1) & 1;
#pragma unroll
          for (int c = 0; c < 5; ++c) {
            acc[c] = mfma32(uf[sidx & 1][c], xf[sidx & 1], acc[c]);
            SB();
            if (c == 0) {
              if (sidx == 3) { WAIT_L(0); BAR(); }
              xf[nb_] = ldsr<u32x4>(base + xoff[nx]);
              rdu(base, nx, nb_, 0);
            } else if (c < 4) {
              rdu(base, nx, nb_, c);
              if (c == 3) rdu(base, nx, nb_, 4);
            }
            SB();
          }
        }
      }
#pragma unroll
      for (int c = 0; c < 5; ++c) {
        char* dst = xrF + ((size_t)(mb * 4 + wid) * 40 + (eb * 5 + c) * 2) * 1024 + lane * 16;
        *(u32x4*)dst = pack8(acc[c], 0);
        *(u32x4*)(dst + 1024) = pack8(acc[c], 1);
      }
    }
    STAMP(TF, 3);
  }
}

constexpr int K2_LDS = 5 * 28672;
__global__ __launch_bounds__(512) void k2(const char* __restrict__ TF, const char* __restrict__ xrF, __hip_bfloat16* __restrict__ y) {
  extern __shared__ __attribute__((aligned(16))) char smem_[];
  lds_char* smem = (lds_char*)smem_;
  const int wid = __builtin_amdgcn_readfirstlane(threadIdx.x >> 6), lane = threadIdx.x & 63;
  const int xcd = blockIdx.x & 7, idx = blockIdx.x >> 3;
  const int mb = (xcd >> 1) * 4 + (idx & 3), nb = (xcd & 1) * 8 + (idx >> 2);
  STAMP(TF, 4);
  if (wid >= 4) {
    const int w4 = wid - 4;
    const char* ts = TF + (size_t)nb * 122880 + w4 * 3072;
    const char* xs = xrF + (size_t)(mb * 4 + w4) * 40960;
    const unsigned lb = (unsigned)(size_t)smem;
    const unsigned vo = lane * 16;
    int slot = 0;
    auto issue = [&]() {
      unsigned base = lb + slot * 28672;
      asm volatile("s_mov_b32 m0, %2\n\ts_nop 0\n\tglobal_load_lds_dwordx4 %0, %1\n\tglobal_load_lds_dwordx4 %0, %1 offset:1024\n\t"
                   "global_load_lds_dwordx4 %0, %1 offset:2048" ::"v"(vo), "s"(ts), "s"(base + w4 * 3072) : "memory");
      glds4(xs, vo, vo, vo, vo, base + 12288 + w4 * 4096);
      ts += 12288; xs += 4096;
      if (++slot == 5) slot = 0;
    };
    issue();
#pragma unroll 1
    for (int ch = 1; ch < 10; ++ch) {
      issue();
      WAIT_V(7);
      BAR();
    }
    WAIT_V(0);
    BAR();
    BAR();
  } else {
    f32x16 acc[3] = {};
    u32x4 xf[2], tf[2][3];
    BAR();
    xf[0] = ldsr<u32x4>(smem + lane * 16 + 12288 + wid * 4096);
#pragma unroll
    for (int c = 0; c < 3; ++c) tf[0][c] = ldsr<u32x4>(smem + lane * 16 + c * 1024);
    int slot = 0;
#pragma unroll 1
    for (int ch = 0; ch < 10; ++ch) {
#pragma unroll
      for (int ks = 0; ks < 4; ++ks) {
        if (ks == 3) { if (++slot == 5) slot = 0; }
        lds_char* base = smem + slot * 28672 + lane * 16;
        const int nx = (ks + 1) & 3, nb_ = (ks + 1) & 1;
#pragma unroll
        for (int c = 0; c < 3; ++c) {
          acc[c] = mfma32(tf[ks & 1][c], xf[ks & 1], acc[c]);
          SB();
          if (c == 0) {
            if (ks == 3) { WAIT_L(0); BAR(); }
            xf[nb_] = ldsr<u32x4>(base + 12288 + wid * 4096 + nx * 1024);
            tf[nb_][0] = ldsr<u32x4>(base + (nx * 3) * 1024);
          } else if (c == 1) {
            tf[nb_][1] = ldsr<u32x4>(base + (nx * 3 + 1) * 1024);
            tf[nb_][2] = ldsr<u32x4>(base + (nx * 3 + 2) * 1024);
          }
          SB();
        }
      }
    }
    STAMP(TF, 5);
    const int h = lane >> 5, r = lane & 31;
    lds_char* eb = smem + 28672 + wid * 6656;
#pragma unroll
    for (int c = 0; c < 3; ++c) {
      ldsw<u32x4>(eb + r * 208 + c * 64 + h * 32, pack8(acc[c], 0));
      ldsw<u32x4>(eb + r * 208 + c * 64 + h * 32 + 16, pack8(acc[c], 1));
    }
    char* dst = (char*)y + ((size_t)(mb * 128 + wid * 32) * F_ + nb * 96) * 2;
#pragma unroll
    for (int i = 0; i < 6; ++i) {
      int q = i * 64 + lane, row = q / 12, chn = q % 12;
      *(u32x4*)(dst + (size_t)row * (F_ * 2) + chn * 16) = ldsr<u32x4>(eb + row * 208 + chn * 16);
    }
    WAIT_V(0);
    STAMP(TF, 6);
  }
}

extern "C" void kernel_launch(void* const* d_in, const int* in_sizes, int n_in, void* d_out, int out_size, void* d_ws,
                              size_t ws_size, hipStream_t stream) {
  const __hip_bfloat16* x = (const __hip_bfloat16*)d_in[0];
  const int* qw = (const int*)d_in[1];
  const __hip_bfloat16* scales = (const __hip_bfloat16*)d_in[2];
  const __hip_bfloat16* zeros = (const __hip_bfloat16*)d_in[3];
  const __hip_bfloat16* U = (const __hip_bfloat16*)d_in[4];
  const __hip_bfloat16* V = (const __hip_bfloat16*)d_in[5];
  char* TF = (char*)d_ws;
  char* xrF = TF + 2 * 1024 * 1024;
  (void)hipFuncSetAttribute((const void*)k1, hipFuncAttributeMaxDynamicSharedMemorySize, K1_LDS);
  (void)hipFuncSetAttribute((const void*)k2, hipFuncAttributeMaxDynamicSharedMemorySize, K2_LDS);
  k1<<<256, 512, K1_LDS, stream>>>(x, qw, scales, zeros, U, V, TF, xrF);
  k2<<<256, 512, K2_LDS, stream>>>(TF, xrF, (__hip_bfloat16*)d_out);
}
```

```cpp
#include <hip/hip_runtime.h>
#include <hip/hip_bf16.h>

typedef __attribute__((ext_vector_type(8))) __bf16 bf16x8;
typedef __attribute__((ext_vector_type(2))) __bf16 bf16x2;
typedef __attribute__((ext_vector_type(4))) short s16x4;
typedef __attribute__((ext_vector_type(16))) float f32x16;
typedef __attribute__((ext_vector_type(4))) float f32x4;
typedef __attribute__((ext_vector_type(4))) unsigned u32x4;
typedef __attribute__((ext_vector_type(2))) unsigned u32x2;
typedef __attribute__((address_space(3))) s16x4 lds_s16x4;
typedef __attribute__((address_space(3))) char lds_char;
typedef __attribute__((address_space(3))) unsigned lds_u32;
typedef __attribute__((address_space(1))) unsigned long long gu64;
typedef unsigned long long u64;

#define STAMP(i)
#define Y_AUX 2
#define XCD_PERM 0x54321706
#define POLL_SLEEP 4
constexpr int D_ = 640, F_ = 1536;

#define WAIT_V(n) asm volatile("s_waitcnt vmcnt(" #n ")" ::: "memory")
#define WAIT_L(n) asm volatile("s_waitcnt lgkmcnt(" #n ")" ::: "memory")
#define BAR() __builtin_amdgcn_s_barrier()
#define SB() __builtin_amdgcn_sched_barrier(0)
#define RLX_AGENT __ATOMIC_RELAXED, __HIP_MEMORY_SCOPE_AGENT
#define RLX_WG __ATOMIC_RELAXED, __HIP_MEMORY_SCOPE_WORKGROUP

__device__ __forceinline__ void set_m0(unsigned ldsaddr) { asm volatile("s_mov_b32 m0, %0\n\ts_nop 0" ::"s"(ldsaddr) : "memory"); }
template <int OFF> __device__ __forceinline__ void glds(const void* sbase, unsigned voff) {
  asm volatile("global_load_lds_dwordx4 %0, %1 offset:%2" ::"v"(voff), "s"(sbase), "i"(OFF) : "memory");
}
__device__ __forceinline__ void touch(const void* sbase, unsigned voff) {
  asm volatile("global_load_lds_dword %0, %1" ::"v"(voff), "s"(sbase) : "memory");
}
__device__ __forceinline__ unsigned pack2(float a, float b) {
  bf16x2 p; p[0] = (__bf16)a; p[1] = (__bf16)b;
  return __builtin_bit_cast(unsigned, p);
}
__device__ __forceinline__ float bflo(unsigned u) { return __builtin_bit_cast(float, u << 16); }
__device__ __forceinline__ float bfhi(unsigned u) { return __builtin_bit_cast(float, u & 0xffff0000u); }
__device__ __forceinline__ u32x4 trread2(lds_char* p0, lds_char* p1) {
  u32x2 lo = __builtin_bit_cast(u32x2, __builtin_amdgcn_ds_read_tr16_b64_v4i16((lds_s16x4*)p0));
  u32x2 hi = __builtin_bit_cast(u32x2, __builtin_amdgcn_ds_read_tr16_b64_v4i16((lds_s16x4*)p1));
  return u32x4{lo[0], lo[1], hi[0], hi[1]};
}
template <typename T> __device__ __forceinline__ T ldsr(lds_char* p) { return *(__attribute__((address_space(3))) T*)p; }
template <typename T> __device__ __forceinline__ void ldsw(lds_char* p, T v) { *(__attribute__((address_space(3))) T*)p = v; }
__device__ __forceinline__ f32x16 mfma32(u32x4 a, u32x4 b, f32x16 c) {
  return __builtin_amdgcn_mfma_f32_32x32x16_bf16(__builtin_bit_cast(bf16x8, a), __builtin_bit_cast(bf16x8, b), c, 0, 0, 0);
}
__device__ __forceinline__ u32x4 pack8(const f32x16& a, int s) {
  u32x4 r;
  r[0] = pack2(a[8 * s + 0], a[8 * s + 1]); r[1] = pack2(a[8 * s + 2], a[8 * s + 3]);
  r[2] = pack2(a[8 * s + 4], a[8 * s + 5]); r[3] = pack2(a[8 * s + 6], a[8 * s + 7]);
  return r;
}
__device__ __forceinline__ u32x2 deq4(u32x4 q, f32x4 s, f32x4 z) {
  float w0, w1, w2, w3;
  asm("v_cvt_f32_i32 %0, %4\n\tv_cvt_f32_i32 %1, %5\n\tv_cvt_f32_i32 %2, %6\n\tv_cvt_f32_i32 %3, %7\n\t"
      "v_fma_f32 %0, %0, %8, %12\n\tv_fma_f32 %1, %1, %9, %13\n\tv_fma_f32 %2, %2, %10, %14\n\tv_fma_f32 %3, %3, %11, %15\n\t"
      "v_cvt_pk_bf16_f32 %0, %0, %1\n\tv_cvt_pk_bf16_f32 %1, %2, %3"
      : "=&v"(w0), "=&v"(w1), "=&v"(w2), "=&v"(w3)
      : "v"(q[0]), "v"(q[1]), "v"(q[2]), "v"(q[3]), "v"(s[0]), "v"(s[1]), "v"(s[2]), "v"(s[3]), "v"(z[0]), "v"(z[1]), "v"(z[2]), "v"(z[3]));
  return u32x2{__builtin_bit_cast(unsigned, w0), __builtin_bit_cast(unsigned, w1)};
}
__device__ __forceinline__ void lds_spin(lds_u32* w, unsigned want) {
  while ((unsigned)__builtin_amdgcn_readfirstlane(__hip_atomic_load(w, RLX_WG)) < want) __builtin_amdgcn_s_sleep(1);
  asm volatile("" ::: "memory");
}
__device__ __forceinline__ void poll_flags(gu64* f, u64 tok) {
#pragma unroll 1
  for (unsigned spins = 0; spins < (1u << 20); ++spins) {
    if (__all(__hip_atomic_load(f, RLX_AGENT) == tok)) break;
    __builtin_amdgcn_s_sleep(POLL_SLEEP);
  }
  __builtin_amdgcn_fence(__ATOMIC_ACQUIRE, "wavefront");
}

constexpr int XRF_OFF = 2 << 20, SYNC_OFF = 6 << 20;
#define FS 16
constexpr int FT = 1, FX = 121, FP = 161;
constexpr int CODE_TOUCH_MAX = 15744;
constexpr int L_SYNC = 156160, L_DUMP = 156416, LDS_BYTES = 156672;

__global__ __launch_bounds__(512, 2) void quip(const __hip_bfloat16* __restrict__ x, const int* __restrict__ qw,
                                               const __hip_bfloat16* __restrict__ scales, const __hip_bfloat16* __restrict__ zeros,
                                               const __hip_bfloat16* __restrict__ U, const __hip_bfloat16* __restrict__ V,
                                               char* ws, __hip_bfloat16* __restrict__ y) {
  extern __shared__ __attribute__((aligned(16))) char smem_[];
  lds_char* smem = (lds_char*)smem_;
  const unsigned lb = (unsigned)(size_t)smem;
  const int wid = __builtin_amdgcn_readfirstlane(threadIdx.x >> 6), lane = threadIdx.x & 63;
  const int xcd = (XCD_PERM >> ((blockIdx.x & 7) * 4)) & 7, idx = blockIdx.x >> 3;
  const int h = lane >> 5, r = lane & 31;
  const int blk = (lane >> 4) & 1, q4 = (lane & 15) >> 2, p4 = lane & 3;
  lds_u32* sw = (lds_u32*)(smem + L_SYNC);
  gu64* sync = (gu64*)(ws + SYNC_OFF);
  const __amdgpu_buffer_rsrc_t wsr = __builtin_amdgcn_make_buffer_rsrc(ws, 0, SYNC_OFF, 0x00020000);
  const bool is_pf = idx < 12;
  if (wid == 0) STAMP(0);

  auto put_token = [&](u64 epoch) {
    if (wid == 3 && lane == 0) {
      const u64 e = epoch + 0x9E3779B97F4A7C15ull;
      sw[0] = (unsigned)e; sw[1] = (unsigned)(e >> 32);
      __hip_atomic_store(sw + 2, 1u, RLX_WG);
      if (is_pf) __hip_atomic_store(sync + (FP + xcd * 12 + idx) * FS, e, RLX_AGENT);
    }
  };
  if (wid == 3) {
    if (lane < 16) sw[lane] = ((lane == 3 || lane == 8) && is_pf) ? 4u : 0u;
  } else if (wid == 1 && idx < 2) {
    u64 pc;
    asm volatile("s_getpc_b64 %0" : "=s"(pc));
    set_m0(lb + L_DUMP);
    touch((const char*)pc, min(idx * 8192 + lane * 128, CODE_TOUCH_MAX));
  }
  int myflag = 0;

  if (is_pf) {
    if (wid == 0) {
      set_m0(lb + L_DUMP);
      const bool tg = xcd < 6;
      const unsigned ao = tg ? (lane >> 1) * (F_ * 4) + (lane & 1) * 128 : lane * (D_ * 2);
      const unsigned bo = tg ? (lane >> 3) * (F_ * 2) + (lane & 7) * 128 : lane * 128;
      const char* ab = tg ? (const char*)qw + (size_t)((xcd & 1) * 320) * (F_ * 4) : (const char*)x + (size_t)((xcd - 6) * 1024) * (D_ * 2);
      const char* bb = tg ? (const char*)V + (xcd >> 1) * 1024 : (const char*)U;
      const int na = tg ? 10 : 16, nt_ = tg ? 18 : 26, np = tg ? 24 : 10;
      const int aj = tg ? 32 * F_ * 4 : 64 * D_ * 2, ak = tg ? 256 : 128;
      const int bj = tg ? 8 * F_ * 2 : 8192, bk = tg ? 64 * F_ * 2 : 64 * D_ * 2;
      int p = 0, j = idx, n = 0;
      __builtin_amdgcn_s_waitcnt(0x0F70);
#pragma unroll 1
      while (p < np) {
        if (j < na) touch(ab + j * aj + p * ak, ao);
        else touch(bb + (j - na) * bj + p * bk, bo);
        j += 12;
        if (j >= nt_) { j -= nt_; ++p; }
        if (++n == 3) BAR();
      }
      STAMP(7);
    } else {
      WAIT_L(0);
      BAR();
      if (wid == 3) put_token(__hip_atomic_load(sync, RLX_AGENT));
    }
  } else if (xcd < 6) {
    const int t = idx - 12;
    const int mt = (xcd & 1) * 5 + t % 5, nt = (xcd >> 1) * 4 + t / 5;
    myflag = FT + nt * 10 + mt;
    if (wid >= 4) {
      const int w4 = wid - 4, rr = lane >> 4, c16 = lane & 15;
      unsigned ag[4], bg[4];
#pragma unroll
      for (int i = 0; i < 4; ++i) {
        int row = i * 4 + rr;
        ag[i] = row * (F_ * 4) + ((c16 ^ row) * 16) - i * 1024;
        bg[i] = row * (F_ * 2) + ((c16 ^ (rr << 2)) * 16) - i * 1024;
      }
      const char* ap = (const char*)qw + (size_t)(mt * 64 + w4 * 16) * (F_ * 4);
      const char* bp = (const char*)V + nt * 256 + (size_t)(w4 * 16) * (F_ * 2);
      unsigned so = lb + w4 * 4096;
      auto issue = [&](int p) {
        unsigned base = so + (p & 3) * 32768;
        set_m0(base);
        glds<0>(ap, ag[0]); glds<1024>(ap, ag[1]); glds<2048>(ap, ag[2]); glds<3072>(ap, ag[3]);
        set_m0(base + 16384);
        glds<0>(bp, bg[0]); glds<1024>(bp, bg[1]); glds<2048>(bp, bg[2]); glds<3072>(bp, bg[3]);
        ap += 256; bp += 64 * F_ * 2;
      };
      issue(0);
      issue(1);
      BAR();
#pragma unroll 1
      for (int p = 2; p < 24; ++p) {
        WAIT_V(8);
        BAR();
        issue(p);
      }
      WAIT_V(8);
      BAR();
      WAIT_V(0);
      BAR();
    } else {
      const int m = wid & 1, kl = wid >> 1;
      {
        const size_t o = (size_t)((mt * 2 + m) * F_ + kl * 768) * 2;
        lds_char* T = smem + 131072 + m * 12288 + kl * 3072;
        u32x4 s8[2], z8[2];
#pragma unroll
        for (int j = 0; j < 2; ++j) {
          int e = j ? 64 + (lane & 31) : lane;
          s8[j] = *(const u32x4*)((const char*)scales + o + e * 16);
          z8[j] = *(const u32x4*)((const char*)zeros + o + e * 16);
        }
        const u64 epoch = __hip_atomic_load(sync, RLX_AGENT);
#pragma unroll
        for (int j = 0; j < 2; ++j) {
          int e = j ? 64 + (lane & 31) : lane;
          f32x4 sa, sb, za, zb;
          sa[0] = bflo(s8[j][0]); sa[1] = bfhi(s8[j][0]); sa[2] = bflo(s8[j][1]); sa[3] = bfhi(s8[j][1]);
          sb[0] = bflo(s8[j][2]); sb[1] = bfhi(s8[j][2]); sb[2] = bflo(s8[j][3]); sb[3] = bfhi(s8[j][3]);
          za[0] = -bflo(z8[j][0]) * sa[0]; za[1] = -bfhi(z8[j][0]) * sa[1]; za[2] = -bflo(z8[j][1]) * sa[2]; za[3] = -bfhi(z8[j][1]) * sa[3];
          zb[0] = -bflo(z8[j][2]) * sb[0]; zb[1] = -bfhi(z8[j][2]) * sb[1]; zb[2] = -bflo(z8[j][3]) * sb[2]; zb[3] = -bfhi(z8[j][3]) * sb[3];
          ldsw<f32x4>(T + e * 32, sa); ldsw<f32x4>(T + e * 32 + 16, sb);
          ldsw<f32x4>(T + 6144 + e * 32, za); ldsw<f32x4>(T + 6144 + e * 32 + 16, zb);
        }
        put_token(epoch);
        WAIT_L(0);
        BAR();
      }
      int aoff[4], boff[4];
#pragma unroll
      for (int c = 0; c < 4; ++c) {
        int ch = 8 * kl + 4 * (c >> 1) + 2 * h + (c & 1);
        aoff[c] = (m * 32 + r) * 256 + ((ch ^ (r & 15)) * 16);
        boff[c] = 16384 + (32 * kl + 8 * h + q4) * 256 + ((c ^ (2 * kl) ^ q4) * 64) + 32 * (p4 & 1) + 16 * blk + 8 * (p4 >> 1);
      }
      lds_char* tp = smem + 131072 + m * 12288 + (32 * kl + 8 * h) * 4;
      f32x16 acc[4] = {};
      f32x4 ts[4], tz[4];
      u32x4 av[4];
      u32x4 af[2][2] = {}, bf[2][2][4] = {};
      auto rdtab = [&](int o, int c) {
        ts[c] = ldsr<f32x4>(tp + o + (c >> 1) * 64 + (c & 1) * 16);
        tz[c] = ldsr<f32x4>(tp + o + 6144 + (c >> 1) * 64 + (c & 1) * 16);
      };
      auto step = [&](int slot) {
        const int buf = slot & 1;
        lds_char* sl = smem + slot * 32768;
        BAR();
#pragma unroll
        for (int c = 0; c < 4; ++c) av[c] = ldsr<u32x4>(sl + aoff[c]);
        SB();
#pragma unroll
        for (int u = 0; u < 8; ++u) {
          acc[u & 3] = mfma32(af[buf ^ 1][u >> 2], bf[buf ^ 1][u >> 2][u & 3], acc[u & 3]);
          SB();
          if (u < 4) {
#pragma unroll
            for (int v = 2 * u; v < 2 * u + 2; ++v)
              bf[buf][v >> 2][v & 3] = trread2(sl + boff[v & 3] + (16 * (v >> 2)) * 256, sl + boff[v & 3] + (16 * (v >> 2) + 4) * 256);
          } else {
            const int c = u - 4;
            u32x2 o = deq4(av[c], ts[c], tz[c]);
            af[buf][c >> 1][(c & 1) * 2] = o[0]; af[buf][c >> 1][(c & 1) * 2 + 1] = o[1];
            SB();
            rdtab((slot + 1) * 256, c);
          }
          SB();
        }
      };
#pragma unroll
      for (int c = 0; c < 4; ++c) rdtab(0, c);
#pragma unroll 1
      for (int p = 0; p < 24; p += 4) {
        step(0); step(1); step(2); step(3);
        tp += 1024;
      }
#pragma unroll
      for (int u = 0; u < 8; ++u) {
        const int c = (u & 1) + (u < 4 ? 2 : 0), s2 = (u >> 1) & 1;
        acc[c] = mfma32(af[1][s2], bf[1][s2][c], acc[c]);
      }
      if (wid == 0) STAMP(1);
      lds_char* RED = smem + m * 16384;
#pragma unroll
      for (int c = 0; c < 2; ++c)
#pragma unroll
        for (int j = 0; j < 4; ++j) {
          const f32x16& g = acc[2 + c];
          ldsw<f32x4>(RED + kl * 8192 + c * 4096 + j * 1024 + lane * 16, f32x4{g[4 * j], g[4 * j + 1], g[4 * j + 2], g[4 * j + 3]});
        }
      WAIT_L(0);
      if (lane == 0) __hip_atomic_fetch_add(sw + 6 + m, 1u, RLX_WG);
      lds_spin(sw + 6 + m, 2);
#pragma unroll
      for (int c = 0; c < 2; ++c) {
        f32x16 o = acc[c];
#pragma unroll
        for (int j = 0; j < 4; ++j) {
          f32x4 v = ldsr<f32x4>(RED + (kl ^ 1) * 8192 + c * 4096 + j * 1024 + lane * 16);
          o[4 * j] += v[0]; o[4 * j + 1] += v[1]; o[4 * j + 2] += v[2]; o[4 * j + 3] += v[3];
        }
        const int n32 = nt * 4 + kl * 2 + c;
        const unsigned oo = (((n32 / 3) * 40 + (mt * 2 + m) * 2) * 3 + n32 % 3) * 1024;
        __builtin_amdgcn_raw_buffer_store_b128(pack8(o, 0), wsr, lane * 16, oo, 16);
        __builtin_amdgcn_raw_buffer_store_b128(pack8(o, 1), wsr, lane * 16 + 3072, oo, 16);
      }
    }
  } else {
    const int t = idx - 12;
    const int xb_ = (xcd - 6) * 4 + (t & 3), eb = t >> 2;
    myflag = FX + xb_ * 5 + eb;
    if (wid >= 4) {
      const int w4 = wid - 4;
      unsigned xg[4], ug[4];
#pragma unroll
      for (int i = 0; i < 4; ++i) {
        int row = i * 8 + (lane >> 3);
        xg[i] = row * (D_ * 2) + (((lane & 7) ^ ((row >> 1) & 7)) * 16) - i * 1024;
        ug[i] = (i * 4 + (lane >> 4)) * (D_ * 2) + (((lane & 15) ^ ((lane >> 4) << 2)) * 16) - i * 1024;
      }
      const char* xp = (const char*)x + (size_t)(xb_ * 256 + w4 * 64) * (D_ * 2);
      const char* up = (const char*)U + eb * 256 + (size_t)(w4 * 16) * (D_ * 2);
      unsigned so = 0;
      auto issue = [&]() {
        set_m0(lb + so + 32768 + w4 * 4096);
        glds<0>(up, ug[0]); glds<1024>(up, ug[1]); glds<2048>(up, ug[2]); glds<3072>(up, ug[3]);
        set_m0(lb + so + w4 * 8192);
        glds<0>(xp, xg[0]); glds<1024>(xp, xg[1]); glds<2048>(xp, xg[2]); glds<3072>(xp, xg[3]);
        set_m0(lb + so + w4 * 8192 + 4096);
        const char* xq = xp + 32 * D_ * 2;
        glds<0>(xq, xg[0]); glds<1024>(xq, xg[1]); glds<2048>(xq, xg[2]); glds<3072>(xq, xg[3]);
        xp += 128; up += 64 * D_ * 2;
        so = so == 98304 ? 0 : so + 49152;
      };
      issue();
      WAIT_V(0);
      BAR();
      issue();
#pragma unroll 1
      for (int st = 2; st < 10; ++st) {
        issue();
        WAIT_V(12);
        BAR();
      }
      WAIT_V(0);
      BAR();
    } else {
      int xoff[4], uoff[4];
#pragma unroll
      for (int s = 0; s < 4; ++s) {
        xoff[s] = (wid * 64 + r) * 128 + (((2 * s + h) ^ ((r >> 1) & 7)) * 16);
        uoff[s] = 32768 + (8 * h + q4) * 256 + ((s ^ q4) * 64) + blk * 32 + p4 * 8;
      }
      f32x16 acc[2][4] = {};
      u32x4 xf[2][2], uf[2][4];
      auto rd = [&](lds_char* base, int s, int buf, int i) {
        if (i < 2) xf[buf][i] = ldsr<u32x4>(base + xoff[s] + i * 4096);
        else uf[buf][i - 2] = trread2(base + uoff[i - 2] + s * 4096, base + uoff[i - 2] + s * 4096 + 1024);
      };
      if (wid == 3) put_token(__hip_atomic_load(sync, RLX_AGENT));
      WAIT_L(0);
      BAR();
#pragma unroll
      for (int i = 0; i < 6; ++i) rd(smem, 0, 0, i);
      unsigned so = 0;
#pragma unroll 1
      for (int st = 0; st < 10; ++st) {
#pragma unroll
        for (int s = 0; s < 4; ++s) {
          if (s == 3) so = so == 98304 ? 0 : so + 49152;
          lds_char* base = smem + so;
#pragma unroll
          for (int u = 0; u < 8; ++u) {
            acc[u & 1][u >> 1] = mfma32(uf[s & 1][u >> 1], xf[s & 1][u & 1], acc[u & 1][u >> 1]);
            SB();
            if (u == 0 && s == 3 && st < 9) { WAIT_L(0); BAR(); }
            if (u < 6) rd(base, (s + 1) & 3, (s + 1) & 1, u);
            SB();
          }
        }
      }
#pragma unroll
      for (int u = 0; u < 8; ++u) {
        const unsigned o = XRF_OFF + ((xb_ * 8 + wid * 2 + (u & 1)) * 40 + (eb * 4 + (u >> 1)) * 2) * 1024;
        __builtin_amdgcn_raw_buffer_store_b128(pack8(acc[u & 1][u >> 1], 0), wsr, lane * 16, o, 16);
        __builtin_amdgcn_raw_buffer_store_b128(pack8(acc[u & 1][u >> 1], 1), wsr, lane * 16 + 1024, o, 16);
      }
      if (wid == 0) STAMP(3);
    }
  }

  const int mb = (xcd >> 1) * 4 + (idx & 3), nb = (xcd & 1) * 8 + (idx >> 2);
  const unsigned vo = lane * 16;
  const char* xsrc = ws + XRF_OFF + (mb * 4 + (wid & 3)) * 40960;
  auto ldx = [&](int ch) {
    asm volatile("s_mov_b32 m0, %2\n\ts_nop 0\n\tglobal_load_lds_dwordx4 %0, %1 sc1\n\tglobal_load_lds_dwordx4 %0, %1 offset:1024 sc1\n\t"
                 "global_load_lds_dwordx4 %0, %1 offset:2048 sc1\n\tglobal_load_lds_dwordx4 %0, %1 offset:3072 sc1"
                 ::"v"(vo), "s"(xsrc + ch * 4096), "s"(lb + (ch % 5) * 28672 + 12288 + (wid & 3) * 4096) : "memory");
  };
  if (wid < 4) {
    lds_spin(sw + 2, 1);
    const u64 tok = (u64)sw[0] | ((u64)sw[1] << 32);
    if (!is_pf) {
      if (wid == 0) STAMP(10);
      if (lane == 0) __hip_atomic_fetch_add(sw + 8, 1u, RLX_WG);
      asm volatile("s_waitcnt vmcnt(0) lgkmcnt(0)" ::: "memory");
      if (lane == 0) {
        if (__hip_atomic_fetch_add(sw + 3, 1u, RLX_WG) == 3) __hip_atomic_store(sync + myflag * FS, tok, RLX_AGENT);
      }
      if (wid == 0) STAMP(2);
    }
    if (wid == 0) {
      lds_spin(sw + 3, 4);
      const int n0 = (nb * 3) >> 2, n1 = (nb * 3 + 2) >> 2;
      poll_flags(sync + (FT + n0 * 10 + min(lane, (n1 - n0) * 10 + 9)) * FS, tok);
      if (lane == 0) __hip_atomic_store(sw + 5, 1u, RLX_WG);
      STAMP(4);
    } else {
      lds_spin(sw + 5, 1);
    }
    const char* tsrc = ws + nb * 122880 + wid * 3072;
    auto ldt = [&](int ch) {
      asm volatile("s_mov_b32 m0, %2\n\ts_nop 0\n\tglobal_load_lds_dwordx4 %0, %1 sc1\n\tglobal_load_lds_dwordx4 %0, %1 offset:1024 sc1\n\t"
                   "global_load_lds_dwordx4 %0, %1 offset:2048 sc1" ::"v"(vo), "s"(tsrc + ch * 12288), "s"(lb + (ch % 5) * 28672 + wid * 3072) : "memory");
    };
    ldt(0); ldt(1); ldt(2);
    WAIT_V(6); BAR(); ldt(3);
    WAIT_V(6); BAR(); ldt(4);
    WAIT_V(6); BAR(); ldt(5); ldx(5);
    WAIT_V(10); BAR(); ldt(6); ldx(6);
    WAIT_V(14); BAR(); ldt(7); ldx(7);
    WAIT_V(14); BAR(); ldt(8); ldx(8);
    WAIT_V(14); BAR(); ldt(9); ldx(9);
    WAIT_V(14); BAR();
    WAIT_V(7); BAR();
    WAIT_V(0); BAR();
    if (blockIdx.x == 0 && wid == 1) {
#pragma unroll 1
      for (int i = 0; i < 4; ++i) {
        poll_flags(sync + (1 + i * 64 + lane) * FS, tok);
      }
      if (lane == 0) __hip_atomic_store(sync, tok, RLX_AGENT);
    }
  } else {
    const int w4 = wid - 4;
    if (wid == 4) {
      lds_spin(sw + 2, 1);
      const u64 tok = (u64)sw[0] | ((u64)sw[1] << 32);
      poll_flags(sync + (FX + (mb >> 1) * 5 + min(lane, 4)) * FS, tok);
      lds_spin(sw + 8, 4);
      if (lane == 0) __hip_atomic_store(sw + 4, 1u, RLX_WG);
      STAMP(8);
    } else {
      lds_spin(sw + 4, 1);
    }
#pragma unroll
    for (int ch = 0; ch < 5; ++ch) ldx(ch);
    f32x16 acc[3] = {};
    u32x4 tf[3][3], xf[3];
    lds_char* tb = smem + lane * 16;
    lds_char* xb = smem + lane * 16 + 12288 + w4 * 4096;
    auto rdf = [&](int ks, int c) {
      const int o = ((ks >> 2) % 5) * 28672;
      if (c < 3) tf[ks % 3][c] = ldsr<u32x4>(tb + o + ((ks & 3) * 3 + c) * 1024);
      else xf[ks % 3] = ldsr<u32x4>(xb + o + (ks & 3) * 1024);
    };
    WAIT_V(16);
    BAR();
#pragma unroll
    for (int c = 0; c < 8; ++c) rdf(c >> 2, 3 - (c & 3));
#pragma unroll
    for (int ks = 0; ks < 40; ++ks) {
#pragma unroll
      for (int c = 0; c < 3; ++c) {
        acc[c] = mfma32(tf[ks % 3][c], xf[ks % 3], acc[c]);
        SB();
        if (ks < 38) {
          if (c == 0) {
            if ((ks & 3) == 2) {
              if (ks == 2) WAIT_V(12);
              if (ks == 6) WAIT_V(8);
              if (ks == 10) WAIT_V(4);
              if (ks == 14) WAIT_V(0);
              BAR(); asm volatile("" ::: "memory");
            }
            rdf(ks + 2, 3);
          }
          rdf(ks + 2, c);
        }
        SB();
      }
    }
    if (wid == 4) STAMP(5);
    lds_char* eb = smem + w4 * 6656;
#pragma unroll
    for (int c = 0; c < 3; ++c) {
      ldsw<u32x4>(eb + r * 208 + c * 64 + h * 32, pack8(acc[c], 0));
      ldsw<u32x4>(eb + r * 208 + c * 64 + h * 32 + 16, pack8(acc[c], 1));
    }
    const __amdgpu_buffer_rsrc_t yr = __builtin_amdgcn_make_buffer_rsrc((void*)y, 0, 2048 * F_ * 2, 0x00020000);
    const unsigned yo = ((mb * 128 + w4 * 32) * F_ + nb * 96) * 2;
#pragma unroll
    for (int i = 0; i < 6; ++i) {
      int q = i * 64 + lane, row = q / 12, chn = q % 12;
      __builtin_amdgcn_raw_buffer_store_b128(ldsr<u32x4>(eb + row * 208 + chn * 16), yr, row * (F_ * 2) + chn * 16, yo, Y_AUX);
    }
  }
}

extern "C" void kernel_launch(void* const* d_in, const int* in_sizes, int n_in, void* d_out, int out_size, void* d_ws,
                              size_t ws_size, hipStream_t stream) {
  (void)hipFuncSetAttribute((const void*)quip, hipFuncAttributeMaxDynamicSharedMemorySize, LDS_BYTES);
  quip<<<256, 512, LDS_BYTES, stream>>>((const __hip_bfloat16*)d_in[0], (const int*)d_in[1], (const __hip_bfloat16*)d_in[2],
                                        (const __hip_bfloat16*)d_in[3], (const __hip_bfloat16*)d_in[4], (const __hip_bfloat16*)d_in[5],
                                        (char*)d_ws, (__hip_bfloat16*)d_out);
}
```

```cpp
#include <hip/hip_runtime.h>
#include <hip/hip_bf16.h>

typedef __attribute__((ext_vector_type(8))) __bf16 bf16x8;
typedef __attribute__((ext_vector_type(2))) __bf16 bf16x2;
typedef __attribute__((ext_vector_type(4))) short s16x4;
typedef __attribute__((ext_vector_type(16))) float f32x16;
typedef __attribute__((ext_vector_type(4))) float f32x4;
typedef __attribute__((ext_vector_type(4))) unsigned u32x4;
typedef __attribute__((ext_vector_type(2))) unsigned u32x2;
typedef __attribute__((address_space(3))) s16x4 lds_s16x4;
typedef __attribute__((address_space(3))) char lds_char;
typedef __attribute__((address_space(3))) unsigned lds_u32;
typedef __attribute__((address_space(1))) unsigned long long gu64;
typedef unsigned long long u64;

#define STAMP(i)
#define Y_AUX 2
#define POLL_SLEEP 4
constexpr int D_ = 640, F_ = 1536;

#define WAIT_V(n) asm volatile("s_waitcnt vmcnt(" #n ")" ::: "memory")
#define WAIT_L(n) asm volatile("s_waitcnt lgkmcnt(" #n ")" ::: "memory")
#define BAR() __builtin_amdgcn_s_barrier()
#define SB() __builtin_amdgcn_sched_barrier(0)
#define RLX_AGENT __ATOMIC_RELAXED, __HIP_MEMORY_SCOPE_AGENT
#define RLX_WG __ATOMIC_RELAXED, __HIP_MEMORY_SCOPE_WORKGROUP

__device__ __forceinline__ void set_m0(unsigned ldsaddr) { asm volatile("s_mov_b32 m0, %0\n\ts_nop 0" ::"s"(ldsaddr) : "memory"); }
template <int OFF> __device__ __forceinline__ void glds(const void* sbase, unsigned voff) {
  asm volatile("global_load_lds_dwordx4 %0, %1 offset:%2" ::"v"(voff), "s"(sbase), "i"(OFF) : "memory");
}
__device__ __forceinline__ void touch(const void* sbase, unsigned voff) {
  asm volatile("global_load_lds_dword %0, %1" ::"v"(voff), "s"(sbase) : "memory");
}
__device__ __forceinline__ unsigned pack2(float a, float b) {
  bf16x2 p; p[0] = (__bf16)a; p[1] = (__bf16)b;
  return __builtin_bit_cast(unsigned, p);
}
__device__ __forceinline__ float bflo(unsigned u) { return __builtin_bit_cast(float, u << 16); }
__device__ __forceinline__ float bfhi(unsigned u) { return __builtin_bit_cast(float, u & 0xffff0000u); }
__device__ __forceinline__ u32x4 trread2(lds_char* p0, lds_char* p1) {
  u32x2 lo = __builtin_bit_cast(u32x2, __builtin_amdgcn_ds_read_tr16_b64_v4i16((lds_s16x4*)p0));
  u32x2 hi = __builtin_bit_cast(u32x2, __builtin_amdgcn_ds_read_tr16_b64_v4i16((lds_s16x4*)p1));
  return u32x4{lo[0], lo[1], hi[0], hi[1]};
}
template <typename T> __device__ __forceinline__ T ldsr(lds_char* p) { return *(__attribute__((address_space(3))) T*)p; }
template <typename T> __device__ __forceinline__ void ldsw(lds_char* p, T v) { *(__attribute__((address_space(3))) T*)p = v; }
__device__ __forceinline__ f32x16 mfma32(u32x4 a, u32x4 b, f32x16 c) {
  return __builtin_amdgcn_mfma_f32_32x32x16_bf16(__builtin_bit_cast(bf16x8, a), __builtin_bit_cast(bf16x8, b), c, 0, 0, 0);
}
__device__ __forceinline__ u32x4 pack8(const f32x16& a, int s) {
  u32x4 r;
  r[0] = pack2(a[8 * s + 0], a[8 * s + 1]); r[1] = pack2(a[8 * s + 2], a[8 * s + 3]);
  r[2] = pack2(a[8 * s + 4], a[8 * s + 5]); r[3] = pack2(a[8 * s + 6], a[8 * s + 7]);
  return r;
}
__device__ __forceinline__ u32x2 deq4(u32x4 q, f32x4 s, f32x4 z) {
  float w0, w1, w2, w3;
  asm("v_cvt_f32_i32 %0, %4\n\tv_cvt_f32_i32 %1, %5\n\tv_cvt_f32_i32 %2, %6\n\tv_cvt_f32_i32 %3, %7\n\t"
      "v_fma_f32 %0, %0, %8, %12\n\tv_fma_f32 %1, %1, %9, %13\n\tv_fma_f32 %2, %2, %10, %14\n\tv_fma_f32 %3, %3, %11, %15\n\t"
      "v_cvt_pk_bf16_f32 %0, %0, %1\n\tv_cvt_pk_bf16_f32 %1, %2, %3"
      : "=&v"(w0), "=&v"(w1), "=&v"(w2), "=&v"(w3)
      : "v"(q[0]), "v"(q[1]), "v"(q[2]), "v"(q[3]), "v"(s[0]), "v"(s[1]), "v"(s[2]), "v"(s[3]), "v"(z[0]), "v"(z[1]), "v"(z[2]), "v"(z[3]));
  return u32x2{__builtin_bit_cast(unsigned, w0), __builtin_bit_cast(unsigned, w1)};
}
__device__ __forceinline__ void lds_spin(lds_u32* w, unsigned want) {
  while ((unsigned)__builtin_amdgcn_readfirstlane(__hip_atomic_load(w, RLX_WG)) < want) __builtin_amdgcn_s_sleep(1);
  asm volatile("" ::: "memory");
}
__device__ __forceinline__ void poll_flags(gu64* f, u64 tok) {
#pragma unroll 1
  for (unsigned spins = 0; spins < (1u << 20); ++spins) {
    if (__all(__hip_atomic_load(f, RLX_AGENT) == tok)) break;
    __builtin_amdgcn_s_sleep(POLL_SLEEP);
  }
  __builtin_amdgcn_fence(__ATOMIC_ACQUIRE, "wavefront");
}

constexpr int XRF_OFF = 2 << 20, SYNC_OFF = 6 << 20;
constexpr int FT = 16, FX = 16 + 12 * 16, FP = FX + 16 * 16;
constexpr int CODE_TOUCH_MAX = 14336;
constexpr int L_SYNC = 156160, L_DUMP = 156416, LDS_BYTES = 156672;

__global__ __launch_bounds__(512, 2) void quip(const __hip_bfloat16* __restrict__ x, const int* __restrict__ qw,
                                               const __hip_bfloat16* __restrict__ scales, const __hip_bfloat16* __restrict__ zeros,
                                               const __hip_bfloat16* __restrict__ U, const __hip_bfloat16* __restrict__ V,
                                               char* ws, __hip_bfloat16* __restrict__ y) {
  extern __shared__ __attribute__((aligned(16))) char smem_[];
  lds_char* smem = (lds_char*)smem_;
  const unsigned lb = (unsigned)(size_t)smem;
  const int wid = __builtin_amdgcn_readfirstlane(threadIdx.x >> 6), lane = threadIdx.x & 63;
  const int xcd = blockIdx.x & 7, idx = blockIdx.x >> 3;
  const int h = lane >> 5, r = lane & 31;
  const int blk = (lane >> 4) & 1, q4 = (lane & 15) >> 2, p4 = lane & 3;
  lds_u32* sw = (lds_u32*)(smem + L_SYNC);
  gu64* sync = (gu64*)(ws + SYNC_OFF);
  const __amdgpu_buffer_rsrc_t wsr = __builtin_amdgcn_make_buffer_rsrc(ws, 0, SYNC_OFF, 0x00020000);
  const bool is_pf = xcd < 6 && idx < 12;
  if (wid == 0) STAMP(0);

  auto put_token = [&](u64 epoch) {
    if (wid == 3 && lane == 0) {
      const u64 e = epoch + 0x9E3779B97F4A7C15ull;
      sw[0] = (unsigned)e; sw[1] = (unsigned)(e >> 32);
      __hip_atomic_store(sw + 2, 1u, RLX_WG);
      if (is_pf) __hip_atomic_store(sync + FP + xcd * 12 + idx, e, RLX_AGENT);
    }
  };
  if (wid == 3) {
    if (lane < 16) sw[lane] = ((lane == 3 || lane == 8) && is_pf) ? 4u : 0u;
  } else if (wid == 1 && idx < 2) {
    u64 pc;
    asm volatile("s_getpc_b64 %0" : "=s"(pc));
    set_m0(lb + L_DUMP);
    touch((const char*)pc, min(idx * 8192 + lane * 128, CODE_TOUCH_MAX));
  }
  int myflag = 0;

  if (is_pf) {
    if (wid == 0) {
      const int a = xcd & 1, b = xcd >> 1;
      set_m0(lb + L_DUMP);
      const unsigned qo = (lane >> 1) * (F_ * 4) + (lane & 1) * 128, vo = (lane >> 3) * (F_ * 2) + (lane & 7) * 128;
      const char* qb = (const char*)qw + (size_t)(a * 320) * (F_ * 4);
      const char* vb = (const char*)V + b * 1024;
      int p = 0, j = idx;
#pragma unroll 1
      for (int n = 0; n < 36; ++n) {
        if (j < 10) touch(qb + (size_t)j * 32 * (F_ * 4) + p * 256, qo);
        else touch(vb + (size_t)(p * 64 + (j - 10) * 8) * (F_ * 2), vo);
        j += 12;
        if (j >= 18) { j -= 18; ++p; }
        if (n == 2) BAR();
      }
      STAMP(7);
    } else {
      WAIT_L(0);
      BAR();
      if (wid == 3) put_token(__hip_atomic_load(sync, RLX_AGENT));
    }
  } else if (xcd < 6) {
    const int t = idx - 12;
    const int mt = (xcd & 1) * 5 + t % 5, nt = (xcd >> 1) * 4 + t / 5;
    myflag = FT + nt * 16 + mt;
    if (wid >= 4) {
      const int w4 = wid - 4, rr = lane >> 4, c16 = lane & 15;
      unsigned ag[4], bg[4];
#pragma unroll
      for (int i = 0; i < 4; ++i) {
        int row = i * 4 + rr;
        ag[i] = row * (F_ * 4) + ((c16 ^ row) * 16) - i * 1024;
        bg[i] = row * (F_ * 2) + ((c16 ^ (rr << 2)) * 16) - i * 1024;
      }
      const char* ap = (const char*)qw + (size_t)(mt * 64 + w4 * 16) * (F_ * 4);
      const char* bp = (const char*)V + nt * 256 + (size_t)(w4 * 16) * (F_ * 2);
      unsigned so = lb + w4 * 4096;
      auto issue = [&](int p) {
        unsigned base = so + (p & 3) * 32768;
        set_m0(base);
        glds<0>(ap, ag[0]); glds<1024>(ap, ag[1]); glds<2048>(ap, ag[2]); glds<3072>(ap, ag[3]);
        set_m0(base + 16384);
        glds<0>(bp, bg[0]); glds<1024>(bp, bg[1]); glds<2048>(bp, bg[2]); glds<3072>(bp, bg[3]);
        ap += 256; bp += 64 * F_ * 2;
      };
      issue(0);
      issue(1);
      BAR();
#pragma unroll 1
      for (int p = 2; p < 24; ++p) {
        WAIT_V(8);
        BAR();
        issue(p);
      }
      WAIT_V(8);
      BAR();
      WAIT_V(0);
      BAR();
    } else {
      const int m = wid & 1, kl = wid >> 1;
      {
        const size_t o = (size_t)((mt * 2 + m) * F_ + kl * 768) * 2;
        lds_char* T = smem + 131072 + m * 12288 + kl * 3072;
        u32x4 s8[2], z8[2];
#pragma unroll
        for (int j = 0; j < 2; ++j) {
          int e = j ? 64 + (lane & 31) : lane;
          s8[j] = *(const u32x4*)((const char*)scales + o + e * 16);
          z8[j] = *(const u32x4*)((const char*)zeros + o + e * 16);
        }
        const u64 epoch = __hip_atomic_load(sync, RLX_AGENT);
#pragma unroll
        for (int j = 0; j < 2; ++j) {
          int e = j ? 64 + (lane & 31) : lane;
          f32x4 sa, sb, za, zb;
          sa[0] = bflo(s8[j][0]); sa[1] = bfhi(s8[j][0]); sa[2] = bflo(s8[j][1]); sa[3] = bfhi(s8[j][1]);
          sb[0] = bflo(s8[j][2]); sb[1] = bfhi(s8[j][2]); sb[2] = bflo(s8[j][3]); sb[3] = bfhi(s8[j][3]);
          za[0] = -bflo(z8[j][0]) * sa[0]; za[1] = -bfhi(z8[j][0]) * sa[1]; za[2] = -bflo(z8[j][1]) * sa[2]; za[3] = -bfhi(z8[j][1]) * sa[3];
          zb[0] = -bflo(z8[j][2]) * sb[0]; zb[1] = -bfhi(z8[j][2]) * sb[1]; zb[2] = -bflo(z8[j][3]) * sb[2]; zb[3] = -bfhi(z8[j][3]) * sb[3];
          ldsw<f32x4>(T + e * 32, sa); ldsw<f32x4>(T + e * 32 + 16, sb);
          ldsw<f32x4>(T + 6144 + e * 32, za); ldsw<f32x4>(T + 6144 + e * 32 + 16, zb);
        }
        put_token(epoch);
        WAIT_L(0);
        BAR();
      }
      int aoff[4], boff[4];
#pragma unroll
      for (int c = 0; c < 4; ++c) {
        int ch = 8 * kl + 4 * (c >> 1) + 2 * h + (c & 1);
        aoff[c] = (m * 32 + r) * 256 + ((ch ^ (r & 15)) * 16);
        boff[c] = 16384 + (32 * kl + 8 * h + q4) * 256 + ((c ^ (2 * kl) ^ q4) * 64) + 32 * (p4 & 1) + 16 * blk + 8 * (p4 >> 1);
      }
      lds_char* tp = smem + 131072 + m * 12288 + (32 * kl + 8 * h) * 4;
      f32x16 acc[4] = {};
      f32x4 ts[4], tz[4];
      u32x4 av[4];
      u32x4 af[2][2] = {}, bf[2][2][4] = {};
      auto rdtab = [&](int o, int c) {
        ts[c] = ldsr<f32x4>(tp + o + (c >> 1) * 64 + (c & 1) * 16);
        tz[c] = ldsr<f32x4>(tp + o + 6144 + (c >> 1) * 64 + (c & 1) * 16);
      };
      auto step = [&](int slot) {
        const int buf = slot & 1;
        lds_char* sl = smem + slot * 32768;
        BAR();
#pragma unroll
        for (int c = 0; c < 4; ++c) av[c] = ldsr<u32x4>(sl + aoff[c]);
        SB();
#pragma unroll
        for (int u = 0; u < 8; ++u) {
          acc[u & 3] = mfma32(af[buf ^ 1][u >> 2], bf[buf ^ 1][u >> 2][u & 3], acc[u & 3]);
          SB();
          if (u < 4) {
#pragma unroll
            for (int v = 2 * u; v < 2 * u + 2; ++v)
              bf[buf][v >> 2][v & 3] = trread2(sl + boff[v & 3] + (16 * (v >> 2)) * 256, sl + boff[v & 3] + (16 * (v >> 2) + 4) * 256);
          } else {
            const int c = u - 4;
            u32x2 o = deq4(av[c], ts[c], tz[c]);
            af[buf][c >> 1][(c & 1) * 2] = o[0]; af[buf][c >> 1][(c & 1) * 2 + 1] = o[1];
            SB();
            rdtab((slot + 1) * 256, c);
          }
          SB();
        }
      };
#pragma unroll
      for (int c = 0; c < 4; ++c) rdtab(0, c);
#pragma unroll 1
      for (int p = 0; p < 24; p += 4) {
        step(0); step(1); step(2); step(3);
        tp += 1024;
      }
#pragma unroll
      for (int u = 0; u < 8; ++u) acc[u & 3] = mfma32(af[1][u >> 2], bf[1][u >> 2][u & 3], acc[u & 3]);
      if (wid == 0) STAMP(1);
      lds_char* RED = smem + m * 16384;
#pragma unroll
      for (int c = 0; c < 2; ++c)
#pragma unroll
        for (int j = 0; j < 4; ++j) {
          const f32x16& g = acc[2 + c];
          ldsw<f32x4>(RED + kl * 8192 + c * 4096 + j * 1024 + lane * 16, f32x4{g[4 * j], g[4 * j + 1], g[4 * j + 2], g[4 * j + 3]});
        }
      WAIT_L(0);
      if (lane == 0) __hip_atomic_fetch_add(sw + 6 + m, 1u, RLX_WG);
      lds_spin(sw + 6 + m, 2);
#pragma unroll
      for (int c = 0; c < 2; ++c) {
        f32x16 o = acc[c];
#pragma unroll
        for (int j = 0; j < 4; ++j) {
          f32x4 v = ldsr<f32x4>(RED + (kl ^ 1) * 8192 + c * 4096 + j * 1024 + lane * 16);
          o[4 * j] += v[0]; o[4 * j + 1] += v[1]; o[4 * j + 2] += v[2]; o[4 * j + 3] += v[3];
        }
        const int n32 = nt * 4 + kl * 2 + c;
        const unsigned oo = (((n32 / 3) * 40 + (mt * 2 + m) * 2) * 3 + n32 % 3) * 1024;
        __builtin_amdgcn_raw_buffer_store_b128(pack8(o, 0), wsr, lane * 16, oo, 16);
        __builtin_amdgcn_raw_buffer_store_b128(pack8(o, 1), wsr, lane * 16 + 3072, oo, 16);
      }
    }
  } else {
    const int mi = idx & 7, eb = idx >> 3;
    const int xmb = (xcd - 6) * 8 + mi;
    myflag = FX + xmb * 16 + eb;
    if (wid >= 4) {
      const int w4 = wid - 4;
      unsigned xg[4], ug[5];
#pragma unroll
      for (int i = 0; i < 4; ++i) {
        int row = i * 8 + (lane >> 3);
        xg[i] = row * (D_ * 2) + (((lane & 7) ^ ((row >> 1) & 7)) * 16) - i * 1024;
      }
#pragma unroll
      for (int i = 0; i < 5; ++i) {
        int off = i * 1024 + lane * 16;
        ug[i] = (off / 320) * (D_ * 2) + off % 320 - (i & 3) * 1024;
      }
      const char* xb = (const char*)x + (size_t)(xmb * 128 + w4 * 32) * (D_ * 2);
      const char* ub = (const char*)U + eb * 320 + (size_t)(w4 * 16) * (D_ * 2);
      int ks = mi + ((eb * 5 + 1) >> 1);
      if (ks >= 10) ks -= 10;
      auto issue = [&](int st) {
        unsigned base = lb + (st & 3) * 36864;
        const char* xp = xb + ks * 128;
        const char* up = ub + (size_t)ks * 64 * (D_ * 2);
        set_m0(base + w4 * 4096);
        glds<0>(xp, xg[0]); glds<1024>(xp, xg[1]); glds<2048>(xp, xg[2]); glds<3072>(xp, xg[3]);
        set_m0(base + 16384 + w4 * 5120);
        glds<0>(up, ug[0]); glds<1024>(up, ug[1]); glds<2048>(up, ug[2]); glds<3072>(up, ug[3]);
        set_m0(base + 16384 + w4 * 5120 + 4096);
        glds<0>(up, ug[4]);
        if (++ks == 10) ks = 0;
      };
      issue(0);
      {
        set_m0(lb + L_DUMP);
        int kt = ks + (w4 >> 1);
        if (kt >= 10) kt -= 10;
        if (w4 < 2 || !(eb & 1)) touch((const char*)x + (size_t)(xmb * 128 + (w4 & 1) * 64) * (D_ * 2) + kt * 128, lane * (D_ * 2));
        if (w4 == 3 && mi < 6) {
          int ku = 8 + mi / 3 + ((eb * 5 + 1) >> 1);
          if (ku >= 10) ku -= 10;
          touch((const char*)U + (size_t)ku * 64 * (D_ * 2) + ((eb * 320) & ~127) + (mi % 3) * 128, lane * (D_ * 2));
        }
      }
#pragma unroll 1
      for (int st = 1; st < 10; ++st) {
        issue(st);
        WAIT_V(9);
        BAR();
      }
      WAIT_V(0);
      BAR();
    } else {
      int xoff[4];
#pragma unroll
      for (int s = 0; s < 4; ++s) xoff[s] = (wid * 32 + r) * 128 + (((2 * s + h) ^ ((r >> 1) & 7)) * 16);
      const int uoff = 16384 + (8 * h + q4) * 320 + blk * 32 + p4 * 8;
      f32x16 acc[5] = {};
      u32x4 xf[2], uf[2][5];
      auto rdu = [&](lds_char* base, int s, int buf, int c) {
        uf[buf][c] = trread2(base + uoff + c * 64 + (16 * s) * 320, base + uoff + c * 64 + (16 * s + 4) * 320);
      };
      if (wid == 3) put_token(__hip_atomic_load(sync, RLX_AGENT));
      WAIT_L(0);
      BAR();
      xf[0] = ldsr<u32x4>(smem + xoff[0]);
#pragma unroll
      for (int c = 0; c < 5; ++c) rdu(smem, 0, 0, c);
#pragma unroll 1
      for (int st = 0; st < 10; ++st) {
#pragma unroll
        for (int s = 0; s < 4; ++s) {
          lds_char* base = smem + ((s < 3 ? st : st + 1) & 3) * 36864;
          const int nx = (s + 1) & 3, nb_ = (s + 1) & 1;
#pragma unroll
          for (int c = 0; c < 5; ++c) {
            acc[c] = mfma32(uf[s & 1][c], xf[s & 1], acc[c]);
            SB();
            if (c == 0) {
              if (s == 3 && st < 9) { BAR(); asm volatile("" ::: "memory"); }
              xf[nb_] = ldsr<u32x4>(base + xoff[nx]);
              rdu(base, nx, nb_, 0);
            } else if (c < 4) {
              rdu(base, nx, nb_, c);
              if (c == 3) rdu(base, nx, nb_, 4);
            }
            SB();
          }
        }
      }
#pragma unroll
      for (int c = 0; c < 5; ++c) {
        const unsigned o = XRF_OFF + ((xmb * 4 + wid) * 40 + (eb * 5 + c) * 2) * 1024;
        __builtin_amdgcn_raw_buffer_store_b128(pack8(acc[c], 0), wsr, lane * 16, o, 16);
        __builtin_amdgcn_raw_buffer_store_b128(pack8(acc[c], 1), wsr, lane * 16 + 1024, o, 16);
      }
      if (wid == 0) STAMP(3);
    }
  }

  const int mb = (xcd >> 1) * 4 + (idx & 3), nb = (xcd & 1) * 8 + (idx >> 2);
  if (wid < 4) {
    lds_spin(sw + 2, 1);
    const u64 tok = (u64)sw[0] | ((u64)sw[1] << 32);
    if (!is_pf) {
      if (wid == 0) STAMP(10);
      if (lane == 0) __hip_atomic_fetch_add(sw + 8, 1u, RLX_WG);
      asm volatile("s_waitcnt vmcnt(0) lgkmcnt(0)" ::: "memory");
      if (lane == 0) {
        if (__hip_atomic_fetch_add(sw + 3, 1u, RLX_WG) == 3) __hip_atomic_store(sync + myflag, tok, RLX_AGENT);
      }
      if (wid == 0) STAMP(2);
    }
    if (wid == 0) {
      lds_spin(sw + 3, 4);
      lds_spin(sw + 4, 1);
      const int n0 = (nb * 3) >> 2, n1 = (nb * 3 + 2) >> 2;
      const int l20 = lane < 20 ? lane : 0;
      poll_flags(sync + FT + (l20 < 10 ? n0 * 16 + l20 : n1 * 16 + l20 - 10), tok);
      if (lane == 0) __hip_atomic_store(sw + 5, 1u, RLX_WG);
      STAMP(4);
    } else {
      lds_spin(sw + 5, 1);
    }
    const char* tsrc = ws + nb * 122880 + wid * 3072;
    unsigned tdst = lb + wid * 3072;
    const unsigned vo = lane * 16;
    auto issue = [&]() {
      asm volatile("s_mov_b32 m0, %2\n\ts_nop 0\n\tglobal_load_lds_dwordx4 %0, %1 sc1\n\tglobal_load_lds_dwordx4 %0, %1 offset:1024 sc1\n\t"
                   "global_load_lds_dwordx4 %0, %1 offset:2048 sc1" ::"v"(vo), "s"(tsrc), "s"(tdst) : "memory");
      tsrc += 12288; tdst += 12288;
    };
    issue(); issue(); issue(); issue();
#pragma unroll 1
    for (int ch = 0; ch < 6; ++ch) {
      WAIT_V(9);
      BAR();
      issue();
    }
    WAIT_V(9); BAR(); WAIT_V(6); BAR(); WAIT_V(3); BAR(); WAIT_V(0); BAR();
    if (blockIdx.x == 0 && wid == 1) {
#pragma unroll 1
      for (int i = 0; i < 4; ++i) {
        const int f = i * 64 + lane;
        const int o = f < 120 ? FT + (f / 10) * 16 + f % 10 : (f < 184 ? FX + ((f - 120) >> 2) * 16 + (f & 3) : FP + f - 184);
        poll_flags(sync + o, tok);
      }
      if (lane == 0) __hip_atomic_store(sync, tok, RLX_AGENT);
    }
  } else {
    const int w4 = wid - 4;
    if (wid == 4) {
      lds_spin(sw + 2, 1);
      const u64 tok = (u64)sw[0] | ((u64)sw[1] << 32);
      poll_flags(sync + FX + mb * 16 + (lane & 3), tok);
      lds_spin(sw + 8, 4);
      if (lane == 0) __hip_atomic_store(sw + 4, 1u, RLX_WG);
      STAMP(8);
    } else {
      lds_spin(sw + 4, 1);
    }
    u32x4 xf[40];
    const unsigned xo = XRF_OFF + (mb * 4 + w4) * 40960;
#pragma unroll
    for (int i = 0; i < 40; ++i) xf[i] = __builtin_amdgcn_raw_buffer_load_b128(wsr, lane * 16 + (i & 3) * 1024, xo + (i >> 2) * 4096, 16);
    SB();
    if (wid == 4) STAMP(11);
    f32x16 acc[3] = {};
    u32x4 tf[3][3];
    lds_char* tb = smem + lane * 16;
    BAR();
    if (wid == 4) STAMP(9);
#pragma unroll
    for (int c = 0; c < 6; ++c) tf[c / 3][c % 3] = ldsr<u32x4>(tb + c * 1024);
#pragma unroll
    for (int ks = 0; ks < 40; ++ks) {
#pragma unroll
      for (int c = 0; c < 3; ++c) {
        acc[c] = mfma32(tf[ks % 3][c], xf[ks], acc[c]);
        SB();
        if (ks < 38) {
          if (c == 0 && (ks & 3) == 2) { BAR(); asm volatile("" ::: "memory"); }
          tf[(ks + 2) % 3][c] = ldsr<u32x4>(tb + ((ks + 2) * 3 + c) * 1024);
        }
        SB();
      }
    }
    if (wid == 4) STAMP(5);
    lds_char* eb = smem + 122880 + w4 * 6656;
#pragma unroll
    for (int c = 0; c < 3; ++c) {
      ldsw<u32x4>(eb + r * 208 + c * 64 + h * 32, pack8(acc[c], 0));
      ldsw<u32x4>(eb + r * 208 + c * 64 + h * 32 + 16, pack8(acc[c], 1));
    }
    const __amdgpu_buffer_rsrc_t yr = __builtin_amdgcn_make_buffer_rsrc((void*)y, 0, 2048 * F_ * 2, 0x00020000);
    const unsigned yo = ((mb * 128 + w4 * 32) * F_ + nb * 96) * 2;
#pragma unroll
    for (int i = 0; i < 6; ++i) {
      int q = i * 64 + lane, row = q / 12, chn = q % 12;
      __builtin_amdgcn_raw_buffer_store_b128(ldsr<u32x4>(eb + row * 208 + chn * 16), yr, row * (F_ * 2) + chn * 16, yo, Y_AUX);
    }
  }
}

extern "C" void kernel_launch(void* const* d_in, const int* in_sizes, int n_in, void* d_out, int out_size, void* d_ws,
                              size_t ws_size, hipStream_t stream) {
  (void)hipFuncSetAttribute((const void*)quip, hipFuncAttributeMaxDynamicSharedMemorySize, LDS_BYTES);
  quip<<<256, 512, LDS_BYTES, stream>>>((const __hip_bfloat16*)d_in[0], (const int*)d_in[1], (const __hip_bfloat16*)d_in[2],
                                        (const __hip_bfloat16*)d_in[3], (const __hip_bfloat16*)d_in[4], (const __hip_bfloat16*)d_in[5],
                                        (char*)d_ws, (__hip_bfloat16*)d_out);
}
```

```cpp
#define AMP 0
#define AMP_BLK 121
#include <hip/hip_runtime.h>
#include <hip/hip_bf16.h>

typedef __attribute__((ext_vector_type(8))) __bf16 bf16x8;
typedef __attribute__((ext_vector_type(2))) __bf16 bf16x2;
typedef __attribute__((ext_vector_type(4))) short s16x4;
typedef __attribute__((ext_vector_type(16))) float f32x16;
typedef __attribute__((ext_vector_type(4))) float f32x4;
typedef __attribute__((ext_vector_type(4))) unsigned u32x4;
typedef __attribute__((ext_vector_type(2))) unsigned u32x2;
typedef __attribute__((address_space(3))) s16x4 lds_s16x4;
typedef __attribute__((address_space(3))) char lds_char;
typedef __attribute__((address_space(3))) unsigned lds_u32;
typedef __attribute__((address_space(1))) unsigned long long gu64;
typedef unsigned long long u64;

#define STAMP(i) do { if ((threadIdx.x & 63) == 0) sw[16 + (i)] = (unsigned)__builtin_amdgcn_s_memrealtime(); } while (0)
#define Y_AUX 2
#define XCD_PERM 0x54321706
#define POLL_SLEEP 4
constexpr int D_ = 640, F_ = 1536;

#define WAIT_V(n) asm volatile("s_waitcnt vmcnt(" #n ")" ::: "memory")
#define WAIT_L(n) asm volatile("s_waitcnt lgkmcnt(" #n ")" ::: "memory")
#define BAR() __builtin_amdgcn_s_barrier()
#define SB() __builtin_amdgcn_sched_barrier(0)
#define RLX_AGENT __ATOMIC_RELAXED, __HIP_MEMORY_SCOPE_AGENT
#define RLX_WG __ATOMIC_RELAXED, __HIP_MEMORY_SCOPE_WORKGROUP

__device__ __forceinline__ void set_m0(unsigned ldsaddr) { asm volatile("s_mov_b32 m0, %0\n\ts_nop 0" ::"s"(ldsaddr) : "memory"); }
template <int OFF> __device__ __forceinline__ void glds(const void* sbase, unsigned voff) {
  asm volatile("global_load_lds_dwordx4 %0, %1 offset:%2" ::"v"(voff), "s"(sbase), "i"(OFF) : "memory");
}
__device__ __forceinline__ void touch(const void* sbase, unsigned voff) {
  asm volatile("global_load_lds_dword %0, %1" ::"v"(voff), "s"(sbase) : "memory");
}
__device__ __forceinline__ unsigned pack2(float a, float b) {
  bf16x2 p; p[0] = (__bf16)a; p[1] = (__bf16)b;
  return __builtin_bit_cast(unsigned, p);
}
__device__ __forceinline__ float bflo(unsigned u) { return __builtin_bit_cast(float, u << 16); }
__device__ __forceinline__ float bfhi(unsigned u) { return __builtin_bit_cast(float, u & 0xffff0000u); }
__device__ __forceinline__ u32x4 trread2(lds_char* p0, lds_char* p1) {
  u32x2 lo = __builtin_bit_cast(u32x2, __builtin_amdgcn_ds_read_tr16_b64_v4i16((lds_s16x4*)p0));
  u32x2 hi = __builtin_bit_cast(u32x2, __builtin_amdgcn_ds_read_tr16_b64_v4i16((lds_s16x4*)p1));
  return u32x4{lo[0], lo[1], hi[0], hi[1]};
}
template <typename T> __device__ __forceinline__ T ldsr(lds_char* p) { return *(__attribute__((address_space(3))) T*)p; }
template <typename T> __device__ __forceinline__ void ldsw(lds_char* p, T v) { *(__attribute__((address_space(3))) T*)p = v; }
__device__ __forceinline__ f32x16 mfma32(u32x4 a, u32x4 b, f32x16 c) {
  return __builtin_amdgcn_mfma_f32_32x32x16_bf16(__builtin_bit_cast(bf16x8, a), __builtin_bit_cast(bf16x8, b), c, 0, 0, 0);
}
__device__ __forceinline__ u32x4 pack8(const f32x16& a, int s) {
  u32x4 r;
  r[0] = pack2(a[8 * s + 0], a[8 * s + 1]); r[1] = pack2(a[8 * s + 2], a[8 * s + 3]);
  r[2] = pack2(a[8 * s + 4], a[8 * s + 5]); r[3] = pack2(a[8 * s + 6], a[8 * s + 7]);
  return r;
}
__device__ __forceinline__ u32x2 deq4(u32x4 q, f32x4 s, f32x4 z) {
  float w0, w1, w2, w3;
  asm("v_cvt_f32_i32 %0, %4\n\tv_cvt_f32_i32 %1, %5\n\tv_cvt_f32_i32 %2, %6\n\tv_cvt_f32_i32 %3, %7\n\t"
      "v_fma_f32 %0, %0, %8, %12\n\tv_fma_f32 %1, %1, %9, %13\n\tv_fma_f32 %2, %2, %10, %14\n\tv_fma_f32 %3, %3, %11, %15\n\t"
      "v_cvt_pk_bf16_f32 %0, %0, %1\n\tv_cvt_pk_bf16_f32 %1, %2, %3"
      : "=&v"(w0), "=&v"(w1), "=&v"(w2), "=&v"(w3)
      : "v"(q[0]), "v"(q[1]), "v"(q[2]), "v"(q[3]), "v"(s[0]), "v"(s[1]), "v"(s[2]), "v"(s[3]), "v"(z[0]), "v"(z[1]), "v"(z[2]), "v"(z[3]));
  return u32x2{__builtin_bit_cast(unsigned, w0), __builtin_bit_cast(unsigned, w1)};
}
__device__ __forceinline__ void lds_spin(lds_u32* w, unsigned want) {
  while ((unsigned)__builtin_amdgcn_readfirstlane(__hip_atomic_load(w, RLX_WG)) < want) __builtin_amdgcn_s_sleep(1);
  asm volatile("" ::: "memory");
}
__device__ __forceinline__ void poll_flags(gu64* f, u64 tok) {
#pragma unroll 1
  for (unsigned spins = 0; spins < (1u << 20); ++spins) {
    if (__all(__hip_atomic_load(f, RLX_AGENT) == tok)) break;
    __builtin_amdgcn_s_sleep(POLL_SLEEP);
  }
  __builtin_amdgcn_fence(__ATOMIC_ACQUIRE, "wavefront");
}

constexpr int XRF_OFF = 2 << 20, SYNC_OFF = 6 << 20;
#define FS 16
constexpr int FT = 1, FX = 121, FP = 161;
constexpr int CODE_TOUCH_MAX = 15744;
constexpr int L_SYNC = 156160, L_DUMP = 156416, LDS_BYTES = 156672;

__global__ __launch_bounds__(512, 2) void quip(const __hip_bfloat16* __restrict__ x, const int* __restrict__ qw,
                                               const __hip_bfloat16* __restrict__ scales, const __hip_bfloat16* __restrict__ zeros,
                                               const __hip_bfloat16* __restrict__ U, const __hip_bfloat16* __restrict__ V,
                                               char* ws, __hip_bfloat16* __restrict__ y) {
  extern __shared__ __attribute__((aligned(16))) char smem_[];
  lds_char* smem = (lds_char*)smem_;
  const unsigned lb = (unsigned)(size_t)smem;
  const int wid = __builtin_amdgcn_readfirstlane(threadIdx.x >> 6), lane = threadIdx.x & 63;
  const int xcd = (XCD_PERM >> ((blockIdx.x & 7) * 4)) & 7, idx = blockIdx.x >> 3;
  const int h = lane >> 5, r = lane & 31;
  const int blk = (lane >> 4) & 1, q4 = (lane & 15) >> 2, p4 = lane & 3;
  lds_u32* sw = (lds_u32*)(smem + L_SYNC);
  gu64* sync = (gu64*)(ws + SYNC_OFF);
  const __amdgpu_buffer_rsrc_t wsr = __builtin_amdgcn_make_buffer_rsrc(ws, 0, SYNC_OFF, 0x00020000);
  const bool is_pf = idx < 12;
  if (wid == 0) STAMP(0);

  auto put_token = [&](u64 epoch) {
    if (wid == 3 && lane == 0) {
      const u64 e = epoch + 0x9E3779B97F4A7C15ull;
      sw[0] = (unsigned)e; sw[1] = (unsigned)(e >> 32);
      __hip_atomic_store(sw + 2, 1u, RLX_WG);
      if (is_pf) __hip_atomic_store(sync + (FP + xcd * 12 + idx) * FS, e, RLX_AGENT);
    }
  };
  if (wid == 3) {
    if (lane < 16) sw[lane] = ((lane == 3 || lane == 8) && is_pf) ? 4u : 0u;
  } else if (wid == 1 && idx < 2) {
    u64 pc;
    asm volatile("s_getpc_b64 %0" : "=s"(pc));
    set_m0(lb + L_DUMP);
    touch((const char*)pc, min(idx * 8192 + lane * 128, CODE_TOUCH_MAX));
  }
  int myflag = 0;

  if (is_pf) {
    if (wid == 0) {
      set_m0(lb + L_DUMP);
      const bool tg = xcd < 6;
      const unsigned ao = tg ? (lane >> 1) * (F_ * 4) + (lane & 1) * 128 : lane * (D_ * 2);
      const unsigned bo = tg ? (lane >> 3) * (F_ * 2) + (lane & 7) * 128 : lane * 128;
      const char* ab = tg ? (const char*)qw + (size_t)((xcd & 1) * 320) * (F_ * 4) : (const char*)x + (size_t)((xcd - 6) * 1024) * (D_ * 2);
      const char* bb = tg ? (const char*)V + (xcd >> 1) * 1024 : (const char*)U;
      const int na = tg ? 10 : 16, nt_ = tg ? 18 : 26, np = tg ? 24 : 10;
      const int aj = tg ? 32 * F_ * 4 : 64 * D_ * 2, ak = tg ? 256 : 128;
      const int bj = tg ? 8 * F_ * 2 : 8192, bk = tg ? 64 * F_ * 2 : 64 * D_ * 2;
      int p = 0, j = idx, n = 0;
      __builtin_amdgcn_s_waitcnt(0x0F70);
#pragma unroll 1
      while (p < np) {
        if (j < na) touch(ab + j * aj + p * ak, ao);
        else touch(bb + (j - na) * bj + p * bk, bo);
        j += 12;
        if (j >= nt_) { j -= nt_; ++p; }
        if (++n == 3) BAR();
      }
      STAMP(7);
    } else {
      WAIT_L(0);
      BAR();
      if (wid == 3) put_token(__hip_atomic_load(sync, RLX_AGENT));
    }
  } else if (xcd < 6) {
    const int t = idx - 12;
    const int mt = (xcd & 1) * 5 + t % 5, nt = (xcd >> 1) * 4 + t / 5;
    myflag = FT + nt * 10 + mt;
    if (wid >= 4) {
      const int w4 = wid - 4, rr = lane >> 4, c16 = lane & 15;
      unsigned ag[4], bg[4];
#pragma unroll
      for (int i = 0; i < 4; ++i) {
        int row = i * 4 + rr;
        ag[i] = row * (F_ * 4) + ((c16 ^ row) * 16) - i * 1024;
        bg[i] = row * (F_ * 2) + ((c16 ^ (rr << 2)) * 16) - i * 1024;
      }
      const char* ap = (const char*)qw + (size_t)(mt * 64 + w4 * 16) * (F_ * 4);
      const char* bp = (const char*)V + nt * 256 + (size_t)(w4 * 16) * (F_ * 2);
      unsigned so = lb + w4 * 4096;
      auto issue = [&](int p) {
        unsigned base = so + (p & 3) * 32768;
        set_m0(base);
        glds<0>(ap, ag[0]); glds<1024>(ap, ag[1]); glds<2048>(ap, ag[2]); glds<3072>(ap, ag[3]);
        set_m0(base + 16384);
        glds<0>(bp, bg[0]); glds<1024>(bp, bg[1]); glds<2048>(bp, bg[2]); glds<3072>(bp, bg[3]);
        ap += 256; bp += 64 * F_ * 2;
      };
      issue(0);
      issue(1);
      BAR();
#pragma unroll 1
      for (int p = 2; p < 24; ++p) {
        WAIT_V(8);
        BAR();
        issue(p);
      }
      WAIT_V(8);
      BAR();
      WAIT_V(0);
      BAR();
    } else {
      const int m = wid & 1, kl = wid >> 1;
      {
        const size_t o = (size_t)((mt * 2 + m) * F_ + kl * 768) * 2;
        lds_char* T = smem + 131072 + m * 12288 + kl * 3072;
        u32x4 s8[2], z8[2];
#pragma unroll
        for (int j = 0; j < 2; ++j) {
          int e = j ? 64 + (lane & 31) : lane;
          s8[j] = *(const u32x4*)((const char*)scales + o + e * 16);
          z8[j] = *(const u32x4*)((const char*)zeros + o + e * 16);
        }
        const u64 epoch = __hip_atomic_load(sync, RLX_AGENT);
#pragma unroll
        for (int j = 0; j < 2; ++j) {
          int e = j ? 64 + (lane & 31) : lane;
          f32x4 sa, sb, za, zb;
          sa[0] = bflo(s8[j][0]); sa[1] = bfhi(s8[j][0]); sa[2] = bflo(s8[j][1]); sa[3] = bfhi(s8[j][1]);
          sb[0] = bflo(s8[j][2]); sb[1] = bfhi(s8[j][2]); sb[2] = bflo(s8[j][3]); sb[3] = bfhi(s8[j][3]);
          za[0] = -bflo(z8[j][0]) * sa[0]; za[1] = -bfhi(z8[j][0]) * sa[1]; za[2] = -bflo(z8[j][1]) * sa[2]; za[3] = -bfhi(z8[j][1]) * sa[3];
          zb[0] = -bflo(z8[j][2]) * sb[0]; zb[1] = -bfhi(z8[j][2]) * sb[1]; zb[2] = -bflo(z8[j][3]) * sb[2]; zb[3] = -bfhi(z8[j][3]) * sb[3];
          ldsw<f32x4>(T + e * 32, sa); ldsw<f32x4>(T + e * 32 + 16, sb);
          ldsw<f32x4>(T + 6144 + e * 32, za); ldsw<f32x4>(T + 6144 + e * 32 + 16, zb);
        }
        put_token(epoch);
        WAIT_L(0);
        BAR();
      }
      int aoff[4], boff[4];
#pragma unroll
      for (int c = 0; c < 4; ++c) {
        int ch = 8 * kl + 4 * (c >> 1) + 2 * h + (c & 1);
        aoff[c] = (m * 32 + r) * 256 + ((ch ^ (r & 15)) * 16);
        boff[c] = 16384 + (32 * kl + 8 * h + q4) * 256 + ((c ^ (2 * kl) ^ q4) * 64) + 32 * (p4 & 1) + 16 * blk + 8 * (p4 >> 1);
      }
      lds_char* tp = smem + 131072 + m * 12288 + (32 * kl + 8 * h) * 4;
      f32x16 acc[4] = {};
      f32x4 ts[4], tz[4];
      u32x4 av[4];
      u32x4 af[2][2] = {}, bf[2][2][4] = {};
      auto rdtab = [&](int o, int c) {
        ts[c] = ldsr<f32x4>(tp + o + (c >> 1) * 64 + (c & 1) * 16);
        tz[c] = ldsr<f32x4>(tp + o + 6144 + (c >> 1) * 64 + (c & 1) * 16);
      };
      auto step = [&](int slot) {
        const int buf = slot & 1;
        lds_char* sl = smem + slot * 32768;
        BAR();
#pragma unroll
        for (int c = 0; c < 4; ++c) av[c] = ldsr<u32x4>(sl + aoff[c]);
        SB();
#pragma unroll
        for (int u = 0; u < 8; ++u) {
          acc[u & 3] = mfma32(af[buf ^ 1][u >> 2], bf[buf ^ 1][u >> 2][u & 3], acc[u & 3]);
          SB();
          if (u < 4) {
#pragma unroll
            for (int v = 2 * u; v < 2 * u + 2; ++v)
              bf[buf][v >> 2][v & 3] = trread2(sl + boff[v & 3] + (16 * (v >> 2)) * 256, sl + boff[v & 3] + (16 * (v >> 2) + 4) * 256);
          } else {
            const int c = u - 4;
            u32x2 o = deq4(av[c], ts[c], tz[c]);
            af[buf][c >> 1][(c & 1) * 2] = o[0]; af[buf][c >> 1][(c & 1) * 2 + 1] = o[1];
            SB();
            rdtab((slot + 1) * 256, c);
          }
          SB();
        }
      };
      if (wid == 0) STAMP(9);
#pragma unroll
      for (int c = 0; c < 4; ++c) rdtab(0, c);
#pragma unroll 1
      for (int p = 0; p < 24; p += 4) {
        step(0); step(1); step(2); step(3);
        tp += 1024;
      }
#pragma unroll
      for (int u = 0; u < 8; ++u) {
        const int c = (u & 1) + (u < 4 ? 2 : 0), s2 = (u >> 1) & 1;
        acc[c] = mfma32(af[1][s2], bf[1][s2][c], acc[c]);
      }
      if (wid == 0) STAMP(1);
      lds_char* RED = smem + m * 16384;
#pragma unroll
      for (int c = 0; c < 2; ++c)
#pragma unroll
        for (int j = 0; j < 4; ++j) {
          const f32x16& g = acc[2 + c];
          ldsw<f32x4>(RED + kl * 8192 + c * 4096 + j * 1024 + lane * 16, f32x4{g[4 * j], g[4 * j + 1], g[4 * j + 2], g[4 * j + 3]});
        }
      WAIT_L(0);
      if (lane == 0) __hip_atomic_fetch_add(sw + 6 + m, 1u, RLX_WG);
      lds_spin(sw + 6 + m, 2);
#pragma unroll
      for (int c = 0; c < 2; ++c) {
        f32x16 o = acc[c];
#pragma unroll
        for (int j = 0; j < 4; ++j) {
          f32x4 v = ldsr<f32x4>(RED + (kl ^ 1) * 8192 + c * 4096 + j * 1024 + lane * 16);
          o[4 * j] += v[0]; o[4 * j + 1] += v[1]; o[4 * j + 2] += v[2]; o[4 * j + 3] += v[3];
        }
        const int n32 = nt * 4 + kl * 2 + c;
        const unsigned oo = (((n32 / 3) * 40 + (mt * 2 + m) * 2) * 3 + n32 % 3) * 1024;
        __builtin_amdgcn_raw_buffer_store_b128(pack8(o, 0), wsr, lane * 16, oo, 16);
        __builtin_amdgcn_raw_buffer_store_b128(pack8(o, 1), wsr, lane * 16 + 3072, oo, 16);
      }
    }
  } else {
    const int t = idx - 12;
    const int xb_ = (xcd - 6) * 4 + (t & 3), eb = t >> 2;
    myflag = FX + xb_ * 5 + eb;
    if (wid >= 4) {
      const int w4 = wid - 4;
      unsigned xg[4], ug[4];
#pragma unroll
      for (int i = 0; i < 4; ++i) {
        int row = i * 8 + (lane >> 3);
        xg[i] = row * (D_ * 2) + (((lane & 7) ^ ((row >> 1) & 7)) * 16) - i * 1024;
        ug[i] = (i * 4 + (lane >> 4)) * (D_ * 2) + (((lane & 15) ^ ((lane >> 4) << 2)) * 16) - i * 1024;
      }
      const char* xp = (const char*)x + (size_t)(xb_ * 256 + w4 * 64) * (D_ * 2);
      const char* up = (const char*)U + eb * 256 + (size_t)(w4 * 16) * (D_ * 2);
      unsigned so = 0;
      auto issue = [&]() {
        set_m0(lb + so + 32768 + w4 * 4096);
        glds<0>(up, ug[0]); glds<1024>(up, ug[1]); glds<2048>(up, ug[2]); glds<3072>(up, ug[3]);
        set_m0(lb + so + w4 * 8192);
        glds<0>(xp, xg[0]); glds<1024>(xp, xg[1]); glds<2048>(xp, xg[2]); glds<3072>(xp, xg[3]);
        set_m0(lb + so + w4 * 8192 + 4096);
        const char* xq = xp + 32 * D_ * 2;
        glds<0>(xq, xg[0]); glds<1024>(xq, xg[1]); glds<2048>(xq, xg[2]); glds<3072>(xq, xg[3]);
        xp += 128; up += 64 * D_ * 2;
        so = so == 98304 ? 0 : so + 49152;
      };
      issue();
      WAIT_V(0);
      BAR();
      issue();
#pragma unroll 1
      for (int st = 2; st < 10; ++st) {
        issue();
        WAIT_V(12);
        BAR();
      }
      WAIT_V(0);
      BAR();
    } else {
      int xoff[4], uoff[4];
#pragma unroll
      for (int s = 0; s < 4; ++s) {
        xoff[s] = (wid * 64 + r) * 128 + (((2 * s + h) ^ ((r >> 1) & 7)) * 16);
        uoff[s] = 32768 + (8 * h + q4) * 256 + ((s ^ q4) * 64) + blk * 32 + p4 * 8;
      }
      f32x16 acc[2][4] = {};
      u32x4 xf[2][2], uf[2][4];
      auto rd = [&](lds_char* base, int s, int buf, int i) {
        if (i < 2) xf[buf][i] = ldsr<u32x4>(base + xoff[s] + i * 4096);
        else uf[buf][i - 2] = trread2(base + uoff[i - 2] + s * 4096, base + uoff[i - 2] + s * 4096 + 1024);
      };
      if (wid == 3) put_token(__hip_atomic_load(sync, RLX_AGENT));
      WAIT_L(0);
      BAR();
#pragma unroll
      for (int i = 0; i < 6; ++i) rd(smem, 0, 0, i);
      unsigned so = 0;
#pragma unroll 1
      for (int st = 0; st < 10; ++st) {
#pragma unroll
        for (int s = 0; s < 4; ++s) {
          if (s == 3) so = so == 98304 ? 0 : so + 49152;
          lds_char* base = smem + so;
#pragma unroll
          for (int u = 0; u < 8; ++u) {
            acc[u & 1][u >> 1] = mfma32(uf[s & 1][u >> 1], xf[s & 1][u & 1], acc[u & 1][u >> 1]);
            SB();
            if (u == 0 && s == 3 && st < 9) { WAIT_L(0); BAR(); }
            if (u < 6) rd(base, (s + 1) & 3, (s + 1) & 1, u);
            SB();
          }
        }
      }
#pragma unroll
      for (int u = 0; u < 8; ++u) {
        const unsigned o = XRF_OFF + ((xb_ * 8 + wid * 2 + (u & 1)) * 40 + (eb * 4 + (u >> 1)) * 2) * 1024;
        __builtin_amdgcn_raw_buffer_store_b128(pack8(acc[u & 1][u >> 1], 0), wsr, lane * 16, o, 16);
        __builtin_amdgcn_raw_buffer_store_b128(pack8(acc[u & 1][u >> 1], 1), wsr, lane * 16 + 1024, o, 16);
      }
      if (wid == 0) STAMP(3);
    }
  }

  const int mb = (xcd >> 1) * 4 + (idx & 3), nb = (xcd & 1) * 8 + (idx >> 2);
  const unsigned vo = lane * 16;
  const char* xsrc = ws + XRF_OFF + (mb * 4 + (wid & 3)) * 40960;
  auto ldx = [&](int ch) {
    asm volatile("s_mov_b32 m0, %2\n\ts_nop 0\n\tglobal_load_lds_dwordx4 %0, %1 sc1\n\tglobal_load_lds_dwordx4 %0, %1 offset:1024 sc1\n\t"
                 "global_load_lds_dwordx4 %0, %1 offset:2048 sc1\n\tglobal_load_lds_dwordx4 %0, %1 offset:3072 sc1"
                 ::"v"(vo), "s"(xsrc + ch * 4096), "s"(lb + (ch % 5) * 28672 + 12288 + (wid & 3) * 4096) : "memory");
  };
  if (wid < 4) {
    lds_spin(sw + 2, 1);
    const u64 tok = (u64)sw[0] | ((u64)sw[1] << 32);
    if (!is_pf) {
      if (wid == 0) STAMP(10);
      if (lane == 0) __hip_atomic_fetch_add(sw + 8, 1u, RLX_WG);
      asm volatile("s_waitcnt vmcnt(0) lgkmcnt(0)" ::: "memory");
      if (lane == 0) {
        if (__hip_atomic_fetch_add(sw + 3, 1u, RLX_WG) == 3) __hip_atomic_store(sync + myflag * FS, tok, RLX_AGENT);
      }
      if (wid == 0) STAMP(2);
    }
    if (wid == 0) {
      lds_spin(sw + 3, 4);
      const int n0 = (nb * 3) >> 2, n1 = (nb * 3 + 2) >> 2;
      poll_flags(sync + (FT + n0 * 10 + min(lane, (n1 - n0) * 10 + 9)) * FS, tok);
      if (lane == 0) __hip_atomic_store(sw + 5, 1u, RLX_WG);
      STAMP(4);
    } else {
      lds_spin(sw + 5, 1);
    }
    const char* tsrc = ws + nb * 122880 + wid * 3072;
    auto ldt = [&](int ch) {
      asm volatile("s_mov_b32 m0, %2\n\ts_nop 0\n\tglobal_load_lds_dwordx4 %0, %1 sc1\n\tglobal_load_lds_dwordx4 %0, %1 offset:1024 sc1\n\t"
                   "global_load_lds_dwordx4 %0, %1 offset:2048 sc1" ::"v"(vo), "s"(tsrc + ch * 12288), "s"(lb + (ch % 5) * 28672 + wid * 3072) : "memory");
    };
    ldt(0); ldt(1); ldt(2);
    WAIT_V(6); BAR(); ldt(3);
    WAIT_V(6); BAR(); ldt(4);
    WAIT_V(6); BAR(); ldt(5); ldx(5);
    WAIT_V(10); BAR(); ldt(6); ldx(6);
    WAIT_V(14); BAR(); ldt(7); ldx(7);
    WAIT_V(14); BAR(); ldt(8); ldx(8);
    WAIT_V(14); BAR(); ldt(9); ldx(9);
    WAIT_V(14); BAR();
    WAIT_V(7); BAR();
    WAIT_V(0); BAR();
    if (blockIdx.x == 0 && wid == 1) {
#pragma unroll 1
      for (int i = 0; i < 4; ++i) {
        poll_flags(sync + (1 + i * 64 + lane) * FS, tok);
      }
      if (lane == 0) __hip_atomic_store(sync, tok, RLX_AGENT);
    }
  } else {
    const int w4 = wid - 4;
    if (wid == 4) {
      lds_spin(sw + 2, 1);
      const u64 tok = (u64)sw[0] | ((u64)sw[1] << 32);
      poll_flags(sync + (FX + (mb >> 1) * 5 + min(lane, 4)) * FS, tok);
      lds_spin(sw + 8, 4);
      if (lane == 0) __hip_atomic_store(sw + 4, 1u, RLX_WG);
      STAMP(8);
    } else {
      lds_spin(sw + 4, 1);
    }
#pragma unroll
    for (int ch = 0; ch < 5; ++ch) ldx(ch);
    f32x16 acc[3] = {};
    u32x4 tf[3][3], xf[3];
    lds_char* tb = smem + lane * 16;
    lds_char* xb = smem + lane * 16 + 12288 + w4 * 4096;
    auto rdf = [&](int ks, int c) {
      const int o = ((ks >> 2) % 5) * 28672;
      if (c < 3) tf[ks % 3][c] = ldsr<u32x4>(tb + o + ((ks & 3) * 3 + c) * 1024);
      else xf[ks % 3] = ldsr<u32x4>(xb + o + (ks & 3) * 1024);
    };
    WAIT_V(16);
    BAR();
#pragma unroll
    for (int c = 0; c < 8; ++c) rdf(c >> 2, 3 - (c & 3));
#pragma unroll
    for (int ks = 0; ks < 40; ++ks) {
#pragma unroll
      for (int c = 0; c < 3; ++c) {
        acc[c] = mfma32(tf[ks % 3][c], xf[ks % 3], acc[c]);
        SB();
        if (ks < 38) {
          if (c == 0) {
            if ((ks & 3) == 2) {
              if (ks == 2) WAIT_V(12);
              if (ks == 6) WAIT_V(8);
              if (ks == 10) WAIT_V(4);
              if (ks == 14) WAIT_V(0);
              BAR(); asm volatile("" ::: "memory");
            }
            rdf(ks + 2, 3);
          }
          rdf(ks + 2, c);
        }
        SB();
      }
    }
    if (wid == 4) STAMP(5);
    lds_char* eb = smem + w4 * 6656;
#pragma unroll
    for (int c = 0; c < 3; ++c) {
      ldsw<u32x4>(eb + r * 208 + c * 64 + h * 32, pack8(acc[c], 0));
      ldsw<u32x4>(eb + r * 208 + c * 64 + h * 32 + 16, pack8(acc[c], 1));
    }
    const __amdgpu_buffer_rsrc_t yr = __builtin_amdgcn_make_buffer_rsrc((void*)y, 0, 2048 * F_ * 2, 0x00020000);
    const unsigned yo = ((mb * 128 + w4 * 32) * F_ + nb * 96) * 2;
#pragma unroll
    for (int i = 0; i < 6; ++i) {
      int q = i * 64 + lane, row = q / 12, chn = q % 12;
      __builtin_amdgcn_raw_buffer_store_b128(ldsr<u32x4>(eb + row * 208 + chn * 16), yr, row * (F_ * 2) + chn * 16, yo, Y_AUX);
    }
    WAIT_V(0);
    if (wid == 4) STAMP(6);
    if (wid == 4 && blockIdx.x == AMP_BLK) {
      WAIT_L(0);
      unsigned d = __builtin_amdgcn_readfirstlane(sw[16 + AMP] - sw[16]);
      unsigned t0 = (unsigned)__builtin_amdgcn_s_memrealtime();
      while ((unsigned)__builtin_amdgcn_s_memrealtime() - t0 < 3 * d) __builtin_amdgcn_s_sleep(1);
    }
  }
}

extern "C" void kernel_launch(void* const* d_in, const int* in_sizes, int n_in, void* d_out, int out_size, void* d_ws,
                              size_t ws_size, hipStream_t stream) {
  (void)hipFuncSetAttribute((const void*)quip, hipFuncAttributeMaxDynamicSharedMemorySize, LDS_BYTES);
  quip<<<256, 512, LDS_BYTES, stream>>>((const __hip_bfloat16*)d_in[0], (const int*)d_in[1], (const __hip_bfloat16*)d_in[2],
                                        (const __hip_bfloat16*)d_in[3], (const __hip_bfloat16*)d_in[4], (const __hip_bfloat16*)d_in[5],
                                        (char*)d_ws, (__hip_bfloat16*)d_out);
}
```

```cpp
#include <hip/hip_runtime.h>
#include <hip/hip_bf16.h>

typedef __attribute__((ext_vector_type(8))) __bf16 bf16x8;
typedef __attribute__((ext_vector_type(2))) __bf16 bf16x2;
typedef __attribute__((ext_vector_type(4))) short s16x4;
typedef __attribute__((ext_vector_type(16))) float f32x16;
typedef __attribute__((ext_vector_type(4))) float f32x4;
typedef __attribute__((ext_vector_type(4))) unsigned u32x4;
typedef __attribute__((ext_vector_type(2))) unsigned u32x2;
typedef __attribute__((address_space(3))) s16x4 lds_s16x4;
typedef __attribute__((address_space(3))) char lds_char;
typedef __attribute__((address_space(3))) unsigned lds_u32;
typedef __attribute__((address_space(1))) unsigned long long gu64;
typedef unsigned long long u64;

#define STAMP(i)
#define Y_AUX 2
#define XCD_PERM 0x54321706
#define POLL_SLEEP 4
constexpr int D_ = 640, F_ = 1536;

#define WAIT_V(n) asm volatile("s_waitcnt vmcnt(" #n ")" ::: "memory")
#define WAIT_L(n) asm volatile("s_waitcnt lgkmcnt(" #n ")" ::: "memory")
#define BAR() __builtin_amdgcn_s_barrier()
#define SB() __builtin_amdgcn_sched_barrier(0)
#define RLX_AGENT __ATOMIC_RELAXED, __HIP_MEMORY_SCOPE_AGENT
#define RLX_WG __ATOMIC_RELAXED, __HIP_MEMORY_SCOPE_WORKGROUP

__device__ __forceinline__ void set_m0(unsigned ldsaddr) { asm volatile("s_mov_b32 m0, %0\n\ts_nop 0" ::"s"(ldsaddr) : "memory"); }
template <int OFF> __device__ __forceinline__ void glds(const void* sbase, unsigned voff) {
  asm volatile("global_load_lds_dwordx4 %0, %1 offset:%2" ::"v"(voff), "s"(sbase), "i"(OFF) : "memory");
}
__device__ __forceinline__ void touch(const void* sbase, unsigned voff) {
  asm volatile("global_load_lds_dword %0, %1" ::"v"(voff), "s"(sbase) : "memory");
}
__device__ __forceinline__ unsigned pack2(float a, float b) {
  bf16x2 p; p[0] = (__bf16)a; p[1] = (__bf16)b;
  return __builtin_bit_cast(unsigned, p);
}
__device__ __forceinline__ float bflo(unsigned u) { return __builtin_bit_cast(float, u << 16); }
__device__ __forceinline__ float bfhi(unsigned u) { return __builtin_bit_cast(float, u & 0xffff0000u); }
__device__ __forceinline__ u32x4 trread2(lds_char* p0, lds_char* p1) {
  u32x2 lo = __builtin_bit_cast(u32x2, __builtin_amdgcn_ds_read_tr16_b64_v4i16((lds_s16x4*)p0));
  u32x2 hi = __builtin_bit_cast(u32x2, __builtin_amdgcn_ds_read_tr16_b64_v4i16((lds_s16x4*)p1));
  return u32x4{lo[0], lo[1], hi[0], hi[1]};
}
template <typename T> __device__ __forceinline__ T ldsr(lds_char* p) { return *(__attribute__((address_space(3))) T*)p; }
template <typename T> __device__ __forceinline__ void ldsw(lds_char* p, T v) { *(__attribute__((address_space(3))) T*)p = v; }
__device__ __forceinline__ f32x16 mfma32(u32x4 a, u32x4 b, f32x16 c) {
  return __builtin_amdgcn_mfma_f32_32x32x16_bf16(__builtin_bit_cast(bf16x8, a), __builtin_bit_cast(bf16x8, b), c, 0, 0, 0);
}
__device__ __forceinline__ u32x4 pack8(const f32x16& a, int s) {
  u32x4 r;
  r[0] = pack2(a[8 * s + 0], a[8 * s + 1]); r[1] = pack2(a[8 * s + 2], a[8 * s + 3]);
  r[2] = pack2(a[8 * s + 4], a[8 * s + 5]); r[3] = pack2(a[8 * s + 6], a[8 * s + 7]);
  return r;
}
__device__ __forceinline__ u32x2 deq4(u32x4 q, f32x4 s, f32x4 z) {
  float w0, w1, w2, w3;
  asm("v_cvt_f32_i32 %0, %4\n\tv_cvt_f32_i32 %1, %5\n\tv_cvt_f32_i32 %2, %6\n\tv_cvt_f32_i32 %3, %7\n\t"
      "v_fma_f32 %0, %0, %8, %12\n\tv_fma_f32 %1, %1, %9, %13\n\tv_fma_f32 %2, %2, %10, %14\n\tv_fma_f32 %3, %3, %11, %15\n\t"
      "v_cvt_pk_bf16_f32 %0, %0, %1\n\tv_cvt_pk_bf16_f32 %1, %2, %3"
      : "=&v"(w0), "=&v"(w1), "=&v"(w2), "=&v"(w3)
      : "v"(q[0]), "v"(q[1]), "v"(q[2]), "v"(q[3]), "v"(s[0]), "v"(s[1]), "v"(s[2]), "v"(s[3]), "v"(z[0]), "v"(z[1]), "v"(z[2]), "v"(z[3]));
  return u32x2{__builtin_bit_cast(unsigned, w0), __builtin_bit_cast(unsigned, w1)};
}
__device__ __forceinline__ void lds_spin(lds_u32* w, unsigned want) {
  while ((unsigned)__builtin_amdgcn_readfirstlane(__hip_atomic_load(w, RLX_WG)) < want) __builtin_amdgcn_s_sleep(1);
  asm volatile("" ::: "memory");
}
__device__ __forceinline__ void poll_flags(gu64* f, u64 tok) {
#pragma unroll 1
  for (unsigned spins = 0; spins < (1u << 20); ++spins) {
    if (__all(__hip_atomic_load(f, RLX_AGENT) == tok)) break;
    __builtin_amdgcn_s_sleep(POLL_SLEEP);
  }
  __builtin_amdgcn_fence(__ATOMIC_ACQUIRE, "wavefront");
}

constexpr int XRF_OFF = 2 << 20, SYNC_OFF = 6 << 20;
#define FS 16
constexpr int FT = 1, FX = 121;
constexpr int FA = 161 * FS;
constexpr int CODE_TOUCH_MAX = 15744;
constexpr int L_SYNC = 156160, L_DUMP = 156416, LDS_BYTES = 156672;

__global__ __launch_bounds__(512, 2) void quip(const __hip_bfloat16* __restrict__ x, const int* __restrict__ qw,
                                               const __hip_bfloat16* __restrict__ scales, const __hip_bfloat16* __restrict__ zeros,
                                               const __hip_bfloat16* __restrict__ U, const __hip_bfloat16* __restrict__ V,
                                               char* ws, __hip_bfloat16* __restrict__ y) {
  extern __shared__ __attribute__((aligned(16))) char smem_[];
  lds_char* smem = (lds_char*)smem_;
  const unsigned lb = (unsigned)(size_t)smem;
  const int wid = __builtin_amdgcn_readfirstlane(threadIdx.x >> 6), lane = threadIdx.x & 63;
  const int xcd = (XCD_PERM >> ((blockIdx.x & 7) * 4)) & 7, idx = blockIdx.x >> 3;
  const int h = lane >> 5, r = lane & 31;
  const int blk = (lane >> 4) & 1, q4 = (lane & 15) >> 2, p4 = lane & 3;
  lds_u32* sw = (lds_u32*)(smem + L_SYNC);
  gu64* sync = (gu64*)(ws + SYNC_OFF);
  const __amdgpu_buffer_rsrc_t wsr = __builtin_amdgcn_make_buffer_rsrc(ws, 0, SYNC_OFF, 0x00020000);
  const bool is_pf = idx < 12;
  if (wid == 0) STAMP(0);

  auto put_token = [&](u64 epoch) {
    if (wid == 3 && lane == 0) {
      const u64 e = epoch + 0x9E3779B97F4A7C15ull;
      sw[0] = (unsigned)e; sw[1] = (unsigned)(e >> 32);
      __hip_atomic_store(sw + 2, 1u, RLX_WG);
      __hip_atomic_store(sync + FA + blockIdx.x, e, RLX_AGENT);
    }
  };
  if (wid == 3) {
    if (lane < 16) sw[lane] = ((lane == 3 || lane == 8) && is_pf) ? 4u : 0u;
  } else if (wid == 1 && idx < 2) {
    u64 pc;
    asm volatile("s_getpc_b64 %0" : "=s"(pc));
    set_m0(lb + L_DUMP);
    touch((const char*)pc, min(idx * 8192 + lane * 128, CODE_TOUCH_MAX));
  }
  int myflag = 0;

  if (is_pf) {
    if (wid == 0) {
      set_m0(lb + L_DUMP);
      const bool tg = xcd < 6;
      const unsigned ao = tg ? (lane >> 1) * (F_ * 4) + (lane & 1) * 128 : lane * (D_ * 2);
      const unsigned bo = tg ? (lane >> 3) * (F_ * 2) + (lane & 7) * 128 : lane * 128;
      const char* ab = tg ? (const char*)qw + (size_t)((xcd & 1) * 320) * (F_ * 4) : (const char*)x + (size_t)((xcd - 6) * 1024) * (D_ * 2);
      const char* bb = tg ? (const char*)V + (xcd >> 1) * 1024 : (const char*)U;
      const int na = tg ? 10 : 16, nt_ = tg ? 18 : 26, np = tg ? 24 : 10;
      const int aj = tg ? 32 * F_ * 4 : 64 * D_ * 2, ak = tg ? 256 : 128;
      const int bj = tg ? 8 * F_ * 2 : 8192, bk = tg ? 64 * F_ * 2 : 64 * D_ * 2;
      int p = 0, j = idx, n = 0;
      __builtin_amdgcn_s_waitcnt(0x0F70);
#pragma unroll 1
      while (p < np) {
        if (j < na) touch(ab + j * aj + p * ak, ao);
        else touch(bb + (j - na) * bj + p * bk, bo);
        j += 12;
        if (j >= nt_) { j -= nt_; ++p; }
        if (++n == 3) BAR();
      }
      STAMP(7);
    } else {
      WAIT_L(0);
      BAR();
      if (wid == 3) put_token(__hip_atomic_load(sync, RLX_AGENT));
      if (blockIdx.x == 0 && wid == 2) {
        lds_spin(sw + 2, 1);
        const u64 tok = (u64)sw[0] | ((u64)sw[1] << 32);
#pragma unroll 1
        for (int i = 0; i < 4; ++i) poll_flags(sync + FA + i * 64 + lane, tok);
        if (lane == 0) __hip_atomic_store(sync, tok, RLX_AGENT);
      }
    }
  } else if (xcd < 6) {
    const int t = idx - 12;
    const int mt = (xcd & 1) * 5 + t % 5, nt = (xcd >> 1) * 4 + t / 5;
    myflag = FT + nt * 10 + mt;
    if (wid >= 4) {
      const int w4 = wid - 4, rr = lane >> 4, c16 = lane & 15;
      unsigned ag[4], bg[4];
#pragma unroll
      for (int i = 0; i < 4; ++i) {
        int row = i * 4 + rr;
        ag[i] = row * (F_ * 4) + ((c16 ^ row) * 16) - i * 1024;
        bg[i] = row * (F_ * 2) + ((c16 ^ (rr << 2)) * 16) - i * 1024;
      }
      const char* ap = (const char*)qw + (size_t)(mt * 64 + w4 * 16) * (F_ * 4);
      const char* bp = (const char*)V + nt * 256 + (size_t)(w4 * 16) * (F_ * 2);
      unsigned so = lb + w4 * 4096;
      auto issue = [&](int p) {
        unsigned base = so + (p & 3) * 32768;
        set_m0(base);
        glds<0>(ap, ag[0]); glds<1024>(ap, ag[1]); glds<2048>(ap, ag[2]); glds<3072>(ap, ag[3]);
        set_m0(base + 16384);
        glds<0>(bp, bg[0]); glds<1024>(bp, bg[1]); glds<2048>(bp, bg[2]); glds<3072>(bp, bg[3]);
        ap += 256; bp += 64 * F_ * 2;
      };
      issue(0);
      issue(1);
      BAR();
#pragma unroll 1
      for (int p = 2; p < 24; ++p) {
        WAIT_V(8);
        BAR();
        issue(p);
      }
      WAIT_V(8);
      BAR();
      WAIT_V(0);
      BAR();
    } else {
      const int m = wid & 1, kl = wid >> 1;
      {
        const size_t o = (size_t)((mt * 2 + m) * F_ + kl * 768) * 2;
        lds_char* T = smem + 131072 + m * 12288 + kl * 3072;
        u32x4 s8[2], z8[2];
#pragma unroll
        for (int j = 0; j < 2; ++j) {
          int e = j ? 64 + (lane & 31) : lane;
          s8[j] = *(const u32x4*)((const char*)scales + o + e * 16);
          z8[j] = *(const u32x4*)((const char*)zeros + o + e * 16);
        }
        const u64 epoch = __hip_atomic_load(sync, RLX_AGENT);
#pragma unroll
        for (int j = 0; j < 2; ++j) {
          int e = j ? 64 + (lane & 31) : lane;
          f32x4 sa, sb, za, zb;
          sa[0] = bflo(s8[j][0]); sa[1] = bfhi(s8[j][0]); sa[2] = bflo(s8[j][1]); sa[3] = bfhi(s8[j][1]);
          sb[0] = bflo(s8[j][2]); sb[1] = bfhi(s8[j][2]); sb[2] = bflo(s8[j][3]); sb[3] = bfhi(s8[j][3]);
          za[0] = -bflo(z8[j][0]) * sa[0]; za[1] = -bfhi(z8[j][0]) * sa[1]; za[2] = -bflo(z8[j][1]) * sa[2]; za[3] = -bfhi(z8[j][1]) * sa[3];
          zb[0] = -bflo(z8[j][2]) * sb[0]; zb[1] = -bfhi(z8[j][2]) * sb[1]; zb[2] = -bflo(z8[j][3]) * sb[2]; zb[3] = -bfhi(z8[j][3]) * sb[3];
          ldsw<f32x4>(T + e * 32, sa); ldsw<f32x4>(T + e * 32 + 16, sb);
          ldsw<f32x4>(T + 6144 + e * 32, za); ldsw<f32x4>(T + 6144 + e * 32 + 16, zb);
        }
        put_token(epoch);
        WAIT_L(0);
        BAR();
      }
      int aoff[4], boff[4];
#pragma unroll
      for (int c = 0; c < 4; ++c) {
        int ch = 8 * kl + 4 * (c >> 1) + 2 * h + (c & 1);
        aoff[c] = (m * 32 + r) * 256 + ((ch ^ (r & 15)) * 16);
        boff[c] = 16384 + (32 * kl + 8 * h + q4) * 256 + ((c ^ (2 * kl) ^ q4) * 64) + 32 * (p4 & 1) + 16 * blk + 8 * (p4 >> 1);
      }
      lds_char* tp = smem + 131072 + m * 12288 + (32 * kl + 8 * h) * 4;
      f32x16 acc[4] = {};
      f32x4 ts[4], tz[4];
      u32x4 av[4];
      u32x4 af[2][2] = {}, bf[2][2][4] = {};
      auto rdtab = [&](int o, int c) {
        ts[c] = ldsr<f32x4>(tp + o + (c >> 1) * 64 + (c & 1) * 16);
        tz[c] = ldsr<f32x4>(tp + o + 6144 + (c >> 1) * 64 + (c & 1) * 16);
      };
      auto step = [&](int slot) {
        const int buf = slot & 1;
        lds_char* sl = smem + slot * 32768;
        BAR();
#pragma unroll
        for (int c = 0; c < 4; ++c) av[c] = ldsr<u32x4>(sl + aoff[c]);
        SB();
#pragma unroll
        for (int u = 0; u < 8; ++u) {
          acc[u & 3] = mfma32(af[buf ^ 1][u >> 2], bf[buf ^ 1][u >> 2][u & 3], acc[u & 3]);
          SB();
          if (u < 4) {
#pragma unroll
            for (int v = 2 * u; v < 2 * u + 2; ++v)
              bf[buf][v >> 2][v & 3] = trread2(sl + boff[v & 3] + (16 * (v >> 2)) * 256, sl + boff[v & 3] + (16 * (v >> 2) + 4) * 256);
          } else {
            const int c = u - 4;
            u32x2 o = deq4(av[c], ts[c], tz[c]);
            af[buf][c >> 1][(c & 1) * 2] = o[0]; af[buf][c >> 1][(c & 1) * 2 + 1] = o[1];
            SB();
            rdtab((slot + 1) * 256, c);
          }
          SB();
        }
      };
#pragma unroll
      for (int c = 0; c < 4; ++c) rdtab(0, c);
#pragma unroll 1
      for (int p = 0; p < 24; p += 4) {
        step(0); step(1); step(2); step(3);
        tp += 1024;
      }
#pragma unroll
      for (int u = 0; u < 8; ++u) {
        const int c = (u & 1) + (u < 4 ? 2 : 0), s2 = (u >> 1) & 1;
        acc[c] = mfma32(af[1][s2], bf[1][s2][c], acc[c]);
      }
      if (wid == 0) STAMP(1);
      lds_char* RED = smem + m * 16384;
#pragma unroll
      for (int c = 0; c < 2; ++c)
#pragma unroll
        for (int j = 0; j < 4; ++j) {
          const f32x16& g = acc[2 + c];
          ldsw<f32x4>(RED + kl * 8192 + c * 4096 + j * 1024 + lane * 16, f32x4{g[4 * j], g[4 * j + 1], g[4 * j + 2], g[4 * j + 3]});
        }
      WAIT_L(0);
      if (lane == 0) __hip_atomic_fetch_add(sw + 6 + m, 1u, RLX_WG);
      lds_spin(sw + 6 + m, 2);
#pragma unroll
      for (int c = 0; c < 2; ++c) {
        f32x16 o = acc[c];
#pragma unroll
        for (int j = 0; j < 4; ++j) {
          f32x4 v = ldsr<f32x4>(RED + (kl ^ 1) * 8192 + c * 4096 + j * 1024 + lane * 16);
          o[4 * j] += v[0]; o[4 * j + 1] += v[1]; o[4 * j + 2] += v[2]; o[4 * j + 3] += v[3];
        }
        const int n32 = nt * 4 + kl * 2 + c;
        const unsigned oo = (((n32 / 3) * 40 + (mt * 2 + m) * 2) * 3 + n32 % 3) * 1024;
        __builtin_amdgcn_raw_buffer_store_b128(pack8(o, 0), wsr, lane * 16, oo, 16);
        __builtin_amdgcn_raw_buffer_store_b128(pack8(o, 1), wsr, lane * 16 + 3072, oo, 16);
      }
    }
  } else {
    const int t = idx - 12;
    const int xb_ = (xcd - 6) * 4 + (t & 3), eb = t >> 2;
    myflag = FX + xb_ * 5 + eb;
    if (wid >= 4) {
      const int w4 = wid - 4;
      unsigned xg[4], ug[4];
#pragma unroll
      for (int i = 0; i < 4; ++i) {
        int row = i * 8 + (lane >> 3);
        xg[i] = row * (D_ * 2) + (((lane & 7) ^ ((row >> 1) & 7)) * 16) - i * 1024;
        ug[i] = (i * 4 + (lane >> 4)) * (D_ * 2) + (((lane & 15) ^ ((lane >> 4) << 2)) * 16) - i * 1024;
      }
      const char* xp = (const char*)x + (size_t)(xb_ * 256 + w4 * 64) * (D_ * 2);
      const char* up = (const char*)U + eb * 256 + (size_t)(w4 * 16) * (D_ * 2);
      unsigned so = 0;
      auto issue = [&]() {
        set_m0(lb + so + 32768 + w4 * 4096);
        glds<0>(up, ug[0]); glds<1024>(up, ug[1]); glds<2048>(up, ug[2]); glds<3072>(up, ug[3]);
        set_m0(lb + so + w4 * 8192);
        glds<0>(xp, xg[0]); glds<1024>(xp, xg[1]); glds<2048>(xp, xg[2]); glds<3072>(xp, xg[3]);
        set_m0(lb + so + w4 * 8192 + 4096);
        const char* xq = xp + 32 * D_ * 2;
        glds<0>(xq, xg[0]); glds<1024>(xq, xg[1]); glds<2048>(xq, xg[2]); glds<3072>(xq, xg[3]);
        xp += 128; up += 64 * D_ * 2;
        so = so == 98304 ? 0 : so + 49152;
      };
      issue();
      WAIT_V(0);
      BAR();
      issue();
#pragma unroll 1
      for (int st = 2; st < 10; ++st) {
        issue();
        WAIT_V(12);
        BAR();
      }
      WAIT_V(0);
      BAR();
    } else {
      int xoff[4], uoff[4];
#pragma unroll
      for (int s = 0; s < 4; ++s) {
        xoff[s] = (wid * 64 + r) * 128 + (((2 * s + h) ^ ((r >> 1) & 7)) * 16);
        uoff[s] = 32768 + (8 * h + q4) * 256 + ((s ^ q4) * 64) + blk * 32 + p4 * 8;
      }
      f32x16 acc[2][4] = {};
      u32x4 xf[2][2], uf[2][4];
      auto rd = [&](lds_char* base, int s, int buf, int i) {
        if (i < 2) xf[buf][i] = ldsr<u32x4>(base + xoff[s] + i * 4096);
        else uf[buf][i - 2] = trread2(base + uoff[i - 2] + s * 4096, base + uoff[i - 2] + s * 4096 + 1024);
      };
      if (wid == 3) put_token(__hip_atomic_load(sync, RLX_AGENT));
      WAIT_L(0);
      BAR();
#pragma unroll
      for (int i = 0; i < 6; ++i) rd(smem, 0, 0, i);
      unsigned so = 0;
#pragma unroll 1
      for (int st = 0; st < 10; ++st) {
#pragma unroll
        for (int s = 0; s < 4; ++s) {
          if (s == 3) so = so == 98304 ? 0 : so + 49152;
          lds_char* base = smem + so;
#pragma unroll
          for (int u = 0; u < 8; ++u) {
            acc[u & 1][u >> 1] = mfma32(uf[s & 1][u >> 1], xf[s & 1][u & 1], acc[u & 1][u >> 1]);
            SB();
            if (u == 0 && s == 3 && st < 9) { WAIT_L(0); BAR(); }
            if (u < 6) rd(base, (s + 1) & 3, (s + 1) & 1, u);
            SB();
          }
        }
      }
#pragma unroll
      for (int u = 0; u < 8; ++u) {
        const unsigned o = XRF_OFF + ((xb_ * 8 + wid * 2 + (u & 1)) * 40 + (eb * 4 + (u >> 1)) * 2) * 1024;
        __builtin_amdgcn_raw_buffer_store_b128(pack8(acc[u & 1][u >> 1], 0), wsr, lane * 16, o, 16);
        __builtin_amdgcn_raw_buffer_store_b128(pack8(acc[u & 1][u >> 1], 1), wsr, lane * 16 + 1024, o, 16);
      }
      if (wid == 0) STAMP(3);
    }
  }

  const int mb = (xcd >> 1) * 4 + (idx & 3), nb = (xcd & 1) * 8 + (idx >> 2);
  const unsigned vo = lane * 16;
  const char* xsrc = ws + XRF_OFF + (mb * 4 + (wid & 3)) * 40960;
  auto ldx = [&](int ch) {
    asm volatile("s_mov_b32 m0, %2\n\ts_nop 0\n\tglobal_load_lds_dwordx4 %0, %1 sc1\n\tglobal_load_lds_dwordx4 %0, %1 offset:1024 sc1\n\t"
                 "global_load_lds_dwordx4 %0, %1 offset:2048 sc1\n\tglobal_load_lds_dwordx4 %0, %1 offset:3072 sc1"
                 ::"v"(vo), "s"(xsrc + ch * 4096), "s"(lb + (ch % 5) * 28672 + 12288 + (wid & 3) * 4096) : "memory");
  };
  if (wid < 4) {
    lds_spin(sw + 2, 1);
    const u64 tok = (u64)sw[0] | ((u64)sw[1] << 32);
    if (!is_pf) {
      if (wid == 0) STAMP(10);
      if (lane == 0) __hip_atomic_fetch_add(sw + 8, 1u, RLX_WG);
      asm volatile("s_waitcnt vmcnt(0) lgkmcnt(0)" ::: "memory");
      if (lane == 0) {
        if (__hip_atomic_fetch_add(sw + 3, 1u, RLX_WG) == 3) __hip_atomic_store(sync + myflag * FS, tok, RLX_AGENT);
      }
      if (wid == 0) STAMP(2);
    }
    if (wid == 0) {
      lds_spin(sw + 3, 4);
      const int n0 = (nb * 3) >> 2, n1 = (nb * 3 + 2) >> 2;
      poll_flags(sync + (FT + n0 * 10 + min(lane, (n1 - n0) * 10 + 9)) * FS, tok);
      if (lane == 0) __hip_atomic_store(sw + 5, 1u, RLX_WG);
      STAMP(4);
    } else {
      lds_spin(sw + 5, 1);
    }
    const char* tsrc = ws + nb * 122880 + wid * 3072;
    auto ldt = [&](int ch) {
      asm volatile("s_mov_b32 m0, %2\n\ts_nop 0\n\tglobal_load_lds_dwordx4 %0, %1 sc1\n\tglobal_load_lds_dwordx4 %0, %1 offset:1024 sc1\n\t"
                   "global_load_lds_dwordx4 %0, %1 offset:2048 sc1" ::"v"(vo), "s"(tsrc + ch * 12288), "s"(lb + (ch % 5) * 28672 + wid * 3072) : "memory");
    };
    ldt(0); ldt(1); ldt(2);
    WAIT_V(6); BAR(); ldt(3);
    WAIT_V(6); BAR(); ldt(4);
    WAIT_V(6); BAR(); ldt(5); ldx(5);
    WAIT_V(10); BAR(); ldt(6); ldx(6);
    WAIT_V(14); BAR(); ldt(7); ldx(7);
    WAIT_V(14); BAR(); ldt(8); ldx(8);
    WAIT_V(14); BAR(); ldt(9); ldx(9);
    WAIT_V(14); BAR();
    WAIT_V(7); BAR();
    WAIT_V(0); BAR();
  } else {
    const int w4 = wid - 4;
    if (wid == 4) {
      lds_spin(sw + 2, 1);
      const u64 tok = (u64)sw[0] | ((u64)sw[1] << 32);
      poll_flags(sync + (FX + (mb >> 1) * 5 + min(lane, 4)) * FS, tok);
      lds_spin(sw + 8, 4);
      if (lane == 0) __hip_atomic_store(sw + 4, 1u, RLX_WG);
      STAMP(8);
    } else {
      lds_spin(sw + 4, 1);
    }
#pragma unroll
    for (int ch = 0; ch < 5; ++ch) ldx(ch);
    f32x16 acc[3] = {};
    u32x4 tf[3][3], xf[3];
    lds_char* tb = smem + lane * 16;
    lds_char* xb = smem + lane * 16 + 12288 + w4 * 4096;
    auto rdf = [&](int ks, int c) {
      const int o = ((ks >> 2) % 5) * 28672;
      if (c < 3) tf[ks % 3][c] = ldsr<u32x4>(tb + o + ((ks & 3) * 3 + c) * 1024);
      else xf[ks % 3] = ldsr<u32x4>(xb + o + (ks & 3) * 1024);
    };
    WAIT_V(16);
    BAR();
#pragma unroll
    for (int c = 0; c < 8; ++c) rdf(c >> 2, 3 - (c & 3));
#pragma unroll
    for (int ks = 0; ks < 40; ++ks) {
#pragma unroll
      for (int c = 0; c < 3; ++c) {
        acc[c] = mfma32(tf[ks % 3][c], xf[ks % 3], acc[c]);
        SB();
        if (ks < 38) {
          if (c == 0) {
            if ((ks & 3) == 2) {
              if (ks == 2) WAIT_V(12);
              if (ks == 6) WAIT_V(8);
              if (ks == 10) WAIT_V(4);
              if (ks == 14) WAIT_V(0);
              BAR(); asm volatile("" ::: "memory");
            }
            rdf(ks + 2, 3);
          }
          rdf(ks + 2, c);
        }
        SB();
      }
    }
    if (wid == 4) STAMP(5);
    lds_char* eb = smem + w4 * 6656;
#pragma unroll
    for (int c = 0; c < 3; ++c) {
      ldsw<u32x4>(eb + r * 208 + c * 64 + h * 32, pack8(acc[c], 0));
      ldsw<u32x4>(eb + r * 208 + c * 64 + h * 32 + 16, pack8(acc[c], 1));
    }
    const __amdgpu_buffer_rsrc_t yr = __builtin_amdgcn_make_buffer_rsrc((void*)y, 0, 2048 * F_ * 2, 0x00020000);
    const unsigned yo = ((mb * 128 + w4 * 32) * F_ + nb * 96) * 2;
#pragma unroll
    for (int i = 0; i < 6; ++i) {
      int q = i * 64 + lane, row = q / 12, chn = q % 12;
      __builtin_amdgcn_raw_buffer_store_b128(ldsr<u32x4>(eb + row * 208 + chn * 16), yr, row * (F_ * 2) + chn * 16, yo, Y_AUX);
    }
  }
}

extern "C" void kernel_launch(void* const* d_in, const int* in_sizes, int n_in, void* d_out, int out_size, void* d_ws,
                              size_t ws_size, hipStream_t stream) {
  (void)hipFuncSetAttribute((const void*)quip, hipFuncAttributeMaxDynamicSharedMemorySize, LDS_BYTES);
  quip<<<256, 512, LDS_BYTES, stream>>>((const __hip_bfloat16*)d_in[0], (const int*)d_in[1], (const __hip_bfloat16*)d_in[2],
                                        (const __hip_bfloat16*)d_in[3], (const __hip_bfloat16*)d_in[4], (const __hip_bfloat16*)d_in[5],
                                        (char*)d_ws, (__hip_bfloat16*)d_out);
}
```

```cpp
#define AMP 0
#define AMP_BLK 121
#define AMPF 1
#include <hip/hip_runtime.h>
#include <hip/hip_bf16.h>

typedef __attribute__((ext_vector_type(8))) __bf16 bf16x8;
typedef __attribute__((ext_vector_type(2))) __bf16 bf16x2;
typedef __attribute__((ext_vector_type(4))) short s16x4;
typedef __attribute__((ext_vector_type(16))) float f32x16;
typedef __attribute__((ext_vector_type(4))) float f32x4;
typedef __attribute__((ext_vector_type(4))) unsigned u32x4;
typedef __attribute__((ext_vector_type(2))) unsigned u32x2;
typedef __attribute__((address_space(3))) s16x4 lds_s16x4;
typedef __attribute__((address_space(3))) char lds_char;
typedef __attribute__((address_space(3))) unsigned lds_u32;
typedef __attribute__((address_space(1))) unsigned long long gu64;
typedef unsigned long long u64;

#define STAMP(i) do { if ((threadIdx.x & 63) == 0) sw[16 + (i)] = (unsigned)__builtin_amdgcn_s_memrealtime(); } while (0)
#define Y_AUX 2
#define XCD_PERM 0x54321706
#define POLL_SLEEP 4
constexpr int D_ = 640, F_ = 1536;

#define WAIT_V(n) asm volatile("s_waitcnt vmcnt(" #n ")" ::: "memory")
#define WAIT_L(n) asm volatile("s_waitcnt lgkmcnt(" #n ")" ::: "memory")
#define BAR() __builtin_amdgcn_s_barrier()
#define SB() __builtin_amdgcn_sched_barrier(0)
#define RLX_AGENT __ATOMIC_RELAXED, __HIP_MEMORY_SCOPE_AGENT
#define RLX_WG __ATOMIC_RELAXED, __HIP_MEMORY_SCOPE_WORKGROUP

__device__ __forceinline__ void set_m0(unsigned ldsaddr) { asm volatile("s_mov_b32 m0, %0\n\ts_nop 0" ::"s"(ldsaddr) : "memory"); }
template <int OFF> __device__ __forceinline__ void glds(const void* sbase, unsigned voff) {
  asm volatile("global_load_lds_dwordx4 %0, %1 offset:%2" ::"v"(voff), "s"(sbase), "i"(OFF) : "memory");
}
__device__ __forceinline__ void touch(const void* sbase, unsigned voff) {
  asm volatile("global_load_lds_dword %0, %1" ::"v"(voff), "s"(sbase) : "memory");
}
__device__ __forceinline__ unsigned pack2(float a, float b) {
  bf16x2 p; p[0] = (__bf16)a; p[1] = (__bf16)b;
  return __builtin_bit_cast(unsigned, p);
}
__device__ __forceinline__ float bflo(unsigned u) { return __builtin_bit_cast(float, u << 16); }
__device__ __forceinline__ float bfhi(unsigned u) { return __builtin_bit_cast(float, u & 0xffff0000u); }
__device__ __forceinline__ u32x4 trread2(lds_char* p0, lds_char* p1) {
  u32x2 lo = __builtin_bit_cast(u32x2, __builtin_amdgcn_ds_read_tr16_b64_v4i16((lds_s16x4*)p0));
  u32x2 hi = __builtin_bit_cast(u32x2, __builtin_amdgcn_ds_read_tr16_b64_v4i16((lds_s16x4*)p1));
  return u32x4{lo[0], lo[1], hi[0], hi[1]};
}
template <typename T> __device__ __forceinline__ T ldsr(lds_char* p) { return *(__attribute__((address_space(3))) T*)p; }
template <typename T> __device__ __forceinline__ void ldsw(lds_char* p, T v) { *(__attribute__((address_space(3))) T*)p = v; }
__device__ __forceinline__ f32x16 mfma32(u32x4 a, u32x4 b, f32x16 c) {
  return __builtin_amdgcn_mfma_f32_32x32x16_bf16(__builtin_bit_cast(bf16x8, a), __builtin_bit_cast(bf16x8, b), c, 0, 0, 0);
}
__device__ __forceinline__ u32x4 pack8(const f32x16& a, int s) {
  u32x4 r;
  r[0] = pack2(a[8 * s + 0], a[8 * s + 1]); r[1] = pack2(a[8 * s + 2], a[8 * s + 3]);
  r[2] = pack2(a[8 * s + 4], a[8 * s + 5]); r[3] = pack2(a[8 * s + 6], a[8 * s + 7]);
  return r;
}
__device__ __forceinline__ u32x2 deq4(u32x4 q, f32x4 s, f32x4 z) {
  float w0, w1, w2, w3;
  asm("v_cvt_f32_i32 %0, %4\n\tv_cvt_f32_i32 %1, %5\n\tv_cvt_f32_i32 %2, %6\n\tv_cvt_f32_i32 %3, %7\n\t"
      "v_fma_f32 %0, %0, %8, %12\n\tv_fma_f32 %1, %1, %9, %13\n\tv_fma_f32 %2, %2, %10, %14\n\tv_fma_f32 %3, %3, %11, %15\n\t"
      "v_cvt_pk_bf16_f32 %0, %0, %1\n\tv_cvt_pk_bf16_f32 %1, %2, %3"
      : "=&v"(w0), "=&v"(w1), "=&v"(w2), "=&v"(w3)
      : "v"(q[0]), "v"(q[1]), "v"(q[2]), "v"(q[3]), "v"(s[0]), "v"(s[1]), "v"(s[2]), "v"(s[3]), "v"(z[0]), "v"(z[1]), "v"(z[2]), "v"(z[3]));
  return u32x2{__builtin_bit_cast(unsigned, w0), __builtin_bit_cast(unsigned, w1)};
}
__device__ __forceinline__ void lds_spin(lds_u32* w, unsigned want) {
  while ((unsigned)__builtin_amdgcn_readfirstlane(__hip_atomic_load(w, RLX_WG)) < want) __builtin_amdgcn_s_sleep(1);
  asm volatile("" ::: "memory");
}
__device__ __forceinline__ void poll_flags(gu64* f, u64 tok) {
#pragma unroll 1
  for (unsigned spins = 0; spins < (1u << 20); ++spins) {
    if (__all(__hip_atomic_load(f, RLX_AGENT) == tok)) break;
    __builtin_amdgcn_s_sleep(POLL_SLEEP);
  }
  __builtin_amdgcn_fence(__ATOMIC_ACQUIRE, "wavefront");
}

constexpr int XRF_OFF = 2 << 20, SYNC_OFF = 6 << 20;
#define FS 16
constexpr int FT = 1, FX = 121;
constexpr int FA = 161 * FS;
constexpr int CODE_TOUCH_MAX = 15744;
constexpr int L_SYNC = 156160, L_DUMP = 156416, LDS_BYTES = 156672;

__global__ __launch_bounds__(512, 2) void quip(const __hip_bfloat16* __restrict__ x, const int* __restrict__ qw,
                                               const __hip_bfloat16* __restrict__ scales, const __hip_bfloat16* __restrict__ zeros,
                                               const __hip_bfloat16* __restrict__ U, const __hip_bfloat16* __restrict__ V,
                                               char* ws, __hip_bfloat16* __restrict__ y) {
  extern __shared__ __attribute__((aligned(16))) char smem_[];
  lds_char* smem = (lds_char*)smem_;
  const unsigned lb = (unsigned)(size_t)smem;
  const int wid = __builtin_amdgcn_readfirstlane(threadIdx.x >> 6), lane = threadIdx.x & 63;
  const int xcd = (XCD_PERM >> ((blockIdx.x & 7) * 4)) & 7, idx = blockIdx.x >> 3;
  const int h = lane >> 5, r = lane & 31;
  const int blk = (lane >> 4) & 1, q4 = (lane & 15) >> 2, p4 = lane & 3;
  lds_u32* sw = (lds_u32*)(smem + L_SYNC);
  gu64* sync = (gu64*)(ws + SYNC_OFF);
  const __amdgpu_buffer_rsrc_t wsr = __builtin_amdgcn_make_buffer_rsrc(ws, 0, SYNC_OFF, 0x00020000);
  const bool is_pf = idx < 12;
  if (wid == 0) STAMP(0);

  auto put_token = [&](u64 epoch) {
    if (wid == 3 && lane == 0) {
      const u64 e = epoch + 0x9E3779B97F4A7C15ull;
      sw[0] = (unsigned)e; sw[1] = (unsigned)(e >> 32);
      __hip_atomic_store(sw + 2, 1u, RLX_WG);
      __hip_atomic_store(sync + FA + blockIdx.x, e, RLX_AGENT);
    }
  };
  if (wid == 3) {
    if (lane < 16) sw[lane] = ((lane == 3 || lane == 8) && is_pf) ? 4u : 0u;
  } else if (wid == 1 && idx < 2) {
    u64 pc;
    asm volatile("s_getpc_b64 %0" : "=s"(pc));
    set_m0(lb + L_DUMP);
    touch((const char*)pc, min(idx * 8192 + lane * 128, CODE_TOUCH_MAX));
  }
  int myflag = 0;

  if (is_pf) {
    if (wid == 0) {
      set_m0(lb + L_DUMP);
      const bool tg = xcd < 6;
      const unsigned ao = tg ? (lane >> 1) * (F_ * 4) + (lane & 1) * 128 : lane * (D_ * 2);
      const unsigned bo = tg ? (lane >> 3) * (F_ * 2) + (lane & 7) * 128 : lane * 128;
      const char* ab = tg ? (const char*)qw + (size_t)((xcd & 1) * 320) * (F_ * 4) : (const char*)x + (size_t)((xcd - 6) * 1024) * (D_ * 2);
      const char* bb = tg ? (const char*)V + (xcd >> 1) * 1024 : (const char*)U;
      const int na = tg ? 10 : 16, nt_ = tg ? 18 : 26, np = tg ? 24 : 10;
      const int aj = tg ? 32 * F_ * 4 : 64 * D_ * 2, ak = tg ? 256 : 128;
      const int bj = tg ? 8 * F_ * 2 : 8192, bk = tg ? 64 * F_ * 2 : 64 * D_ * 2;
      int p = 0, j = idx, n = 0;
      __builtin_amdgcn_s_waitcnt(0x0F70);
#pragma unroll 1
      while (p < np) {
        if (j < na) touch(ab + j * aj + p * ak, ao);
        else touch(bb + (j - na) * bj + p * bk, bo);
        j += 12;
        if (j >= nt_) { j -= nt_; ++p; }
        if (++n == 3) BAR();
      }
      STAMP(7);
    } else {
      WAIT_L(0);
      BAR();
      if (wid == 3) put_token(__hip_atomic_load(sync, RLX_AGENT));
      if (blockIdx.x == 0 && wid == 2) {
        lds_spin(sw + 2, 1);
        const u64 tok = (u64)sw[0] | ((u64)sw[1] << 32);
#pragma unroll 1
        for (int i = 0; i < 4; ++i) poll_flags(sync + FA + i * 64 + lane, tok);
        if (lane == 0) __hip_atomic_store(sync, tok, RLX_AGENT);
      }
    }
  } else if (xcd < 6) {
    const int t = idx - 12;
    const int mt = (xcd & 1) * 5 + t % 5, nt = (xcd >> 1) * 4 + t / 5;
    myflag = FT + nt * 10 + mt;
    if (wid >= 4) {
      const int w4 = wid - 4, rr = lane >> 4, c16 = lane & 15;
      unsigned ag[4], bg[4];
#pragma unroll
      for (int i = 0; i < 4; ++i) {
        int row = i * 4 + rr;
        ag[i] = row * (F_ * 4) + ((c16 ^ row) * 16) - i * 1024;
        bg[i] = row * (F_ * 2) + ((c16 ^ (rr << 2)) * 16) - i * 1024;
      }
      const char* ap = (const char*)qw + (size_t)(mt * 64 + w4 * 16) * (F_ * 4);
      const char* bp = (const char*)V + nt * 256 + (size_t)(w4 * 16) * (F_ * 2);
      unsigned so = lb + w4 * 4096;
      auto issue = [&](int p) {
        unsigned base = so + (p & 3) * 32768;
        set_m0(base);
        glds<0>(ap, ag[0]); glds<1024>(ap, ag[1]); glds<2048>(ap, ag[2]); glds<3072>(ap, ag[3]);
        set_m0(base + 16384);
        glds<0>(bp, bg[0]); glds<1024>(bp, bg[1]); glds<2048>(bp, bg[2]); glds<3072>(bp, bg[3]);
        ap += 256; bp += 64 * F_ * 2;
      };
      issue(0);
      issue(1);
      BAR();
#pragma unroll 1
      for (int p = 2; p < 24; ++p) {
        WAIT_V(8);
        BAR();
        issue(p);
      }
      WAIT_V(8);
      BAR();
      WAIT_V(0);
      BAR();
    } else {
      const int m = wid & 1, kl = wid >> 1;
      {
        const size_t o = (size_t)((mt * 2 + m) * F_ + kl * 768) * 2;
        lds_char* T = smem + 131072 + m * 12288 + kl * 3072;
        u32x4 s8[2], z8[2];
#pragma unroll
        for (int j = 0; j < 2; ++j) {
          int e = j ? 64 + (lane & 31) : lane;
          s8[j] = *(const u32x4*)((const char*)scales + o + e * 16);
          z8[j] = *(const u32x4*)((const char*)zeros + o + e * 16);
        }
        const u64 epoch = __hip_atomic_load(sync, RLX_AGENT);
#pragma unroll
        for (int j = 0; j < 2; ++j) {
          int e = j ? 64 + (lane & 31) : lane;
          f32x4 sa, sb, za, zb;
          sa[0] = bflo(s8[j][0]); sa[1] = bfhi(s8[j][0]); sa[2] = bflo(s8[j][1]); sa[3] = bfhi(s8[j][1]);
          sb[0] = bflo(s8[j][2]); sb[1] = bfhi(s8[j][2]); sb[2] = bflo(s8[j][3]); sb[3] = bfhi(s8[j][3]);
          za[0] = -bflo(z8[j][0]) * sa[0]; za[1] = -bfhi(z8[j][0]) * sa[1]; za[2] = -bflo(z8[j][1]) * sa[2]; za[3] = -bfhi(z8[j][1]) * sa[3];
          zb[0] = -bflo(z8[j][2]) * sb[0]; zb[1] = -bfhi(z8[j][2]) * sb[1]; zb[2] = -bflo(z8[j][3]) * sb[2]; zb[3] = -bfhi(z8[j][3]) * sb[3];
          ldsw<f32x4>(T + e * 32, sa); ldsw<f32x4>(T + e * 32 + 16, sb);
          ldsw<f32x4>(T + 6144 + e * 32, za); ldsw<f32x4>(T + 6144 + e * 32 + 16, zb);
        }
        put_token(epoch);
        WAIT_L(0);
        BAR();
      }
      int aoff[4], boff[4];
#pragma unroll
      for (int c = 0; c < 4; ++c) {
        int ch = 8 * kl + 4 * (c >> 1) + 2 * h + (c & 1);
        aoff[c] = (m * 32 + r) * 256 + ((ch ^ (r & 15)) * 16);
        boff[c] = 16384 + (32 * kl + 8 * h + q4) * 256 + ((c ^ (2 * kl) ^ q4) * 64) + 32 * (p4 & 1) + 16 * blk + 8 * (p4 >> 1);
      }
      lds_char* tp = smem + 131072 + m * 12288 + (32 * kl + 8 * h) * 4;
      f32x16 acc[4] = {};
      f32x4 ts[4], tz[4];
      u32x4 av[4];
      u32x4 af[2][2] = {}, bf[2][2][4] = {};
      auto rdtab = [&](int o, int c) {
        ts[c] = ldsr<f32x4>(tp + o + (c >> 1) * 64 + (c & 1) * 16);
        tz[c] = ldsr<f32x4>(tp + o + 6144 + (c >> 1) * 64 + (c & 1) * 16);
      };
      auto step = [&](int slot) {
        const int buf = slot & 1;
        lds_char* sl = smem + slot * 32768;
        BAR();
#pragma unroll
        for (int c = 0; c < 4; ++c) av[c] = ldsr<u32x4>(sl + aoff[c]);
        SB();
#pragma unroll
        for (int u = 0; u < 8; ++u) {
          acc[u & 3] = mfma32(af[buf ^ 1][u >> 2], bf[buf ^ 1][u >> 2][u & 3], acc[u & 3]);
          SB();
          if (u < 4) {
#pragma unroll
            for (int v = 2 * u; v < 2 * u + 2; ++v)
              bf[buf][v >> 2][v & 3] = trread2(sl + boff[v & 3] + (16 * (v >> 2)) * 256, sl + boff[v & 3] + (16 * (v >> 2) + 4) * 256);
          } else {
            const int c = u - 4;
            u32x2 o = deq4(av[c], ts[c], tz[c]);
            af[buf][c >> 1][(c & 1) * 2] = o[0]; af[buf][c >> 1][(c & 1) * 2 + 1] = o[1];
            SB();
            rdtab((slot + 1) * 256, c);
          }
          SB();
        }
      };
#pragma unroll
      for (int c = 0; c < 4; ++c) rdtab(0, c);
#pragma unroll 1
      for (int p = 0; p < 24; p += 4) {
        step(0); step(1); step(2); step(3);
        tp += 1024;
      }
#pragma unroll
      for (int u = 0; u < 8; ++u) {
        const int c = (u & 1) + (u < 4 ? 2 : 0), s2 = (u >> 1) & 1;
        acc[c] = mfma32(af[1][s2], bf[1][s2][c], acc[c]);
      }
      if (wid == 0) STAMP(1);
      lds_char* RED = smem + m * 16384;
#pragma unroll
      for (int c = 0; c < 2; ++c)
#pragma unroll
        for (int j = 0; j < 4; ++j) {
          const f32x16& g = acc[2 + c];
          ldsw<f32x4>(RED + kl * 8192 + c * 4096 + j * 1024 + lane * 16, f32x4{g[4 * j], g[4 * j + 1], g[4 * j + 2], g[4 * j + 3]});
        }
      WAIT_L(0);
      if (lane == 0) __hip_atomic_fetch_add(sw + 6 + m, 1u, RLX_WG);
      lds_spin(sw + 6 + m, 2);
#pragma unroll
      for (int c = 0; c < 2; ++c) {
        f32x16 o = acc[c];
#pragma unroll
        for (int j = 0; j < 4; ++j) {
          f32x4 v = ldsr<f32x4>(RED + (kl ^ 1) * 8192 + c * 4096 + j * 1024 + lane * 16);
          o[4 * j] += v[0]; o[4 * j + 1] += v[1]; o[4 * j + 2] += v[2]; o[4 * j + 3] += v[3];
        }
        const int n32 = nt * 4 + kl * 2 + c;
        const unsigned oo = (((n32 / 3) * 40 + (mt * 2 + m) * 2) * 3 + n32 % 3) * 1024;
        __builtin_amdgcn_raw_buffer_store_b128(pack8(o, 0), wsr, lane * 16, oo, 16);
        __builtin_amdgcn_raw_buffer_store_b128(pack8(o, 1), wsr, lane * 16 + 3072, oo, 16);
      }
    }
  } else {
    const int t = idx - 12;
    const int xb_ = (xcd - 6) * 4 + (t & 3), eb = t >> 2;
    myflag = FX + xb_ * 5 + eb;
    if (wid >= 4) {
      const int w4 = wid - 4;
      unsigned xg[4], ug[4];
#pragma unroll
      for (int i = 0; i < 4; ++i) {
        int row = i * 8 + (lane >> 3);
        xg[i] = row * (D_ * 2) + (((lane & 7) ^ ((row >> 1) & 7)) * 16) - i * 1024;
        ug[i] = (i * 4 + (lane >> 4)) * (D_ * 2) + (((lane & 15) ^ ((lane >> 4) << 2)) * 16) - i * 1024;
      }
      const char* xp = (const char*)x + (size_t)(xb_ * 256 + w4 * 64) * (D_ * 2);
      const char* up = (const char*)U + eb * 256 + (size_t)(w4 * 16) * (D_ * 2);
      unsigned so = 0;
      auto issue = [&]() {
        set_m0(lb + so + 32768 + w4 * 4096);
        glds<0>(up, ug[0]); glds<1024>(up, ug[1]); glds<2048>(up, ug[2]); glds<3072>(up, ug[3]);
        set_m0(lb + so + w4 * 8192);
        glds<0>(xp, xg[0]); glds<1024>(xp, xg[1]); glds<2048>(xp, xg[2]); glds<3072>(xp, xg[3]);
        set_m0(lb + so + w4 * 8192 + 4096);
        const char* xq = xp + 32 * D_ * 2;
        glds<0>(xq, xg[0]); glds<1024>(xq, xg[1]); glds<2048>(xq, xg[2]); glds<3072>(xq, xg[3]);
        xp += 128; up += 64 * D_ * 2;
        so = so == 98304 ? 0 : so + 49152;
      };
      issue();
      WAIT_V(0);
      BAR();
      issue();
#pragma unroll 1
      for (int st = 2; st < 10; ++st) {
        issue();
        WAIT_V(12);
        BAR();
      }
      WAIT_V(0);
      BAR();
    } else {
      int xoff[4], uoff[4];
#pragma unroll
      for (int s = 0; s < 4; ++s) {
        xoff[s] = (wid * 64 + r) * 128 + (((2 * s + h) ^ ((r >> 1) & 7)) * 16);
        uoff[s] = 32768 + (8 * h + q4) * 256 + ((s ^ q4) * 64) + blk * 32 + p4 * 8;
      }
      f32x16 acc[2][4] = {};
      u32x4 xf[2][2], uf[2][4];
      auto rd = [&](lds_char* base, int s, int buf, int i) {
        if (i < 2) xf[buf][i] = ldsr<u32x4>(base + xoff[s] + i * 4096);
        else uf[buf][i - 2] = trread2(base + uoff[i - 2] + s * 4096, base + uoff[i - 2] + s * 4096 + 1024);
      };
      if (wid == 3) put_token(__hip_atomic_load(sync, RLX_AGENT));
      WAIT_L(0);
      BAR();
#pragma unroll
      for (int i = 0; i < 6; ++i) rd(smem, 0, 0, i);
      unsigned so = 0;
#pragma unroll 1
      for (int st = 0; st < 10; ++st) {
#pragma unroll
        for (int s = 0; s < 4; ++s) {
          if (s == 3) so = so == 98304 ? 0 : so + 49152;
          lds_char* base = smem + so;
#pragma unroll
          for (int u = 0; u < 8; ++u) {
            acc[u & 1][u >> 1] = mfma32(uf[s & 1][u >> 1], xf[s & 1][u & 1], acc[u & 1][u >> 1]);
            SB();
            if (u == 0 && s == 3 && st < 9) { WAIT_L(0); BAR(); }
            if (u < 6) rd(base, (s + 1) & 3, (s + 1) & 1, u);
            SB();
          }
        }
      }
#pragma unroll
      for (int u = 0; u < 8; ++u) {
        const unsigned o = XRF_OFF + ((xb_ * 8 + wid * 2 + (u & 1)) * 40 + (eb * 4 + (u >> 1)) * 2) * 1024;
        __builtin_amdgcn_raw_buffer_store_b128(pack8(acc[u & 1][u >> 1], 0), wsr, lane * 16, o, 16);
        __builtin_amdgcn_raw_buffer_store_b128(pack8(acc[u & 1][u >> 1], 1), wsr, lane * 16 + 1024, o, 16);
      }
      if (wid == 0) STAMP(3);
    }
  }

  const int mb = (xcd >> 1) * 4 + (idx & 3), nb = (xcd & 1) * 8 + (idx >> 2);
  const unsigned vo = lane * 16;
  const char* xsrc = ws + XRF_OFF + (mb * 4 + (wid & 3)) * 40960;
  auto ldx = [&](int ch) {
    asm volatile("s_mov_b32 m0, %2\n\ts_nop 0\n\tglobal_load_lds_dwordx4 %0, %1 sc1\n\tglobal_load_lds_dwordx4 %0, %1 offset:1024 sc1\n\t"
                 "global_load_lds_dwordx4 %0, %1 offset:2048 sc1\n\tglobal_load_lds_dwordx4 %0, %1 offset:3072 sc1"
                 ::"v"(vo), "s"(xsrc + ch * 4096), "s"(lb + (ch % 5) * 28672 + 12288 + (wid & 3) * 4096) : "memory");
  };
  if (wid < 4) {
    lds_spin(sw + 2, 1);
    const u64 tok = (u64)sw[0] | ((u64)sw[1] << 32);
    if (!is_pf) {
      if (wid == 0) STAMP(10);
      if (lane == 0) __hip_atomic_fetch_add(sw + 8, 1u, RLX_WG);
      asm volatile("s_waitcnt vmcnt(0) lgkmcnt(0)" ::: "memory");
      if (lane == 0) {
        if (__hip_atomic_fetch_add(sw + 3, 1u, RLX_WG) == 3) __hip_atomic_store(sync + myflag * FS, tok, RLX_AGENT);
      }
      if (wid == 0) STAMP(2);
    }
    if (wid == 0) {
      lds_spin(sw + 3, 4);
      const int n0 = (nb * 3) >> 2, n1 = (nb * 3 + 2) >> 2;
      poll_flags(sync + (FT + n0 * 10 + min(lane, (n1 - n0) * 10 + 9)) * FS, tok);
      if (lane == 0) __hip_atomic_store(sw + 5, 1u, RLX_WG);
      STAMP(4);
    } else {
      lds_spin(sw + 5, 1);
    }
    const char* tsrc = ws + nb * 122880 + wid * 3072;
    auto ldt = [&](int ch) {
      asm volatile("s_mov_b32 m0, %2\n\ts_nop 0\n\tglobal_load_lds_dwordx4 %0, %1 sc1\n\tglobal_load_lds_dwordx4 %0, %1 offset:1024 sc1\n\t"
                   "global_load_lds_dwordx4 %0, %1 offset:2048 sc1" ::"v"(vo), "s"(tsrc + ch * 12288), "s"(lb + (ch % 5) * 28672 + wid * 3072) : "memory");
    };
    ldt(0); ldt(1); ldt(2);
    WAIT_V(6); BAR(); ldt(3);
    WAIT_V(6); BAR(); ldt(4);
    WAIT_V(6); BAR(); ldt(5); ldx(5);
    WAIT_V(10); BAR(); ldt(6); ldx(6);
    WAIT_V(14); BAR(); ldt(7); ldx(7);
    WAIT_V(14); BAR(); ldt(8); ldx(8);
    WAIT_V(14); BAR(); ldt(9); ldx(9);
    WAIT_V(14); BAR();
    WAIT_V(7); BAR();
    WAIT_V(0); BAR();
  } else {
    const int w4 = wid - 4;
    if (wid == 4) {
      lds_spin(sw + 2, 1);
      const u64 tok = (u64)sw[0] | ((u64)sw[1] << 32);
      poll_flags(sync + (FX + (mb >> 1) * 5 + min(lane, 4)) * FS, tok);
      lds_spin(sw + 8, 4);
      if (lane == 0) __hip_atomic_store(sw + 4, 1u, RLX_WG);
      STAMP(8);
    } else {
      lds_spin(sw + 4, 1);
    }
#pragma unroll
    for (int ch = 0; ch < 5; ++ch) ldx(ch);
    f32x16 acc[3] = {};
    u32x4 tf[3][3], xf[3];
    lds_char* tb = smem + lane * 16;
    lds_char* xb = smem + lane * 16 + 12288 + w4 * 4096;
    auto rdf = [&](int ks, int c) {
      const int o = ((ks >> 2) % 5) * 28672;
      if (c < 3) tf[ks % 3][c] = ldsr<u32x4>(tb + o + ((ks & 3) * 3 + c) * 1024);
      else xf[ks % 3] = ldsr<u32x4>(xb + o + (ks & 3) * 1024);
    };
    WAIT_V(16);
    BAR();
#pragma unroll
    for (int c = 0; c < 8; ++c) rdf(c >> 2, 3 - (c & 3));
#pragma unroll
    for (int ks = 0; ks < 40; ++ks) {
#pragma unroll
      for (int c = 0; c < 3; ++c) {
        acc[c] = mfma32(tf[ks % 3][c], xf[ks % 3], acc[c]);
        SB();
        if (ks < 38) {
          if (c == 0) {
            if ((ks & 3) == 2) {
              if (ks == 2) WAIT_V(12);
              if (ks == 6) WAIT_V(8);
              if (ks == 10) WAIT_V(4);
              if (ks == 14) WAIT_V(0);
              BAR(); asm volatile("" ::: "memory");
            }
            rdf(ks + 2, 3);
          }
          rdf(ks + 2, c);
        }
        SB();
      }
    }
    if (wid == 4) STAMP(5);
    lds_char* eb = smem + w4 * 6656;
#pragma unroll
    for (int c = 0; c < 3; ++c) {
      ldsw<u32x4>(eb + r * 208 + c * 64 + h * 32, pack8(acc[c], 0));
      ldsw<u32x4>(eb + r * 208 + c * 64 + h * 32 + 16, pack8(acc[c], 1));
    }
    const __amdgpu_buffer_rsrc_t yr = __builtin_amdgcn_make_buffer_rsrc((void*)y, 0, 2048 * F_ * 2, 0x00020000);
    const unsigned yo = ((mb * 128 + w4 * 32) * F_ + nb * 96) * 2;
#pragma unroll
    for (int i = 0; i < 6; ++i) {
      int q = i * 64 + lane, row = q / 12, chn = q % 12;
      __builtin_amdgcn_raw_buffer_store_b128(ldsr<u32x4>(eb + row * 208 + chn * 16), yr, row * (F_ * 2) + chn * 16, yo, Y_AUX);
    }
    WAIT_V(0);
    if (wid == 4) STAMP(6);
    if (wid == 4 && blockIdx.x == AMP_BLK) {
      WAIT_L(0);
      unsigned d = __builtin_amdgcn_readfirstlane(sw[16 + AMP] - sw[16]);
      unsigned t0 = (unsigned)__builtin_amdgcn_s_memrealtime();
      while ((unsigned)__builtin_amdgcn_s_memrealtime() - t0 < AMPF * d) __builtin_amdgcn_s_sleep(1);
    }
  }
}

extern "C" void kernel_launch(void* const* d_in, const int* in_sizes, int n_in, void* d_out, int out_size, void* d_ws,
                              size_t ws_size, hipStream_t stream) {
  (void)hipFuncSetAttribute((const void*)quip, hipFuncAttributeMaxDynamicSharedMemorySize, LDS_BYTES);
  quip<<<256, 512, LDS_BYTES, stream>>>((const __hip_bfloat16*)d_in[0], (const int*)d_in[1], (const __hip_bfloat16*)d_in[2],
                                        (const __hip_bfloat16*)d_in[3], (const __hip_bfloat16*)d_in[4], (const __hip_bfloat16*)d_in[5],
                                        (char*)d_ws, (__hip_bfloat16*)d_out);
}
```

```cpp
#include <hip/hip_runtime.h>
#include <hip/hip_bf16.h>

typedef __attribute__((ext_vector_type(8))) __bf16 bf16x8;
typedef __attribute__((ext_vector_type(2))) __bf16 bf16x2;
typedef __attribute__((ext_vector_type(4))) short s16x4;
typedef __attribute__((ext_vector_type(16))) float f32x16;
typedef __attribute__((ext_vector_type(4))) float f32x4;
typedef __attribute__((ext_vector_type(4))) unsigned u32x4;
typedef __attribute__((ext_vector_type(2))) unsigned u32x2;
typedef __attribute__((address_space(3))) s16x4 lds_s16x4;
typedef __attribute__((address_space(3))) char lds_char;
typedef __attribute__((address_space(3))) unsigned lds_u32;
typedef __attribute__((address_space(1))) unsigned long long gu64;
typedef unsigned long long u64;

#define STAMP(i)
#define Y_AUX 2
#define XCD_PERM 0x54321706
#define YPART 1
#define XRV 0
#define YV 0
#define TGATE 0
#define XTOUCH 0
#define POLL_SLEEP 4
constexpr int D_ = 640, F_ = 1536;

#define WAIT_V(n) asm volatile("s_waitcnt vmcnt(" #n ")" ::: "memory")
#define WAIT_L(n) asm volatile("s_waitcnt lgkmcnt(" #n ")" ::: "memory")
#define BAR() __builtin_amdgcn_s_barrier()
#define SB() __builtin_amdgcn_sched_barrier(0)
#define RLX_AGENT __ATOMIC_RELAXED, __HIP_MEMORY_SCOPE_AGENT
#define RLX_WG __ATOMIC_RELAXED, __HIP_MEMORY_SCOPE_WORKGROUP

__device__ __forceinline__ void set_m0(unsigned ldsaddr) { asm volatile("s_mov_b32 m0, %0\n\ts_nop 0" ::"s"(ldsaddr) : "memory"); }
template <int OFF> __device__ __forceinline__ void glds(const void* sbase, unsigned voff) {
  asm volatile("global_load_lds_dwordx4 %0, %1 offset:%2" ::"v"(voff), "s"(sbase), "i"(OFF) : "memory");
}
__device__ __forceinline__ void touch(const void* sbase, unsigned voff) {
  asm volatile("global_load_lds_dword %0, %1" ::"v"(voff), "s"(sbase) : "memory");
}
__device__ __forceinline__ unsigned pack2(float a, float b) {
  bf16x2 p; p[0] = (__bf16)a; p[1] = (__bf16)b;
  return __builtin_bit_cast(unsigned, p);
}
__device__ __forceinline__ float bflo(unsigned u) { return __builtin_bit_cast(float, u << 16); }
__device__ __forceinline__ float bfhi(unsigned u) { return __builtin_bit_cast(float, u & 0xffff0000u); }
__device__ __forceinline__ u32x4 trread2(lds_char* p0, lds_char* p1) {
  u32x2 lo = __builtin_bit_cast(u32x2, __builtin_amdgcn_ds_read_tr16_b64_v4i16((lds_s16x4*)p0));
  u32x2 hi = __builtin_bit_cast(u32x2, __builtin_amdgcn_ds_read_tr16_b64_v4i16((lds_s16x4*)p1));
  return u32x4{lo[0], lo[1], hi[0], hi[1]};
}
template <typename T> __device__ __forceinline__ T ldsr(lds_char* p) { return *(__attribute__((address_space(3))) T*)p; }
template <typename T> __device__ __forceinline__ void ldsw(lds_char* p, T v) { *(__attribute__((address_space(3))) T*)p = v; }
__device__ __forceinline__ f32x16 mfma32(u32x4 a, u32x4 b, f32x16 c) {
  return __builtin_amdgcn_mfma_f32_32x32x16_bf16(__builtin_bit_cast(bf16x8, a), __builtin_bit_cast(bf16x8, b), c, 0, 0, 0);
}
__device__ __forceinline__ u32x4 pack8(const f32x16& a, int s) {
  u32x4 r;
  r[0] = pack2(a[8 * s + 0], a[8 * s + 1]); r[1] = pack2(a[8 * s + 2], a[8 * s + 3]);
  r[2] = pack2(a[8 * s + 4], a[8 * s + 5]); r[3] = pack2(a[8 * s + 6], a[8 * s + 7]);
  return r;
}
__device__ __forceinline__ u32x2 deq4(u32x4 q, f32x4 s, f32x4 z) {
  float w0, w1, w2, w3;
  asm("v_cvt_f32_i32 %0, %4\n\tv_cvt_f32_i32 %1, %5\n\tv_cvt_f32_i32 %2, %6\n\tv_cvt_f32_i32 %3, %7\n\t"
      "v_fma_f32 %0, %0, %8, %12\n\tv_fma_f32 %1, %1, %9, %13\n\tv_fma_f32 %2, %2, %10, %14\n\tv_fma_f32 %3, %3, %11, %15\n\t"
      "v_cvt_pk_bf16_f32 %0, %0, %1\n\tv_cvt_pk_bf16_f32 %1, %2, %3"
      : "=&v"(w0), "=&v"(w1), "=&v"(w2), "=&v"(w3)
      : "v"(q[0]), "v"(q[1]), "v"(q[2]), "v"(q[3]), "v"(s[0]), "v"(s[1]), "v"(s[2]), "v"(s[3]), "v"(z[0]), "v"(z[1]), "v"(z[2]), "v"(z[3]));
  return u32x2{__builtin_bit_cast(unsigned, w0), __builtin_bit_cast(unsigned, w1)};
}
__device__ __forceinline__ void lds_spin(lds_u32* w, unsigned want) {
  while ((unsigned)__builtin_amdgcn_readfirstlane(__hip_atomic_load(w, RLX_WG)) < want) __builtin_amdgcn_s_sleep(1);
  asm volatile("" ::: "memory");
}
__device__ __forceinline__ void poll_flags(gu64* f, u64 tok) {
#pragma unroll 1
  for (unsigned spins = 0; spins < (1u << 20); ++spins) {
    if (__all(__hip_atomic_load(f, RLX_AGENT) == tok)) break;
    __builtin_amdgcn_s_sleep(POLL_SLEEP);
  }
  __builtin_amdgcn_fence(__ATOMIC_ACQUIRE, "wavefront");
}

constexpr int XRF_OFF = 2 << 20, SYNC_OFF = 6 << 20;
#define FS 16
constexpr int FT = 1, FX = 121;
constexpr int FA = 185 * FS;
constexpr int CODE_TOUCH_MAX = 14464;
constexpr int L_SYNC = 156160, L_DUMP = 156416, LDS_BYTES = 156672;

__global__ __launch_bounds__(512, 2) void quip(const __hip_bfloat16* __restrict__ x, const int* __restrict__ qw,
                                               const __hip_bfloat16* __restrict__ scales, const __hip_bfloat16* __restrict__ zeros,
                                               const __hip_bfloat16* __restrict__ U, const __hip_bfloat16* __restrict__ V,
                                               char* ws, __hip_bfloat16* __restrict__ y) {
  extern __shared__ __attribute__((aligned(16))) char smem_[];
  lds_char* smem = (lds_char*)smem_;
  const unsigned lb = (unsigned)(size_t)smem;
  const int wid = __builtin_amdgcn_readfirstlane(threadIdx.x >> 6), lane = threadIdx.x & 63;
  const int xcd = (XCD_PERM >> ((blockIdx.x & 7) * 4)) & 7, idx = blockIdx.x >> 3;
  const int h = lane >> 5, r = lane & 31;
  const int blk = (lane >> 4) & 1, q4 = (lane & 15) >> 2, p4 = lane & 3;
  lds_u32* sw = (lds_u32*)(smem + L_SYNC);
  gu64* sync = (gu64*)(ws + SYNC_OFF);
  const __amdgpu_buffer_rsrc_t wsr = __builtin_amdgcn_make_buffer_rsrc(ws, 0, SYNC_OFF, 0x00020000);
  const bool is_pf = idx < 12 && (XRV == 1 || xcd < 6);
  if (wid == 0) STAMP(0);

  auto put_token = [&](u64 epoch) {
    if (wid == 3 && lane == 0) {
      const u64 e = epoch + 0x9E3779B97F4A7C15ull;
      sw[0] = (unsigned)e; sw[1] = (unsigned)(e >> 32);
      __hip_atomic_store(sw + 2, 1u, RLX_WG);
      __hip_atomic_store(sync + FA + blockIdx.x, e, RLX_AGENT);
    }
  };
  if (wid == 3) {
    if (lane < 16) sw[lane] = ((lane == 3 || lane == 8) && is_pf) ? 4u : 0u;
  } else if (wid == 1 && idx < 2) {
    u64 pc;
    asm volatile("s_getpc_b64 %0" : "=s"(pc));
    set_m0(lb + L_DUMP);
    touch((const char*)pc, min(idx * 8192 + lane * 128, CODE_TOUCH_MAX));
  }
  int myflag = 0;

  if (is_pf) {
    if (wid == 0) {
      set_m0(lb + L_DUMP);
      const bool tg = xcd < 6;
      const unsigned ao = tg ? (lane >> 1) * (F_ * 4) + (lane & 1) * 128 : lane * (D_ * 2);
      const unsigned bo = tg ? (lane >> 3) * (F_ * 2) + (lane & 7) * 128 : lane * 128;
      const char* ab = tg ? (const char*)qw + (size_t)((xcd & 1) * 320) * (F_ * 4) : (const char*)x + (size_t)((xcd - 6) * 1024) * (D_ * 2);
      const char* bb = tg ? (const char*)V + (xcd >> 1) * 1024 : (const char*)U;
      const int na = tg ? 10 : 16, nt_ = tg ? 18 : 26, np = tg ? 24 : 10;
      const int aj = tg ? 32 * F_ * 4 : 64 * D_ * 2, ak = tg ? 256 : 128;
      const int bj = tg ? 8 * F_ * 2 : 8192, bk = tg ? 64 * F_ * 2 : 64 * D_ * 2;
      int p = 0, j = idx, n = 0;
      __builtin_amdgcn_s_waitcnt(0x0F70);
#pragma unroll 1
      while (p < np) {
        if (j < na) touch(ab + j * aj + p * ak, ao);
        else touch(bb + (j - na) * bj + p * bk, bo);
        j += 12;
        if (j >= nt_) { j -= nt_; ++p; }
        if (++n == 3) BAR();
      }
      STAMP(7);
    } else {
      WAIT_L(0);
      BAR();
      if (wid == 3) put_token(__hip_atomic_load(sync, RLX_AGENT));
      if (blockIdx.x == 1 && wid == 2) {
        lds_spin(sw + 2, 1);
        const u64 tok = (u64)sw[0] | ((u64)sw[1] << 32);
#pragma unroll 1
        for (int i = 0; i < 4; ++i) poll_flags(sync + FA + i * 64 + lane, tok);
        if (lane == 0) __hip_atomic_store(sync, tok, RLX_AGENT);
      }
    }
  } else if (xcd < 6) {
    const int t = idx - 12;
    const int mt = (xcd & 1) * 5 + t % 5, nt = (xcd >> 1) * 4 + t / 5;
    myflag = FT + nt * 10 + mt;
    if (wid >= 4) {
      const int w4 = wid - 4, rr = lane >> 4, c16 = lane & 15;
      unsigned ag[4], bg[4];
#pragma unroll
      for (int i = 0; i < 4; ++i) {
        int row = i * 4 + rr;
        ag[i] = row * (F_ * 4) + ((c16 ^ row) * 16) - i * 1024;
        bg[i] = row * (F_ * 2) + ((c16 ^ (rr << 2)) * 16) - i * 1024;
      }
      const char* ap = (const char*)qw + (size_t)(mt * 64 + w4 * 16) * (F_ * 4);
      const char* bp = (const char*)V + nt * 256 + (size_t)(w4 * 16) * (F_ * 2);
      unsigned so = lb + w4 * 4096;
      auto issue = [&](int p) {
        unsigned base = so + (p & 3) * 32768;
        set_m0(base);
        glds<0>(ap, ag[0]); glds<1024>(ap, ag[1]); glds<2048>(ap, ag[2]); glds<3072>(ap, ag[3]);
        set_m0(base + 16384);
        glds<0>(bp, bg[0]); glds<1024>(bp, bg[1]); glds<2048>(bp, bg[2]); glds<3072>(bp, bg[3]);
        ap += 256; bp += 64 * F_ * 2;
      };
      issue(0);
      issue(1);
      BAR();
#pragma unroll 1
      for (int p = 2; p < 24; ++p) {
        WAIT_V(8);
        BAR();
        issue(p);
      }
      WAIT_V(8);
      BAR();
      WAIT_V(0);
      BAR();
    } else {
      const int m = wid & 1, kl = wid >> 1;
      {
        const size_t o = (size_t)((mt * 2 + m) * F_ + kl * 768) * 2;
        lds_char* T = smem + 131072 + m * 12288 + kl * 3072;
        u32x4 s8[2], z8[2];
#pragma unroll
        for (int j = 0; j < 2; ++j) {
          int e = j ? 64 + (lane & 31) : lane;
          s8[j] = *(const u32x4*)((const char*)scales + o + e * 16);
          z8[j] = *(const u32x4*)((const char*)zeros + o + e * 16);
        }
        const u64 epoch = __hip_atomic_load(sync, RLX_AGENT);
#pragma unroll
        for (int j = 0; j < 2; ++j) {
          int e = j ? 64 + (lane & 31) : lane;
          f32x4 sa, sb, za, zb;
          sa[0] = bflo(s8[j][0]); sa[1] = bfhi(s8[j][0]); sa[2] = bflo(s8[j][1]); sa[3] = bfhi(s8[j][1]);
          sb[0] = bflo(s8[j][2]); sb[1] = bfhi(s8[j][2]); sb[2] = bflo(s8[j][3]); sb[3] = bfhi(s8[j][3]);
          za[0] = -bflo(z8[j][0]) * sa[0]; za[1] = -bfhi(z8[j][0]) * sa[1]; za[2] = -bflo(z8[j][1]) * sa[2]; za[3] = -bfhi(z8[j][1]) * sa[3];
          zb[0] = -bflo(z8[j][2]) * sb[0]; zb[1] = -bfhi(z8[j][2]) * sb[1]; zb[2] = -bflo(z8[j][3]) * sb[2]; zb[3] = -bfhi(z8[j][3]) * sb[3];
          ldsw<f32x4>(T + e * 32, sa); ldsw<f32x4>(T + e * 32 + 16, sb);
          ldsw<f32x4>(T + 6144 + e * 32, za); ldsw<f32x4>(T + 6144 + e * 32 + 16, zb);
        }
        put_token(epoch);
        WAIT_L(0);
        BAR();
      }
      int aoff[4], boff[4];
#pragma unroll
      for (int c = 0; c < 4; ++c) {
        int ch = 8 * kl + 4 * (c >> 1) + 2 * h + (c & 1);
        aoff[c] = (m * 32 + r) * 256 + ((ch ^ (r & 15)) * 16);
        boff[c] = 16384 + (32 * kl + 8 * h + q4) * 256 + ((c ^ (2 * kl) ^ q4) * 64) + 32 * (p4 & 1) + 16 * blk + 8 * (p4 >> 1);
      }
      lds_char* tp = smem + 131072 + m * 12288 + (32 * kl + 8 * h) * 4;
      f32x16 acc[4] = {};
      f32x4 ts[4], tz[4];
      u32x4 av[4];
      u32x4 af[2][2] = {}, bf[2][2][4] = {};
      auto rdtab = [&](int o, int c) {
        ts[c] = ldsr<f32x4>(tp + o + (c >> 1) * 64 + (c & 1) * 16);
        tz[c] = ldsr<f32x4>(tp + o + 6144 + (c >> 1) * 64 + (c & 1) * 16);
      };
      auto step = [&](int slot) {
        const int buf = slot & 1;
        lds_char* sl = smem + slot * 32768;
        BAR();
#pragma unroll
        for (int c = 0; c < 4; ++c) av[c] = ldsr<u32x4>(sl + aoff[c]);
        SB();
#pragma unroll
        for (int u = 0; u < 8; ++u) {
          acc[u & 3] = mfma32(af[buf ^ 1][u >> 2], bf[buf ^ 1][u >> 2][u & 3], acc[u & 3]);
          SB();
          if (u < 4) {
#pragma unroll
            for (int v = 2 * u; v < 2 * u + 2; ++v)
              bf[buf][v >> 2][v & 3] = trread2(sl + boff[v & 3] + (16 * (v >> 2)) * 256, sl + boff[v & 3] + (16 * (v >> 2) + 4) * 256);
          } else {
            const int c = u - 4;
            u32x2 o = deq4(av[c], ts[c], tz[c]);
            af[buf][c >> 1][(c & 1) * 2] = o[0]; af[buf][c >> 1][(c & 1) * 2 + 1] = o[1];
            SB();
            rdtab((slot + 1) * 256, c);
          }
          SB();
        }
      };
#pragma unroll
      for (int c = 0; c < 4; ++c) rdtab(0, c);
#pragma unroll 1
      for (int p = 0; p < 24; p += 4) {
        step(0); step(1); step(2); step(3);
        tp += 1024;
      }
      if (wid == 0) STAMP(1);
      lds_char* RED = smem + m * 16384 + lane * 16;
      const int n32 = nt * 4 + kl * 2;
      const unsigned oo0 = (((n32 / 3) * 40 + (mt * 2 + m) * 2) * 3 + n32 % 3) * 1024;
      const unsigned oo1 = ((((n32 + 1) / 3) * 40 + (mt * 2 + m) * 2) * 3 + (n32 + 1) % 3) * 1024;
      SB();
#pragma unroll
      for (int u = 0; u < 8; ++u) {
        const int c = (u < 4 ? 2 : 0) + ((u >> 1) & 1), s2 = u & 1;
        acc[c] = mfma32(af[1][s2], bf[1][s2][c], acc[c]);
        SB();
        if (u >= 3 && u < 7) {
#pragma unroll
          for (int j = 2 * (u - 3); j < 2 * (u - 3) + 2; ++j) {
            const f32x16& g = acc[2 + (j >> 2)];
            const int q = 4 * (j & 3);
            ldsw<f32x4>(RED + kl * 8192 + j * 1024, f32x4{g[q], g[q + 1], g[q + 2], g[q + 3]});
          }
          SB();
        }
      }
      asm volatile("" ::: "memory");
      __hip_atomic_store(sw + 9 + wid, 1u, RLX_WG);
      while (__builtin_amdgcn_readfirstlane(__hip_atomic_load(sw + 9 + (wid ^ 2), RLX_WG)) == 0) {}
      asm volatile("" ::: "memory");
      f32x4 pv[8];
#pragma unroll
      for (int j = 0; j < 8; ++j) pv[j] = ldsr<f32x4>(RED + (kl ^ 1) * 8192 + j * 1024);
      SB();
#pragma unroll
      for (int j = 0; j < 4; ++j) {
        f32x16& o = acc[j >> 1];
#pragma unroll
        for (int e = 0; e < 8; ++e) o[(j & 1) * 8 + e] += pv[2 * j + (e >> 2)][e & 3];
        __builtin_amdgcn_raw_buffer_store_b128(pack8(o, j & 1), wsr, lane * 16 + (j & 1) * 3072, j < 2 ? oo0 : oo1, 16);
      }
    }
  } else {
    const int mi = idx & 7, eb = idx >> 3;
    const int xmb = (xcd - 6) * 8 + mi;
    myflag = FX + xmb * 4 + eb;
    if (wid >= 4) {
      const int w4 = wid - 4;
      unsigned xg[4], ug[5];
#pragma unroll
      for (int i = 0; i < 4; ++i) {
        int row = i * 8 + (lane >> 3);
        xg[i] = row * (D_ * 2) + (((lane & 7) ^ ((row >> 1) & 7)) * 16) - i * 1024;
      }
#pragma unroll
      for (int i = 0; i < 5; ++i) {
        int off = i * 1024 + lane * 16;
        ug[i] = (off / 320) * (D_ * 2) + off % 320 - (i & 3) * 1024;
      }
      const char* xb = (const char*)x + (size_t)(xmb * 128 + w4 * 32) * (D_ * 2);
      const char* ub = (const char*)U + eb * 320 + (size_t)(w4 * 16) * (D_ * 2);
      int ks = mi + ((eb * 5 + 1) >> 1);
      if (ks >= 10) ks -= 10;
      auto issue = [&](int st) {
        unsigned base = lb + (st & 3) * 36864;
        const char* xp = xb + ks * 128;
        const char* up = ub + (size_t)ks * 64 * (D_ * 2);
        set_m0(base + w4 * 4096);
        glds<0>(xp, xg[0]); glds<1024>(xp, xg[1]); glds<2048>(xp, xg[2]); glds<3072>(xp, xg[3]);
        set_m0(base + 16384 + w4 * 5120);
        glds<0>(up, ug[0]); glds<1024>(up, ug[1]); glds<2048>(up, ug[2]); glds<3072>(up, ug[3]);
        set_m0(base + 16384 + w4 * 5120 + 4096);
        glds<0>(up, ug[4]);
        if (++ks == 10) ks = 0;
      };
      issue(0);
      if (XTOUCH) {
        set_m0(lb + L_DUMP);
        int kt = ks + (w4 >> 1);
        if (kt >= 10) kt -= 10;
        if (w4 < 2 || !(eb & 1)) touch((const char*)x + (size_t)(xmb * 128 + (w4 & 1) * 64) * (D_ * 2) + kt * 128, lane * (D_ * 2));
        if (w4 == 3 && mi < 6) {
          int ku = 8 + mi / 3 + ((eb * 5 + 1) >> 1);
          if (ku >= 10) ku -= 10;
          touch((const char*)U + (size_t)ku * 64 * (D_ * 2) + ((eb * 320) & ~127) + (mi % 3) * 128, lane * (D_ * 2));
        }
      }
#pragma unroll 1
      for (int st = 1; st < 10; ++st) {
        issue(st);
        WAIT_V(9);
        BAR();
      }
      WAIT_V(0);
      BAR();
    } else {
      int xoff[4];
#pragma unroll
      for (int s = 0; s < 4; ++s) xoff[s] = (wid * 32 + r) * 128 + (((2 * s + h) ^ ((r >> 1) & 7)) * 16);
      const int uoff = 16384 + (8 * h + q4) * 320 + blk * 32 + p4 * 8;
      f32x16 acc[5] = {};
      u32x4 xf[2], uf[2][5];
      auto rdu = [&](lds_char* base, int s, int buf, int c) {
        uf[buf][c] = trread2(base + uoff + c * 64 + (16 * s) * 320, base + uoff + c * 64 + (16 * s + 4) * 320);
      };
      if (wid == 3) put_token(__hip_atomic_load(sync, RLX_AGENT));
      WAIT_L(0);
      BAR();
      xf[0] = ldsr<u32x4>(smem + xoff[0]);
#pragma unroll
      for (int c = 0; c < 5; ++c) rdu(smem, 0, 0, c);
#pragma unroll 1
      for (int st = 0; st < 10; ++st) {
#pragma unroll
        for (int s = 0; s < 4; ++s) {
          lds_char* base = smem + ((s < 3 ? st : st + 1) & 3) * 36864;
          const int nx = (s + 1) & 3, nb_ = (s + 1) & 1;
#pragma unroll
          for (int c = 0; c < 5; ++c) {
            acc[c] = mfma32(uf[s & 1][c], xf[s & 1], acc[c]);
            SB();
            if (c == 0) {
              if (s == 3 && st < 9) { BAR(); asm volatile("" ::: "memory"); }
              xf[nb_] = ldsr<u32x4>(base + xoff[nx]);
              rdu(base, nx, nb_, 0);
            } else if (c < 4) {
              rdu(base, nx, nb_, c);
              if (c == 3) rdu(base, nx, nb_, 4);
            }
            SB();
          }
        }
      }
#pragma unroll
      for (int c = 0; c < 5; ++c) {
        const unsigned o = XRF_OFF + ((xmb * 4 + wid) * 40 + (eb * 5 + c) * 2) * 1024;
        __builtin_amdgcn_raw_buffer_store_b128(pack8(acc[c], 0), wsr, lane * 16, o, 16);
        __builtin_amdgcn_raw_buffer_store_b128(pack8(acc[c], 1), wsr, lane * 16 + 1024, o, 16);
      }
      if (wid == 0) STAMP(3);
    }
  }

  const int mb = YPART == 0 ? (xcd >> 1) * 4 + (idx & 3) : YPART == 1 ? (xcd >> 2) * 8 + (idx & 7) : idx & 15;
  const int nb = YPART == 0 ? (xcd & 1) * 8 + (idx >> 2) : YPART == 1 ? (xcd & 3) * 4 + (idx >> 3) : xcd * 2 + (idx >> 4);
  const unsigned vo = lane * 16;
  const char* xsrc = ws + XRF_OFF + (mb * 4 + (wid & 3)) * 40960;
  auto ldx = [&](int ch) {
    asm volatile("s_mov_b32 m0, %2\n\ts_nop 0\n\tglobal_load_lds_dwordx4 %0, %1 sc1\n\tglobal_load_lds_dwordx4 %0, %1 offset:1024 sc1\n\t"
                 "global_load_lds_dwordx4 %0, %1 offset:2048 sc1\n\tglobal_load_lds_dwordx4 %0, %1 offset:3072 sc1"
                 ::"v"(vo), "s"(xsrc + ch * 4096), "s"(lb + (ch % 5) * 28672 + 12288 + (wid & 3) * 4096) : "memory");
  };
  if (wid < 4) {
    if (!is_pf) {
      if (wid == 0) STAMP(10);
      if (lane == 0) __hip_atomic_fetch_add(sw + 8, 1u, RLX_WG);
    }
    lds_spin(sw + 2, 1);
    const u64 tok = (u64)sw[0] | ((u64)sw[1] << 32);
    if (!is_pf) {
      asm volatile("s_waitcnt vmcnt(0) lgkmcnt(0)" ::: "memory");
      if (lane == 0) {
        if (__hip_atomic_fetch_add(sw + 3, 1u, RLX_WG) == 3) __hip_atomic_store(sync + myflag * FS, tok, RLX_AGENT);
      }
      if (wid == 0) STAMP(2);
    }
    if (wid == 0) {
      lds_spin(sw + 3, 4);
      if (TGATE) lds_spin(sw + 4, 1);
      const int n0 = (nb * 3) >> 2, n1 = (nb * 3 + 2) >> 2;
      poll_flags(sync + (FT + n0 * 10 + min(lane, (n1 - n0) * 10 + 9)) * FS, tok);
      if (lane == 0) __hip_atomic_store(sw + 5, 1u, RLX_WG);
      STAMP(4);
    } else {
      lds_spin(sw + 5, 1);
    }
    const char* tsrc = ws + nb * 122880 + wid * 3072;
    unsigned tdst = lb + wid * 3072;
    auto issue = [&]() {
      asm volatile("s_mov_b32 m0, %2\n\ts_nop 0\n\tglobal_load_lds_dwordx4 %0, %1 sc1\n\tglobal_load_lds_dwordx4 %0, %1 offset:1024 sc1\n\t"
                   "global_load_lds_dwordx4 %0, %1 offset:2048 sc1" ::"v"(vo), "s"(tsrc), "s"(tdst) : "memory");
      tsrc += 12288; tdst += 12288;
    };
    issue(); issue(); issue(); issue();
#pragma unroll 1
    for (int ch = 0; ch < 6; ++ch) {
      WAIT_V(9);
      BAR();
      issue();
    }
    WAIT_V(9); BAR(); WAIT_V(6); BAR(); WAIT_V(3); BAR(); WAIT_V(0); BAR();
  } else {
    const int w4 = wid - 4;
    if (wid == 4) {
      lds_spin(sw + 2, 1);
      const u64 tok = (u64)sw[0] | ((u64)sw[1] << 32);
      poll_flags(sync + (XRV ? FX + (mb >> 1) * 5 + min(lane, 4) : FX + mb * 4 + (lane & 3)) * FS, tok);
      lds_spin(sw + 8, 4);
      if (lane == 0) __hip_atomic_store(sw + 4, 1u, RLX_WG);
      STAMP(8);
    } else {
      lds_spin(sw + 4, 1);
    }
    u32x4 xf[40];
    const unsigned xo = XRF_OFF + (mb * 4 + w4) * 40960;
#pragma unroll
    for (int ch = 0; ch < 10; ++ch) {
#pragma unroll
      for (int i = 0; i < 4; ++i) xf[ch * 4 + i] = __builtin_amdgcn_raw_buffer_load_b128(wsr, lane * 16 + i * 1024, xo + ch * 4096, 16);
    }
    SB();
    if (wid == 4) STAMP(11);
    f32x16 acc[3] = {};
    u32x4 tf[3][3];
    lds_char* tb = smem + lane * 16;
    BAR();
    if (wid == 4) STAMP(9);
#pragma unroll
    for (int c = 0; c < 6; ++c) tf[c / 3][c % 3] = ldsr<u32x4>(tb + c * 1024);
#pragma unroll
    for (int ks = 0; ks < 40; ++ks) {
#pragma unroll
      for (int c = 0; c < 3; ++c) {
        acc[c] = mfma32(tf[ks % 3][c], xf[ks], acc[c]);
        SB();
        if (ks < 38) {
          if (c == 0 && (ks & 3) == 2) { BAR(); asm volatile("" ::: "memory"); }
          tf[(ks + 2) % 3][c] = ldsr<u32x4>(tb + ((ks + 2) * 3 + c) * 1024);
        }
        SB();
      }
    }
    if (wid == 4) STAMP(5);
    lds_char* eb = smem + (YV ? 0 : 122880) + w4 * 6656;
#pragma unroll
    for (int c = 0; c < 3; ++c) {
      ldsw<u32x4>(eb + r * 208 + c * 64 + h * 32, pack8(acc[c], 0));
      ldsw<u32x4>(eb + r * 208 + c * 64 + h * 32 + 16, pack8(acc[c], 1));
    }
    const __amdgpu_buffer_rsrc_t yr = __builtin_amdgcn_make_buffer_rsrc((void*)y, 0, 2048 * F_ * 2, 0x00020000);
    const unsigned yo = ((mb * 128 + w4 * 32) * F_ + nb * 96) * 2;
#pragma unroll
    for (int i = 0; i < 6; ++i) {
      int q = i * 64 + lane, row = q / 12, chn = q % 12;
      __builtin_amdgcn_raw_buffer_store_b128(ldsr<u32x4>(eb + row * 208 + chn * 16), yr, row * (F_ * 2) + chn * 16, yo, Y_AUX);
    }
  }
}

extern "C" void kernel_launch(void* const* d_in, const int* in_sizes, int n_in, void* d_out, int out_size, void* d_ws,
                              size_t ws_size, hipStream_t stream) {
  (void)hipFuncSetAttribute((const void*)quip, hipFuncAttributeMaxDynamicSharedMemorySize, LDS_BYTES);
  quip<<<256, 512, LDS_BYTES, stream>>>((const __hip_bfloat16*)d_in[0], (const int*)d_in[1], (const __hip_bfloat16*)d_in[2],
                                        (const __hip_bfloat16*)d_in[3], (const __hip_bfloat16*)d_in[4], (const __hip_bfloat16*)d_in[5],
                                        (char*)d_ws, (__hip_bfloat16*)d_out);
}
```

```cpp
#include <hip/hip_runtime.h>
#include <hip/hip_bf16.h>

typedef __attribute__((ext_vector_type(8))) __bf16 bf16x8;
typedef __attribute__((ext_vector_type(2))) __bf16 bf16x2;
typedef __attribute__((ext_vector_type(4))) short s16x4;
typedef __attribute__((ext_vector_type(16))) float f32x16;
typedef __attribute__((ext_vector_type(4))) float f32x4;
typedef __attribute__((ext_vector_type(4))) unsigned u32x4;
typedef __attribute__((ext_vector_type(2))) unsigned u32x2;
typedef __attribute__((address_space(3))) s16x4 lds_s16x4;
typedef __attribute__((address_space(3))) char lds_char;
typedef __attribute__((address_space(3))) unsigned lds_u32;
typedef __attribute__((address_space(1))) unsigned long long gu64;
typedef unsigned long long u64;

#define STAMP(i)
#define Y_AUX 2
#define XCD_PERM 0x54321706
#define YPART 1
#define XRV 0
#define YV 1
#define TGATE 0
#define XTOUCH 0
#define POLL_SLEEP 4
constexpr int D_ = 640, F_ = 1536;

#define WAIT_V(n) asm volatile("s_waitcnt vmcnt(" #n ")" ::: "memory")
#define WAIT_L(n) asm volatile("s_waitcnt lgkmcnt(" #n ")" ::: "memory")
#define BAR() __builtin_amdgcn_s_barrier()
#define SB() __builtin_amdgcn_sched_barrier(0)
#define RLX_AGENT __ATOMIC_RELAXED, __HIP_MEMORY_SCOPE_AGENT
#define RLX_WG __ATOMIC_RELAXED, __HIP_MEMORY_SCOPE_WORKGROUP

__device__ __forceinline__ void set_m0(unsigned ldsaddr) { asm volatile("s_mov_b32 m0, %0\n\ts_nop 0" ::"s"(ldsaddr) : "memory"); }
template <int OFF> __device__ __forceinline__ void glds(const void* sbase, unsigned voff) {
  asm volatile("global_load_lds_dwordx4 %0, %1 offset:%2" ::"v"(voff), "s"(sbase), "i"(OFF) : "memory");
}
__device__ __forceinline__ void touch(const void* sbase, unsigned voff) {
  asm volatile("global_load_lds_dword %0, %1" ::"v"(voff), "s"(sbase) : "memory");
}
__device__ __forceinline__ unsigned pack2(float a, float b) {
  bf16x2 p; p[0] = (__bf16)a; p[1] = (__bf16)b;
  return __builtin_bit_cast(unsigned, p);
}
__device__ __forceinline__ float bflo(unsigned u) { return __builtin_bit_cast(float, u << 16); }
__device__ __forceinline__ float bfhi(unsigned u) { return __builtin_bit_cast(float, u & 0xffff0000u); }
__device__ __forceinline__ u32x4 trread2(lds_char* p0, lds_char* p1) {
  u32x2 lo = __builtin_bit_cast(u32x2, __builtin_amdgcn_ds_read_tr16_b64_v4i16((lds_s16x4*)p0));
  u32x2 hi = __builtin_bit_cast(u32x2, __builtin_amdgcn_ds_read_tr16_b64_v4i16((lds_s16x4*)p1));
  return u32x4{lo[0], lo[1], hi[0], hi[1]};
}
template <typename T> __device__ __forceinline__ T ldsr(lds_char* p) { return *(__attribute__((address_space(3))) T*)p; }
template <typename T> __device__ __forceinline__ void ldsw(lds_char* p, T v) { *(__attribute__((address_space(3))) T*)p = v; }
__device__ __forceinline__ f32x16 mfma32(u32x4 a, u32x4 b, f32x16 c) {
  return __builtin_amdgcn_mfma_f32_32x32x16_bf16(__builtin_bit_cast(bf16x8, a), __builtin_bit_cast(bf16x8, b), c, 0, 0, 0);
}
__device__ __forceinline__ u32x4 pack8(const f32x16& a, int s) {
  u32x4 r;
  r[0] = pack2(a[8 * s + 0], a[8 * s + 1]); r[1] = pack2(a[8 * s + 2], a[8 * s + 3]);
  r[2] = pack2(a[8 * s + 4], a[8 * s + 5]); r[3] = pack2(a[8 * s + 6], a[8 * s + 7]);
  return r;
}
__device__ __forceinline__ u32x2 deq4(u32x4 q, f32x4 s, f32x4 z) {
  float w0, w1, w2, w3;
  asm("v_cvt_f32_i32 %0, %4\n\tv_cvt_f32_i32 %1, %5\n\tv_cvt_f32_i32 %2, %6\n\tv_cvt_f32_i32 %3, %7\n\t"
      "v_fma_f32 %0, %0, %8, %12\n\tv_fma_f32 %1, %1, %9, %13\n\tv_fma_f32 %2, %2, %10, %14\n\tv_fma_f32 %3, %3, %11, %15\n\t"
      "v_cvt_pk_bf16_f32 %0, %0, %1\n\tv_cvt_pk_bf16_f32 %1, %2, %3"
      : "=&v"(w0), "=&v"(w1), "=&v"(w2), "=&v"(w3)
      : "v"(q[0]), "v"(q[1]), "v"(q[2]), "v"(q[3]), "v"(s[0]), "v"(s[1]), "v"(s[2]), "v"(s[3]), "v"(z[0]), "v"(z[1]), "v"(z[2]), "v"(z[3]));
  return u32x2{__builtin_bit_cast(unsigned, w0), __builtin_bit_cast(unsigned, w1)};
}
__device__ __forceinline__ void lds_spin(lds_u32* w, unsigned want) {
  while ((unsigned)__builtin_amdgcn_readfirstlane(__hip_atomic_load(w, RLX_WG)) < want) __builtin_amdgcn_s_sleep(1);
  asm volatile("" ::: "memory");
}
__device__ __forceinline__ void poll_flags(gu64* f, u64 tok) {
#pragma unroll 1
  for (unsigned spins = 0; spins < (1u << 20); ++spins) {
    if (__all(__hip_atomic_load(f, RLX_AGENT) == tok)) break;
    __builtin_amdgcn_s_sleep(POLL_SLEEP);
  }
  __builtin_amdgcn_fence(__ATOMIC_ACQUIRE, "wavefront");
}

constexpr int XRF_OFF = 2 << 20, SYNC_OFF = 6 << 20;
#define FS 16
constexpr int FT = 1, FX = 121;
constexpr int FA = 185 * FS;
constexpr int CODE_TOUCH_MAX = 14464;
constexpr int L_SYNC = 156160, L_DUMP = 156416, LDS_BYTES = 156672;

__global__ __launch_bounds__(512, 2) void quip(const __hip_bfloat16* __restrict__ x, const int* __restrict__ qw,
                                               const __hip_bfloat16* __restrict__ scales, const __hip_bfloat16* __restrict__ zeros,
                                               const __hip_bfloat16* __restrict__ U, const __hip_bfloat16* __restrict__ V,
                                               char* ws, __hip_bfloat16* __restrict__ y) {
  extern __shared__ __attribute__((aligned(16))) char smem_[];
  lds_char* smem = (lds_char*)smem_;
  const unsigned lb = (unsigned)(size_t)smem;
  const int wid = __builtin_amdgcn_readfirstlane(threadIdx.x >> 6), lane = threadIdx.x & 63;
  const int xcd = (XCD_PERM >> ((blockIdx.x & 7) * 4)) & 7, idx = blockIdx.x >> 3;
  const int h = lane >> 5, r = lane & 31;
  const int blk = (lane >> 4) & 1, q4 = (lane & 15) >> 2, p4 = lane & 3;
  lds_u32* sw = (lds_u32*)(smem + L_SYNC);
  gu64* sync = (gu64*)(ws + SYNC_OFF);
  const __amdgpu_buffer_rsrc_t wsr = __builtin_amdgcn_make_buffer_rsrc(ws, 0, SYNC_OFF, 0x00020000);
  const bool is_pf = idx < 12 && (XRV == 1 || xcd < 6);
  if (wid == 0) STAMP(0);

  auto put_token = [&](u64 epoch) {
    if (wid == 3 && lane == 0) {
      const u64 e = epoch + 0x9E3779B97F4A7C15ull;
      sw[0] = (unsigned)e; sw[1] = (unsigned)(e >> 32);
      __hip_atomic_store(sw + 2, 1u, RLX_WG);
      __hip_atomic_store(sync + FA + blockIdx.x, e, RLX_AGENT);
    }
  };
  if (wid == 3) {
    if (lane < 16) sw[lane] = ((lane == 3 || lane == 8) && is_pf) ? 4u : 0u;
  } else if (wid == 1 && idx < 2) {
    u64 pc;
    asm volatile("s_getpc_b64 %0" : "=s"(pc));
    set_m0(lb + L_DUMP);
    touch((const char*)pc, min(idx * 8192 + lane * 128, CODE_TOUCH_MAX));
  }
  int myflag = 0;

  if (is_pf) {
    if (wid == 0) {
      set_m0(lb + L_DUMP);
      const bool tg = xcd < 6;
      const unsigned ao = tg ? (lane >> 1) * (F_ * 4) + (lane & 1) * 128 : lane * (D_ * 2);
      const unsigned bo = tg ? (lane >> 3) * (F_ * 2) + (lane & 7) * 128 : lane * 128;
      const char* ab = tg ? (const char*)qw + (size_t)((xcd & 1) * 320) * (F_ * 4) : (const char*)x + (size_t)((xcd - 6) * 1024) * (D_ * 2);
      const char* bb = tg ? (const char*)V + (xcd >> 1) * 1024 : (const char*)U;
      const int na = tg ? 10 : 16, nt_ = tg ? 18 : 26, np = tg ? 24 : 10;
      const int aj = tg ? 32 * F_ * 4 : 64 * D_ * 2, ak = tg ? 256 : 128;
      const int bj = tg ? 8 * F_ * 2 : 8192, bk = tg ? 64 * F_ * 2 : 64 * D_ * 2;
      int p = 0, j = idx, n = 0;
      __builtin_amdgcn_s_waitcnt(0x0F70);
#pragma unroll 1
      while (p < np) {
        if (j < na) touch(ab + j * aj + p * ak, ao);
        else touch(bb + (j - na) * bj + p * bk, bo);
        j += 12;
        if (j >= nt_) { j -= nt_; ++p; }
        if (++n == 3) BAR();
      }
      STAMP(7);
    } else {
      WAIT_L(0);
      BAR();
      if (wid == 3) put_token(__hip_atomic_load(sync, RLX_AGENT));
      if (blockIdx.x == 1 && wid == 2) {
        lds_spin(sw + 2, 1);
        const u64 tok = (u64)sw[0] | ((u64)sw[1] << 32);
#pragma unroll 1
        for (int i = 0; i < 4; ++i) poll_flags(sync + FA + i * 64 + lane, tok);
        if (lane == 0) __hip_atomic_store(sync, tok, RLX_AGENT);
      }
    }
  } else if (xcd < 6) {
    const int t = idx - 12;
    const int mt = (xcd & 1) * 5 + t % 5, nt = (xcd >> 1) * 4 + t / 5;
    myflag = FT + nt * 10 + mt;
    if (wid >= 4) {
      const int w4 = wid - 4, rr = lane >> 4, c16 = lane & 15;
      unsigned ag[4], bg[4];
#pragma unroll
      for (int i = 0; i < 4; ++i) {
        int row = i * 4 + rr;
        ag[i] = row * (F_ * 4) + ((c16 ^ row) * 16) - i * 1024;
        bg[i] = row * (F_ * 2) + ((c16 ^ (rr << 2)) * 16) - i * 1024;
      }
      const char* ap = (const char*)qw + (size_t)(mt * 64 + w4 * 16) * (F_ * 4);
      const char* bp = (const char*)V + nt * 256 + (size_t)(w4 * 16) * (F_ * 2);
      unsigned so = lb + w4 * 4096;
      auto issue = [&](int p) {
        unsigned base = so + (p & 3) * 32768;
        set_m0(base);
        glds<0>(ap, ag[0]); glds<1024>(ap, ag[1]); glds<2048>(ap, ag[2]); glds<3072>(ap, ag[3]);
        set_m0(base + 16384);
        glds<0>(bp, bg[0]); glds<1024>(bp, bg[1]); glds<2048>(bp, bg[2]); glds<3072>(bp, bg[3]);
        ap += 256; bp += 64 * F_ * 2;
      };
      issue(0);
      issue(1);
      BAR();
#pragma unroll 1
      for (int p = 2; p < 24; ++p) {
        WAIT_V(8);
        BAR();
        issue(p);
      }
      WAIT_V(8);
      BAR();
      WAIT_V(0);
      BAR();
    } else {
      const int m = wid & 1, kl = wid >> 1;
      {
        const size_t o = (size_t)((mt * 2 + m) * F_ + kl * 768) * 2;
        lds_char* T = smem + 131072 + m * 12288 + kl * 3072;
        u32x4 s8[2], z8[2];
#pragma unroll
        for (int j = 0; j < 2; ++j) {
          int e = j ? 64 + (lane & 31) : lane;
          s8[j] = *(const u32x4*)((const char*)scales + o + e * 16);
          z8[j] = *(const u32x4*)((const char*)zeros + o + e * 16);
        }
        const u64 epoch = __hip_atomic_load(sync, RLX_AGENT);
#pragma unroll
        for (int j = 0; j < 2; ++j) {
          int e = j ? 64 + (lane & 31) : lane;
          f32x4 sa, sb, za, zb;
          sa[0] = bflo(s8[j][0]); sa[1] = bfhi(s8[j][0]); sa[2] = bflo(s8[j][1]); sa[3] = bfhi(s8[j][1]);
          sb[0] = bflo(s8[j][2]); sb[1] = bfhi(s8[j][2]); sb[2] = bflo(s8[j][3]); sb[3] = bfhi(s8[j][3]);
          za[0] = -bflo(z8[j][0]) * sa[0]; za[1] = -bfhi(z8[j][0]) * sa[1]; za[2] = -bflo(z8[j][1]) * sa[2]; za[3] = -bfhi(z8[j][1]) * sa[3];
          zb[0] = -bflo(z8[j][2]) * sb[0]; zb[1] = -bfhi(z8[j][2]) * sb[1]; zb[2] = -bflo(z8[j][3]) * sb[2]; zb[3] = -bfhi(z8[j][3]) * sb[3];
          ldsw<f32x4>(T + e * 32, sa); ldsw<f32x4>(T + e * 32 + 16, sb);
          ldsw<f32x4>(T + 6144 + e * 32, za); ldsw<f32x4>(T + 6144 + e * 32 + 16, zb);
        }
        put_token(epoch);
        WAIT_L(0);
        BAR();
      }
      int aoff[4], boff[4];
#pragma unroll
      for (int c = 0; c < 4; ++c) {
        int ch = 8 * kl + 4 * (c >> 1) + 2 * h + (c & 1);
        aoff[c] = (m * 32 + r) * 256 + ((ch ^ (r & 15)) * 16);
        boff[c] = 16384 + (32 * kl + 8 * h + q4) * 256 + ((c ^ (2 * kl) ^ q4) * 64) + 32 * (p4 & 1) + 16 * blk + 8 * (p4 >> 1);
      }
      lds_char* tp = smem + 131072 + m * 12288 + (32 * kl + 8 * h) * 4;
      f32x16 acc[4] = {};
      f32x4 ts[4], tz[4];
      u32x4 av[4];
      u32x4 af[2][2] = {}, bf[2][2][4] = {};
      auto rdtab = [&](int o, int c) {
        ts[c] = ldsr<f32x4>(tp + o + (c >> 1) * 64 + (c & 1) * 16);
        tz[c] = ldsr<f32x4>(tp + o + 6144 + (c >> 1) * 64 + (c & 1) * 16);
      };
      auto step = [&](int slot) {
        const int buf = slot & 1;
        lds_char* sl = smem + slot * 32768;
        BAR();
#pragma unroll
        for (int c = 0; c < 4; ++c) av[c] = ldsr<u32x4>(sl + aoff[c]);
        SB();
#pragma unroll
        for (int u = 0; u < 8; ++u) {
          acc[u & 3] = mfma32(af[buf ^ 1][u >> 2], bf[buf ^ 1][u >> 2][u & 3], acc[u & 3]);
          SB();
          if (u < 4) {
#pragma unroll
            for (int v = 2 * u; v < 2 * u + 2; ++v)
              bf[buf][v >> 2][v & 3] = trread2(sl + boff[v & 3] + (16 * (v >> 2)) * 256, sl + boff[v & 3] + (16 * (v >> 2) + 4) * 256);
          } else {
            const int c = u - 4;
            u32x2 o = deq4(av[c], ts[c], tz[c]);
            af[buf][c >> 1][(c & 1) * 2] = o[0]; af[buf][c >> 1][(c & 1) * 2 + 1] = o[1];
            SB();
            rdtab((slot + 1) * 256, c);
          }
          SB();
        }
      };
#pragma unroll
      for (int c = 0; c < 4; ++c) rdtab(0, c);
#pragma unroll 1
      for (int p = 0; p < 24; p += 4) {
        step(0); step(1); step(2); step(3);
        tp += 1024;
      }
      if (wid == 0) STAMP(1);
      lds_char* RED = smem + m * 16384 + lane * 16;
      const int n32 = nt * 4 + kl * 2;
      const unsigned oo0 = (((n32 / 3) * 40 + (mt * 2 + m) * 2) * 3 + n32 % 3) * 1024;
      const unsigned oo1 = ((((n32 + 1) / 3) * 40 + (mt * 2 + m) * 2) * 3 + (n32 + 1) % 3) * 1024;
      SB();
#pragma unroll
      for (int u = 0; u < 8; ++u) {
        const int c = (u < 4 ? 2 : 0) + ((u >> 1) & 1), s2 = u & 1;
        acc[c] = mfma32(af[1][s2], bf[1][s2][c], acc[c]);
        SB();
        if (u >= 3 && u < 7) {
#pragma unroll
          for (int j = 2 * (u - 3); j < 2 * (u - 3) + 2; ++j) {
            const f32x16& g = acc[2 + (j >> 2)];
            const int q = 4 * (j & 3);
            ldsw<f32x4>(RED + kl * 8192 + j * 1024, f32x4{g[q], g[q + 1], g[q + 2], g[q + 3]});
          }
          SB();
        }
      }
      asm volatile("" ::: "memory");
      __hip_atomic_store(sw + 9 + wid, 1u, RLX_WG);
      while (__builtin_amdgcn_readfirstlane(__hip_atomic_load(sw + 9 + (wid ^ 2), RLX_WG)) == 0) {}
      asm volatile("" ::: "memory");
      f32x4 pv[8];
#pragma unroll
      for (int j = 0; j < 8; ++j) pv[j] = ldsr<f32x4>(RED + (kl ^ 1) * 8192 + j * 1024);
      SB();
#pragma unroll
      for (int j = 0; j < 4; ++j) {
        f32x16& o = acc[j >> 1];
#pragma unroll
        for (int e = 0; e < 8; ++e) o[(j & 1) * 8 + e] += pv[2 * j + (e >> 2)][e & 3];
        __builtin_amdgcn_raw_buffer_store_b128(pack8(o, j & 1), wsr, lane * 16 + (j & 1) * 3072, j < 2 ? oo0 : oo1, 16);
      }
    }
  } else {
    const int mi = idx & 7, eb = idx >> 3;
    const int xmb = (xcd - 6) * 8 + mi;
    myflag = FX + xmb * 4 + eb;
    if (wid >= 4) {
      const int w4 = wid - 4;
      unsigned xg[4], ug[5];
#pragma unroll
      for (int i = 0; i < 4; ++i) {
        int row = i * 8 + (lane >> 3);
        xg[i] = row * (D_ * 2) + (((lane & 7) ^ ((row >> 1) & 7)) * 16) - i * 1024;
      }
#pragma unroll
      for (int i = 0; i < 5; ++i) {
        int off = i * 1024 + lane * 16;
        ug[i] = (off / 320) * (D_ * 2) + off % 320 - (i & 3) * 1024;
      }
      const char* xb = (const char*)x + (size_t)(xmb * 128 + w4 * 32) * (D_ * 2);
      const char* ub = (const char*)U + eb * 320 + (size_t)(w4 * 16) * (D_ * 2);
      int ks = mi + ((eb * 5 + 1) >> 1);
      if (ks >= 10) ks -= 10;
      auto issue = [&](int st) {
        unsigned base = lb + (st & 3) * 36864;
        const char* xp = xb + ks * 128;
        const char* up = ub + (size_t)ks * 64 * (D_ * 2);
        set_m0(base + w4 * 4096);
        glds<0>(xp, xg[0]); glds<1024>(xp, xg[1]); glds<2048>(xp, xg[2]); glds<3072>(xp, xg[3]);
        set_m0(base + 16384 + w4 * 5120);
        glds<0>(up, ug[0]); glds<1024>(up, ug[1]); glds<2048>(up, ug[2]); glds<3072>(up, ug[3]);
        set_m0(base + 16384 + w4 * 5120 + 4096);
        glds<0>(up, ug[4]);
        if (++ks == 10) ks = 0;
      };
      issue(0);
      if (XTOUCH) {
        set_m0(lb + L_DUMP);
        int kt = ks + (w4 >> 1);
        if (kt >= 10) kt -= 10;
        if (w4 < 2 || !(eb & 1)) touch((const char*)x + (size_t)(xmb * 128 + (w4 & 1) * 64) * (D_ * 2) + kt * 128, lane * (D_ * 2));
        if (w4 == 3 && mi < 6) {
          int ku = 8 + mi / 3 + ((eb * 5 + 1) >> 1);
          if (ku >= 10) ku -= 10;
          touch((const char*)U + (size_t)ku * 64 * (D_ * 2) + ((eb * 320) & ~127) + (mi % 3) * 128, lane * (D_ * 2));
        }
      }
#pragma unroll 1
      for (int st = 1; st < 10; ++st) {
        issue(st);
        WAIT_V(9);
        BAR();
      }
      WAIT_V(0);
      BAR();
    } else {
      int xoff[4];
#pragma unroll
      for (int s = 0; s < 4; ++s) xoff[s] = (wid * 32 + r) * 128 + (((2 * s + h) ^ ((r >> 1) & 7)) * 16);
      const int uoff = 16384 + (8 * h + q4) * 320 + blk * 32 + p4 * 8;
      f32x16 acc[5] = {};
      u32x4 xf[2], uf[2][5];
      auto rdu = [&](lds_char* base, int s, int buf, int c) {
        uf[buf][c] = trread2(base + uoff + c * 64 + (16 * s) * 320, base + uoff + c * 64 + (16 * s + 4) * 320);
      };
      if (wid == 3) put_token(__hip_atomic_load(sync, RLX_AGENT));
      WAIT_L(0);
      BAR();
      xf[0] = ldsr<u32x4>(smem + xoff[0]);
#pragma unroll
      for (int c = 0; c < 5; ++c) rdu(smem, 0, 0, c);
#pragma unroll 1
      for (int st = 0; st < 10; ++st) {
#pragma unroll
        for (int s = 0; s < 4; ++s) {
          lds_char* base = smem + ((s < 3 ? st : st + 1) & 3) * 36864;
          const int nx = (s + 1) & 3, nb_ = (s + 1) & 1;
#pragma unroll
          for (int c = 0; c < 5; ++c) {
            acc[c] = mfma32(uf[s & 1][c], xf[s & 1], acc[c]);
            SB();
            if (c == 0) {
              if (s == 3 && st < 9) { BAR(); asm volatile("" ::: "memory"); }
              xf[nb_] = ldsr<u32x4>(base + xoff[nx]);
              rdu(base, nx, nb_, 0);
            } else if (c < 4) {
              rdu(base, nx, nb_, c);
              if (c == 3) rdu(base, nx, nb_, 4);
            }
            SB();
          }
        }
      }
#pragma unroll
      for (int c = 0; c < 5; ++c) {
        const unsigned o = XRF_OFF + ((xmb * 4 + wid) * 40 + (eb * 5 + c) * 2) * 1024;
        __builtin_amdgcn_raw_buffer_store_b128(pack8(acc[c], 0), wsr, lane * 16, o, 16);
        __builtin_amdgcn_raw_buffer_store_b128(pack8(acc[c], 1), wsr, lane * 16 + 1024, o, 16);
      }
      if (wid == 0) STAMP(3);
    }
  }

  const int mb = YPART == 0 ? (xcd >> 1) * 4 + (idx & 3) : YPART == 1 ? (xcd >> 2) * 8 + (idx & 7) : idx & 15;
  const int nb = YPART == 0 ? (xcd & 1) * 8 + (idx >> 2) : YPART == 1 ? (xcd & 3) * 4 + (idx >> 3) : xcd * 2 + (idx >> 4);
  const unsigned vo = lane * 16;
  const char* xsrc = ws + XRF_OFF + (mb * 4 + (wid & 3)) * 40960;
  auto ldx = [&](int ch) {
    asm volatile("s_mov_b32 m0, %2\n\ts_nop 0\n\tglobal_load_lds_dwordx4 %0, %1 sc1\n\tglobal_load_lds_dwordx4 %0, %1 offset:1024 sc1\n\t"
                 "global_load_lds_dwordx4 %0, %1 offset:2048 sc1\n\tglobal_load_lds_dwordx4 %0, %1 offset:3072 sc1"
                 ::"v"(vo), "s"(xsrc + ch * 4096), "s"(lb + (ch % 5) * 28672 + 12288 + (wid & 3) * 4096) : "memory");
  };
  if (wid < 4) {
    if (!is_pf) {
      if (wid == 0) STAMP(10);
      if (lane == 0) __hip_atomic_fetch_add(sw + 8, 1u, RLX_WG);
    }
    lds_spin(sw + 2, 1);
    const u64 tok = (u64)sw[0] | ((u64)sw[1] << 32);
    if (!is_pf) {
      asm volatile("s_waitcnt vmcnt(0) lgkmcnt(0)" ::: "memory");
      if (lane == 0) {
        if (__hip_atomic_fetch_add(sw + 3, 1u, RLX_WG) == 3) __hip_atomic_store(sync + myflag * FS, tok, RLX_AGENT);
      }
      if (wid == 0) STAMP(2);
    }
    if (wid == 0) {
      lds_spin(sw + 3, 4);
      if (TGATE) lds_spin(sw + 4, 1);
      const int n0 = (nb * 3) >> 2, n1 = (nb * 3 + 2) >> 2;
      poll_flags(sync + (FT + n0 * 10 + min(lane, (n1 - n0) * 10 + 9)) * FS, tok);
      if (lane == 0) __hip_atomic_store(sw + 5, 1u, RLX_WG);
      STAMP(4);
    } else {
      lds_spin(sw + 5, 1);
    }
    const char* tsrc = ws + nb * 122880 + wid * 3072;
    auto ldt = [&](int ch) {
      asm volatile("s_mov_b32 m0, %2\n\ts_nop 0\n\tglobal_load_lds_dwordx4 %0, %1 sc1\n\tglobal_load_lds_dwordx4 %0, %1 offset:1024 sc1\n\t"
                   "global_load_lds_dwordx4 %0, %1 offset:2048 sc1" ::"v"(vo), "s"(tsrc + ch * 12288), "s"(lb + (ch % 5) * 28672 + wid * 3072) : "memory");
    };
    ldt(0); ldt(1); ldt(2);
    WAIT_V(6); BAR(); ldt(3);
    WAIT_V(6); BAR(); ldt(4);
    WAIT_V(6); BAR(); ldt(5); ldx(5);
    WAIT_V(10); BAR(); ldt(6); ldx(6);
    WAIT_V(14); BAR(); ldt(7); ldx(7);
    WAIT_V(14); BAR(); ldt(8); ldx(8);
    WAIT_V(14); BAR(); ldt(9); ldx(9);
    WAIT_V(14); BAR();
    WAIT_V(7); BAR();
    WAIT_V(0); BAR();
  } else {
    const int w4 = wid - 4;
    if (wid == 4) {
      lds_spin(sw + 2, 1);
      const u64 tok = (u64)sw[0] | ((u64)sw[1] << 32);
      poll_flags(sync + (XRV ? FX + (mb >> 1) * 5 + min(lane, 4) : FX + mb * 4 + (lane & 3)) * FS, tok);
      lds_spin(sw + 8, 4);
      if (lane == 0) __hip_atomic_store(sw + 4, 1u, RLX_WG);
      STAMP(8);
    } else {
      lds_spin(sw + 4, 1);
    }
#pragma unroll
    for (int ch = 0; ch < 5; ++ch) ldx(ch);
    f32x16 acc[3] = {};
    u32x4 tf[3][3], xf[3];
    lds_char* tb = smem + lane * 16;
    lds_char* xb = smem + lane * 16 + 12288 + w4 * 4096;
    auto rdf = [&](int ks, int c) {
      const int o = ((ks >> 2) % 5) * 28672;
      if (c < 3) tf[ks % 3][c] = ldsr<u32x4>(tb + o + ((ks & 3) * 3 + c) * 1024);
      else xf[ks % 3] = ldsr<u32x4>(xb + o + (ks & 3) * 1024);
    };
    WAIT_V(16);
    BAR();
#pragma unroll
    for (int c = 0; c < 8; ++c) rdf(c >> 2, 3 - (c & 3));
#pragma unroll
    for (int ks = 0; ks < 40; ++ks) {
#pragma unroll
      for (int c = 0; c < 3; ++c) {
        acc[c] = mfma32(tf[ks % 3][c], xf[ks % 3], acc[c]);
        SB();
        if (ks < 38) {
          if (c == 0) {
            if ((ks & 3) == 2) {
              if (ks == 2) WAIT_V(12);
              if (ks == 6) WAIT_V(8);
              if (ks == 10) WAIT_V(4);
              if (ks == 14) WAIT_V(0);
              BAR(); asm volatile("" ::: "memory");
            }
            rdf(ks + 2, 3);
          }
          rdf(ks + 2, c);
        }
        SB();
      }
    }
    if (wid == 4) STAMP(5);
    lds_char* eb = smem + (YV ? 0 : 122880) + w4 * 6656;
#pragma unroll
    for (int c = 0; c < 3; ++c) {
      ldsw<u32x4>(eb + r * 208 + c * 64 + h * 32, pack8(acc[c], 0));
      ldsw<u32x4>(eb + r * 208 + c * 64 + h * 32 + 16, pack8(acc[c], 1));
    }
    const __amdgpu_buffer_rsrc_t yr = __builtin_amdgcn_make_buffer_rsrc((void*)y, 0, 2048 * F_ * 2, 0x00020000);
    const unsigned yo = ((mb * 128 + w4 * 32) * F_ + nb * 96) * 2;
#pragma unroll
    for (int i = 0; i < 6; ++i) {
      int q = i * 64 + lane, row = q / 12, chn = q % 12;
      __builtin_amdgcn_raw_buffer_store_b128(ldsr<u32x4>(eb + row * 208 + chn * 16), yr, row * (F_ * 2) + chn * 16, yo, Y_AUX);
    }
  }
}

extern "C" void kernel_launch(void* const* d_in, const int* in_sizes, int n_in, void* d_out, int out_size, void* d_ws,
                              size_t ws_size, hipStream_t stream) {
  (void)hipFuncSetAttribute((const void*)quip, hipFuncAttributeMaxDynamicSharedMemorySize, LDS_BYTES);
  quip<<<256, 512, LDS_BYTES, stream>>>((const __hip_bfloat16*)d_in[0], (const int*)d_in[1], (const __hip_bfloat16*)d_in[2],
                                        (const __hip_bfloat16*)d_in[3], (const __hip_bfloat16*)d_in[4], (const __hip_bfloat16*)d_in[5],
                                        (char*)d_ws, (__hip_bfloat16*)d_out);
}
```

```cpp
#define AMP 0
#define AMP_BLK 121
#define AMPF 3
#include <hip/hip_runtime.h>
#include <hip/hip_bf16.h>

typedef __attribute__((ext_vector_type(8))) __bf16 bf16x8;
typedef __attribute__((ext_vector_type(2))) __bf16 bf16x2;
typedef __attribute__((ext_vector_type(4))) short s16x4;
typedef __attribute__((ext_vector_type(16))) float f32x16;
typedef __attribute__((ext_vector_type(4))) float f32x4;
typedef __attribute__((ext_vector_type(4))) unsigned u32x4;
typedef __attribute__((ext_vector_type(2))) unsigned u32x2;
typedef __attribute__((address_space(3))) s16x4 lds_s16x4;
typedef __attribute__((address_space(3))) char lds_char;
typedef __attribute__((address_space(3))) unsigned lds_u32;
typedef __attribute__((address_space(1))) unsigned long long gu64;
typedef unsigned long long u64;

#define STAMP(i) do { if ((threadIdx.x & 63) == 0) sw[16 + (i)] = (unsigned)__builtin_amdgcn_s_memrealtime(); } while (0)
#define Y_AUX 2
#define XCD_PERM 0x54321706
#define YPART 1
#define XRV 0
#define YV 0
#define TGATE 0
#define XTOUCH 0
#define POLL_SLEEP 4
constexpr int D_ = 640, F_ = 1536;

#define WAIT_V(n) asm volatile("s_waitcnt vmcnt(" #n ")" ::: "memory")
#define WAIT_L(n) asm volatile("s_waitcnt lgkmcnt(" #n ")" ::: "memory")
#define BAR() __builtin_amdgcn_s_barrier()
#define SB() __builtin_amdgcn_sched_barrier(0)
#define RLX_AGENT __ATOMIC_RELAXED, __HIP_MEMORY_SCOPE_AGENT
#define RLX_WG __ATOMIC_RELAXED, __HIP_MEMORY_SCOPE_WORKGROUP

__device__ __forceinline__ void set_m0(unsigned ldsaddr) { asm volatile("s_mov_b32 m0, %0\n\ts_nop 0" ::"s"(ldsaddr) : "memory"); }
template <int OFF> __device__ __forceinline__ void glds(const void* sbase, unsigned voff) {
  asm volatile("global_load_lds_dwordx4 %0, %1 offset:%2" ::"v"(voff), "s"(sbase), "i"(OFF) : "memory");
}
__device__ __forceinline__ void touch(const void* sbase, unsigned voff) {
  asm volatile("global_load_lds_dword %0, %1" ::"v"(voff), "s"(sbase) : "memory");
}
__device__ __forceinline__ unsigned pack2(float a, float b) {
  bf16x2 p; p[0] = (__bf16)a; p[1] = (__bf16)b;
  return __builtin_bit_cast(unsigned, p);
}
__device__ __forceinline__ float bflo(unsigned u) { return __builtin_bit_cast(float, u << 16); }
__device__ __forceinline__ float bfhi(unsigned u) { return __builtin_bit_cast(float, u & 0xffff0000u); }
__device__ __forceinline__ u32x4 trread2(lds_char* p0, lds_char* p1) {
  u32x2 lo = __builtin_bit_cast(u32x2, __builtin_amdgcn_ds_read_tr16_b64_v4i16((lds_s16x4*)p0));
  u32x2 hi = __builtin_bit_cast(u32x2, __builtin_amdgcn_ds_read_tr16_b64_v4i16((lds_s16x4*)p1));
  return u32x4{lo[0], lo[1], hi[0], hi[1]};
}
template <typename T> __device__ __forceinline__ T ldsr(lds_char* p) { return *(__attribute__((address_space(3))) T*)p; }
template <typename T> __device__ __forceinline__ void ldsw(lds_char* p, T v) { *(__attribute__((address_space(3))) T*)p = v; }
__device__ __forceinline__ f32x16 mfma32(u32x4 a, u32x4 b, f32x16 c) {
  return __builtin_amdgcn_mfma_f32_32x32x16_bf16(__builtin_bit_cast(bf16x8, a), __builtin_bit_cast(bf16x8, b), c, 0, 0, 0);
}
__device__ __forceinline__ u32x4 pack8(const f32x16& a, int s) {
  u32x4 r;
  r[0] = pack2(a[8 * s + 0], a[8 * s + 1]); r[1] = pack2(a[8 * s + 2], a[8 * s + 3]);
  r[2] = pack2(a[8 * s + 4], a[8 * s + 5]); r[3] = pack2(a[8 * s + 6], a[8 * s + 7]);
  return r;
}
__device__ __forceinline__ u32x2 deq4(u32x4 q, f32x4 s, f32x4 z) {
  float w0, w1, w2, w3;
  asm("v_cvt_f32_i32 %0, %4\n\tv_cvt_f32_i32 %1, %5\n\tv_cvt_f32_i32 %2, %6\n\tv_cvt_f32_i32 %3, %7\n\t"
      "v_fma_f32 %0, %0, %8, %12\n\tv_fma_f32 %1, %1, %9, %13\n\tv_fma_f32 %2, %2, %10, %14\n\tv_fma_f32 %3, %3, %11, %15\n\t"
      "v_cvt_pk_bf16_f32 %0, %0, %1\n\tv_cvt_pk_bf16_f32 %1, %2, %3"
      : "=&v"(w0), "=&v"(w1), "=&v"(w2), "=&v"(w3)
      : "v"(q[0]), "v"(q[1]), "v"(q[2]), "v"(q[3]), "v"(s[0]), "v"(s[1]), "v"(s[2]), "v"(s[3]), "v"(z[0]), "v"(z[1]), "v"(z[2]), "v"(z[3]));
  return u32x2{__builtin_bit_cast(unsigned, w0), __builtin_bit_cast(unsigned, w1)};
}
__device__ __forceinline__ void lds_spin(lds_u32* w, unsigned want) {
  while ((unsigned)__builtin_amdgcn_readfirstlane(__hip_atomic_load(w, RLX_WG)) < want) __builtin_amdgcn_s_sleep(1);
  asm volatile("" ::: "memory");
}
__device__ __forceinline__ void poll_flags(gu64* f, u64 tok) {
#pragma unroll 1
  for (unsigned spins = 0; spins < (1u << 20); ++spins) {
    if (__all(__hip_atomic_load(f, RLX_AGENT) == tok)) break;
    __builtin_amdgcn_s_sleep(POLL_SLEEP);
  }
  __builtin_amdgcn_fence(__ATOMIC_ACQUIRE, "wavefront");
}

constexpr int XRF_OFF = 2 << 20, SYNC_OFF = 6 << 20;
#define FS 16
constexpr int FT = 1, FX = 121;
constexpr int FA = 185 * FS;
constexpr int CODE_TOUCH_MAX = 14464;
constexpr int L_SYNC = 156160, L_DUMP = 156416, LDS_BYTES = 156672;

__global__ __launch_bounds__(512, 2) void quip(const __hip_bfloat16* __restrict__ x, const int* __restrict__ qw,
                                               const __hip_bfloat16* __restrict__ scales, const __hip_bfloat16* __restrict__ zeros,
                                               const __hip_bfloat16* __restrict__ U, const __hip_bfloat16* __restrict__ V,
                                               char* ws, __hip_bfloat16* __restrict__ y) {
  extern __shared__ __attribute__((aligned(16))) char smem_[];
  lds_char* smem = (lds_char*)smem_;
  const unsigned lb = (unsigned)(size_t)smem;
  const int wid = __builtin_amdgcn_readfirstlane(threadIdx.x >> 6), lane = threadIdx.x & 63;
  const int xcd = (XCD_PERM >> ((blockIdx.x & 7) * 4)) & 7, idx = blockIdx.x >> 3;
  const int h = lane >> 5, r = lane & 31;
  const int blk = (lane >> 4) & 1, q4 = (lane & 15) >> 2, p4 = lane & 3;
  lds_u32* sw = (lds_u32*)(smem + L_SYNC);
  gu64* sync = (gu64*)(ws + SYNC_OFF);
  const __amdgpu_buffer_rsrc_t wsr = __builtin_amdgcn_make_buffer_rsrc(ws, 0, SYNC_OFF, 0x00020000);
  const bool is_pf = idx < 12 && (XRV == 1 || xcd < 6);
  if (wid == 0) STAMP(0);

  auto put_token = [&](u64 epoch) {
    if (wid == 3 && lane == 0) {
      const u64 e = epoch + 0x9E3779B97F4A7C15ull;
      sw[0] = (unsigned)e; sw[1] = (unsigned)(e >> 32);
      __hip_atomic_store(sw + 2, 1u, RLX_WG);
      __hip_atomic_store(sync + FA + blockIdx.x, e, RLX_AGENT);
    }
  };
  if (wid == 3) {
    if (lane < 16) sw[lane] = ((lane == 3 || lane == 8) && is_pf) ? 4u : 0u;
  } else if (wid == 1 && idx < 2) {
    u64 pc;
    asm volatile("s_getpc_b64 %0" : "=s"(pc));
    set_m0(lb + L_DUMP);
    touch((const char*)pc, min(idx * 8192 + lane * 128, CODE_TOUCH_MAX));
  }
  int myflag = 0;

  if (is_pf) {
    if (wid == 0) {
      set_m0(lb + L_DUMP);
      const bool tg = xcd < 6;
      const unsigned ao = tg ? (lane >> 1) * (F_ * 4) + (lane & 1) * 128 : lane * (D_ * 2);
      const unsigned bo = tg ? (lane >> 3) * (F_ * 2) + (lane & 7) * 128 : lane * 128;
      const char* ab = tg ? (const char*)qw + (size_t)((xcd & 1) * 320) * (F_ * 4) : (const char*)x + (size_t)((xcd - 6) * 1024) * (D_ * 2);
      const char* bb = tg ? (const char*)V + (xcd >> 1) * 1024 : (const char*)U;
      const int na = tg ? 10 : 16, nt_ = tg ? 18 : 26, np = tg ? 24 : 10;
      const int aj = tg ? 32 * F_ * 4 : 64 * D_ * 2, ak = tg ? 256 : 128;
      const int bj = tg ? 8 * F_ * 2 : 8192, bk = tg ? 64 * F_ * 2 : 64 * D_ * 2;
      int p = 0, j = idx, n = 0;
      __builtin_amdgcn_s_waitcnt(0x0F70);
#pragma unroll 1
      while (p < np) {
        if (j < na) touch(ab + j * aj + p * ak, ao);
        else touch(bb + (j - na) * bj + p * bk, bo);
        j += 12;
        if (j >= nt_) { j -= nt_; ++p; }
        if (++n == 3) BAR();
      }
      STAMP(7);
    } else {
      WAIT_L(0);
      BAR();
      if (wid == 3) put_token(__hip_atomic_load(sync, RLX_AGENT));
      if (blockIdx.x == 1 && wid == 2) {
        lds_spin(sw + 2, 1);
        const u64 tok = (u64)sw[0] | ((u64)sw[1] << 32);
#pragma unroll 1
        for (int i = 0; i < 4; ++i) poll_flags(sync + FA + i * 64 + lane, tok);
        if (lane == 0) __hip_atomic_store(sync, tok, RLX_AGENT);
      }
    }
  } else if (xcd < 6) {
    const int t = idx - 12;
    const int mt = (xcd & 1) * 5 + t % 5, nt = (xcd >> 1) * 4 + t / 5;
    myflag = FT + nt * 10 + mt;
    if (wid >= 4) {
      const int w4 = wid - 4, rr = lane >> 4, c16 = lane & 15;
      unsigned ag[4], bg[4];
#pragma unroll
      for (int i = 0; i < 4; ++i) {
        int row = i * 4 + rr;
        ag[i] = row * (F_ * 4) + ((c16 ^ row) * 16) - i * 1024;
        bg[i] = row * (F_ * 2) + ((c16 ^ (rr << 2)) * 16) - i * 1024;
      }
      const char* ap = (const char*)qw + (size_t)(mt * 64 + w4 * 16) * (F_ * 4);
      const char* bp = (const char*)V + nt * 256 + (size_t)(w4 * 16) * (F_ * 2);
      unsigned so = lb + w4 * 4096;
      auto issue = [&](int p) {
        unsigned base = so + (p & 3) * 32768;
        set_m0(base);
        glds<0>(ap, ag[0]); glds<1024>(ap, ag[1]); glds<2048>(ap, ag[2]); glds<3072>(ap, ag[3]);
        set_m0(base + 16384);
        glds<0>(bp, bg[0]); glds<1024>(bp, bg[1]); glds<2048>(bp, bg[2]); glds<3072>(bp, bg[3]);
        ap += 256; bp += 64 * F_ * 2;
      };
      issue(0);
      issue(1);
      BAR();
#pragma unroll 1
      for (int p = 2; p < 24; ++p) {
        WAIT_V(8);
        BAR();
        issue(p);
      }
      WAIT_V(8);
      BAR();
      WAIT_V(0);
      BAR();
    } else {
      const int m = wid & 1, kl = wid >> 1;
      {
        const size_t o = (size_t)((mt * 2 + m) * F_ + kl * 768) * 2;
        lds_char* T = smem + 131072 + m * 12288 + kl * 3072;
        u32x4 s8[2], z8[2];
#pragma unroll
        for (int j = 0; j < 2; ++j) {
          int e = j ? 64 + (lane & 31) : lane;
          s8[j] = *(const u32x4*)((const char*)scales + o + e * 16);
          z8[j] = *(const u32x4*)((const char*)zeros + o + e * 16);
        }
        const u64 epoch = __hip_atomic_load(sync, RLX_AGENT);
#pragma unroll
        for (int j = 0; j < 2; ++j) {
          int e = j ? 64 + (lane & 31) : lane;
          f32x4 sa, sb, za, zb;
          sa[0] = bflo(s8[j][0]); sa[1] = bfhi(s8[j][0]); sa[2] = bflo(s8[j][1]); sa[3] = bfhi(s8[j][1]);
          sb[0] = bflo(s8[j][2]); sb[1] = bfhi(s8[j][2]); sb[2] = bflo(s8[j][3]); sb[3] = bfhi(s8[j][3]);
          za[0] = -bflo(z8[j][0]) * sa[0]; za[1] = -bfhi(z8[j][0]) * sa[1]; za[2] = -bflo(z8[j][1]) * sa[2]; za[3] = -bfhi(z8[j][1]) * sa[3];
          zb[0] = -bflo(z8[j][2]) * sb[0]; zb[1] = -bfhi(z8[j][2]) * sb[1]; zb[2] = -bflo(z8[j][3]) * sb[2]; zb[3] = -bfhi(z8[j][3]) * sb[3];
          ldsw<f32x4>(T + e * 32, sa); ldsw<f32x4>(T + e * 32 + 16, sb);
          ldsw<f32x4>(T + 6144 + e * 32, za); ldsw<f32x4>(T + 6144 + e * 32 + 16, zb);
        }
        put_token(epoch);
        WAIT_L(0);
        BAR();
      }
      int aoff[4], boff[4];
#pragma unroll
      for (int c = 0; c < 4; ++c) {
        int ch = 8 * kl + 4 * (c >> 1) + 2 * h + (c & 1);
        aoff[c] = (m * 32 + r) * 256 + ((ch ^ (r & 15)) * 16);
        boff[c] = 16384 + (32 * kl + 8 * h + q4) * 256 + ((c ^ (2 * kl) ^ q4) * 64) + 32 * (p4 & 1) + 16 * blk + 8 * (p4 >> 1);
      }
      lds_char* tp = smem + 131072 + m * 12288 + (32 * kl + 8 * h) * 4;
      f32x16 acc[4] = {};
      f32x4 ts[4], tz[4];
      u32x4 av[4];
      u32x4 af[2][2] = {}, bf[2][2][4] = {};
      auto rdtab = [&](int o, int c) {
        ts[c] = ldsr<f32x4>(tp + o + (c >> 1) * 64 + (c & 1) * 16);
        tz[c] = ldsr<f32x4>(tp + o + 6144 + (c >> 1) * 64 + (c & 1) * 16);
      };
      auto step = [&](int slot) {
        const int buf = slot & 1;
        lds_char* sl = smem + slot * 32768;
        BAR();
#pragma unroll
        for (int c = 0; c < 4; ++c) av[c] = ldsr<u32x4>(sl + aoff[c]);
        SB();
#pragma unroll
        for (int u = 0; u < 8; ++u) {
          acc[u & 3] = mfma32(af[buf ^ 1][u >> 2], bf[buf ^ 1][u >> 2][u & 3], acc[u & 3]);
          SB();
          if (u < 4) {
#pragma unroll
            for (int v = 2 * u; v < 2 * u + 2; ++v)
              bf[buf][v >> 2][v & 3] = trread2(sl + boff[v & 3] + (16 * (v >> 2)) * 256, sl + boff[v & 3] + (16 * (v >> 2) + 4) * 256);
          } else {
            const int c = u - 4;
            u32x2 o = deq4(av[c], ts[c], tz[c]);
            af[buf][c >> 1][(c & 1) * 2] = o[0]; af[buf][c >> 1][(c & 1) * 2 + 1] = o[1];
            SB();
            rdtab((slot + 1) * 256, c);
          }
          SB();
        }
      };
#pragma unroll
      for (int c = 0; c < 4; ++c) rdtab(0, c);
#pragma unroll 1
      for (int p = 0; p < 24; p += 4) {
        step(0); step(1); step(2); step(3);
        tp += 1024;
      }
      if (wid == 0) STAMP(1);
      lds_char* RED = smem + m * 16384 + lane * 16;
      const int n32 = nt * 4 + kl * 2;
      const unsigned oo0 = (((n32 / 3) * 40 + (mt * 2 + m) * 2) * 3 + n32 % 3) * 1024;
      const unsigned oo1 = ((((n32 + 1) / 3) * 40 + (mt * 2 + m) * 2) * 3 + (n32 + 1) % 3) * 1024;
      SB();
#pragma unroll
      for (int u = 0; u < 8; ++u) {
        const int c = (u < 4 ? 2 : 0) + ((u >> 1) & 1), s2 = u & 1;
        acc[c] = mfma32(af[1][s2], bf[1][s2][c], acc[c]);
        SB();
        if (u >= 3 && u < 7) {
#pragma unroll
          for (int j = 2 * (u - 3); j < 2 * (u - 3) + 2; ++j) {
            const f32x16& g = acc[2 + (j >> 2)];
            const int q = 4 * (j & 3);
            ldsw<f32x4>(RED + kl * 8192 + j * 1024, f32x4{g[q], g[q + 1], g[q + 2], g[q + 3]});
          }
          SB();
        }
      }
      asm volatile("" ::: "memory");
      __hip_atomic_store(sw + 9 + wid, 1u, RLX_WG);
      while (__builtin_amdgcn_readfirstlane(__hip_atomic_load(sw + 9 + (wid ^ 2), RLX_WG)) == 0) {}
      asm volatile("" ::: "memory");
      f32x4 pv[8];
#pragma unroll
      for (int j = 0; j < 8; ++j) pv[j] = ldsr<f32x4>(RED + (kl ^ 1) * 8192 + j * 1024);
      SB();
#pragma unroll
      for (int j = 0; j < 4; ++j) {
        f32x16& o = acc[j >> 1];
#pragma unroll
        for (int e = 0; e < 8; ++e) o[(j & 1) * 8 + e] += pv[2 * j + (e >> 2)][e & 3];
        __builtin_amdgcn_raw_buffer_store_b128(pack8(o, j & 1), wsr, lane * 16 + (j & 1) * 3072, j < 2 ? oo0 : oo1, 16);
      }
    }
  } else {
    const int mi = idx & 7, eb = idx >> 3;
    const int xmb = (xcd - 6) * 8 + mi;
    myflag = FX + xmb * 4 + eb;
    if (wid >= 4) {
      const int w4 = wid - 4;
      unsigned xg[4], ug[5];
#pragma unroll
      for (int i = 0; i < 4; ++i) {
        int row = i * 8 + (lane >> 3);
        xg[i] = row * (D_ * 2) + (((lane & 7) ^ ((row >> 1) & 7)) * 16) - i * 1024;
      }
#pragma unroll
      for (int i = 0; i < 5; ++i) {
        int off = i * 1024 + lane * 16;
        ug[i] = (off / 320) * (D_ * 2) + off % 320 - (i & 3) * 1024;
      }
      const char* xb = (const char*)x + (size_t)(xmb * 128 + w4 * 32) * (D_ * 2);
      const char* ub = (const char*)U + eb * 320 + (size_t)(w4 * 16) * (D_ * 2);
      int ks = mi + ((eb * 5 + 1) >> 1);
      if (ks >= 10) ks -= 10;
      auto issue = [&](int st) {
        unsigned base = lb + (st & 3) * 36864;
        const char* xp = xb + ks * 128;
        const char* up = ub + (size_t)ks * 64 * (D_ * 2);
        set_m0(base + w4 * 4096);
        glds<0>(xp, xg[0]); glds<1024>(xp, xg[1]); glds<2048>(xp, xg[2]); glds<3072>(xp, xg[3]);
        set_m0(base + 16384 + w4 * 5120);
        glds<0>(up, ug[0]); glds<1024>(up, ug[1]); glds<2048>(up, ug[2]); glds<3072>(up, ug[3]);
        set_m0(base + 16384 + w4 * 5120 + 4096);
        glds<0>(up, ug[4]);
        if (++ks == 10) ks = 0;
      };
      issue(0);
      if (XTOUCH) {
        set_m0(lb + L_DUMP);
        int kt = ks + (w4 >> 1);
        if (kt >= 10) kt -= 10;
        if (w4 < 2 || !(eb & 1)) touch((const char*)x + (size_t)(xmb * 128 + (w4 & 1) * 64) * (D_ * 2) + kt * 128, lane * (D_ * 2));
        if (w4 == 3 && mi < 6) {
          int ku = 8 + mi / 3 + ((eb * 5 + 1) >> 1);
          if (ku >= 10) ku -= 10;
          touch((const char*)U + (size_t)ku * 64 * (D_ * 2) + ((eb * 320) & ~127) + (mi % 3) * 128, lane * (D_ * 2));
        }
      }
#pragma unroll 1
      for (int st = 1; st < 10; ++st) {
        issue(st);
        WAIT_V(9);
        BAR();
      }
      WAIT_V(0);
      BAR();
    } else {
      int xoff[4];
#pragma unroll
      for (int s = 0; s < 4; ++s) xoff[s] = (wid * 32 + r) * 128 + (((2 * s + h) ^ ((r >> 1) & 7)) * 16);
      const int uoff = 16384 + (8 * h + q4) * 320 + blk * 32 + p4 * 8;
      f32x16 acc[5] = {};
      u32x4 xf[2], uf[2][5];
      auto rdu = [&](lds_char* base, int s, int buf, int c) {
        uf[buf][c] = trread2(base + uoff + c * 64 + (16 * s) * 320, base + uoff + c * 64 + (16 * s + 4) * 320);
      };
      if (wid == 3) put_token(__hip_atomic_load(sync, RLX_AGENT));
      WAIT_L(0);
      BAR();
      xf[0] = ldsr<u32x4>(smem + xoff[0]);
#pragma unroll
      for (int c = 0; c < 5; ++c) rdu(smem, 0, 0, c);
#pragma unroll 1
      for (int st = 0; st < 10; ++st) {
#pragma unroll
        for (int s = 0; s < 4; ++s) {
          lds_char* base = smem + ((s < 3 ? st : st + 1) & 3) * 36864;
          const int nx = (s + 1) & 3, nb_ = (s + 1) & 1;
#pragma unroll
          for (int c = 0; c < 5; ++c) {
            acc[c] = mfma32(uf[s & 1][c], xf[s & 1], acc[c]);
            SB();
            if (c == 0) {
              if (s == 3 && st < 9) { BAR(); asm volatile("" ::: "memory"); }
              xf[nb_] = ldsr<u32x4>(base + xoff[nx]);
              rdu(base, nx, nb_, 0);
            } else if (c < 4) {
              rdu(base, nx, nb_, c);
              if (c == 3) rdu(base, nx, nb_, 4);
            }
            SB();
          }
        }
      }
#pragma unroll
      for (int c = 0; c < 5; ++c) {
        const unsigned o = XRF_OFF + ((xmb * 4 + wid) * 40 + (eb * 5 + c) * 2) * 1024;
        __builtin_amdgcn_raw_buffer_store_b128(pack8(acc[c], 0), wsr, lane * 16, o, 16);
        __builtin_amdgcn_raw_buffer_store_b128(pack8(acc[c], 1), wsr, lane * 16 + 1024, o, 16);
      }
      if (wid == 0) STAMP(3);
    }
  }

  const int mb = YPART == 0 ? (xcd >> 1) * 4 + (idx & 3) : YPART == 1 ? (xcd >> 2) * 8 + (idx & 7) : idx & 15;
  const int nb = YPART == 0 ? (xcd & 1) * 8 + (idx >> 2) : YPART == 1 ? (xcd & 3) * 4 + (idx >> 3) : xcd * 2 + (idx >> 4);
  const unsigned vo = lane * 16;
  const char* xsrc = ws + XRF_OFF + (mb * 4 + (wid & 3)) * 40960;
  auto ldx = [&](int ch) {
    asm volatile("s_mov_b32 m0, %2\n\ts_nop 0\n\tglobal_load_lds_dwordx4 %0, %1 sc1\n\tglobal_load_lds_dwordx4 %0, %1 offset:1024 sc1\n\t"
                 "global_load_lds_dwordx4 %0, %1 offset:2048 sc1\n\tglobal_load_lds_dwordx4 %0, %1 offset:3072 sc1"
                 ::"v"(vo), "s"(xsrc + ch * 4096), "s"(lb + (ch % 5) * 28672 + 12288 + (wid & 3) * 4096) : "memory");
  };
  if (wid < 4) {
    if (!is_pf) {
      if (wid == 0) STAMP(10);
      if (lane == 0) __hip_atomic_fetch_add(sw + 8, 1u, RLX_WG);
    }
    lds_spin(sw + 2, 1);
    const u64 tok = (u64)sw[0] | ((u64)sw[1] << 32);
    if (!is_pf) {
      asm volatile("s_waitcnt vmcnt(0) lgkmcnt(0)" ::: "memory");
      if (lane == 0) {
        if (__hip_atomic_fetch_add(sw + 3, 1u, RLX_WG) == 3) __hip_atomic_store(sync + myflag * FS, tok, RLX_AGENT);
      }
      if (wid == 0) STAMP(2);
    }
    if (wid == 0) {
      lds_spin(sw + 3, 4);
      if (TGATE) lds_spin(sw + 4, 1);
      const int n0 = (nb * 3) >> 2, n1 = (nb * 3 + 2) >> 2;
      poll_flags(sync + (FT + n0 * 10 + min(lane, (n1 - n0) * 10 + 9)) * FS, tok);
      if (lane == 0) __hip_atomic_store(sw + 5, 1u, RLX_WG);
      STAMP(4);
    } else {
      lds_spin(sw + 5, 1);
    }
    const char* tsrc = ws + nb * 122880 + wid * 3072;
    unsigned tdst = lb + wid * 3072;
    auto issue = [&]() {
      asm volatile("s_mov_b32 m0, %2\n\ts_nop 0\n\tglobal_load_lds_dwordx4 %0, %1 sc1\n\tglobal_load_lds_dwordx4 %0, %1 offset:1024 sc1\n\t"
                   "global_load_lds_dwordx4 %0, %1 offset:2048 sc1" ::"v"(vo), "s"(tsrc), "s"(tdst) : "memory");
      tsrc += 12288; tdst += 12288;
    };
    issue(); issue(); issue(); issue();
#pragma unroll 1
    for (int ch = 0; ch < 6; ++ch) {
      WAIT_V(9);
      BAR();
      issue();
    }
    WAIT_V(9); BAR(); WAIT_V(6); BAR(); WAIT_V(3); BAR(); WAIT_V(0); BAR();
  } else {
    const int w4 = wid - 4;
    if (wid == 4) {
      lds_spin(sw + 2, 1);
      const u64 tok = (u64)sw[0] | ((u64)sw[1] << 32);
      poll_flags(sync + (XRV ? FX + (mb >> 1) * 5 + min(lane, 4) : FX + mb * 4 + (lane & 3)) * FS, tok);
      lds_spin(sw + 8, 4);
      if (lane == 0) __hip_atomic_store(sw + 4, 1u, RLX_WG);
      STAMP(8);
    } else {
      lds_spin(sw + 4, 1);
    }
    u32x4 xf[40];
    const unsigned xo = XRF_OFF + (mb * 4 + w4) * 40960;
#pragma unroll
    for (int ch = 0; ch < 10; ++ch) {
#pragma unroll
      for (int i = 0; i < 4; ++i) xf[ch * 4 + i] = __builtin_amdgcn_raw_buffer_load_b128(wsr, lane * 16 + i * 1024, xo + ch * 4096, 16);
    }
    SB();
    if (wid == 4) STAMP(11);
    f32x16 acc[3] = {};
    u32x4 tf[3][3];
    lds_char* tb = smem + lane * 16;
    BAR();
    if (wid == 4) STAMP(9);
#pragma unroll
    for (int c = 0; c < 6; ++c) tf[c / 3][c % 3] = ldsr<u32x4>(tb + c * 1024);
#pragma unroll
    for (int ks = 0; ks < 40; ++ks) {
#pragma unroll
      for (int c = 0; c < 3; ++c) {
        acc[c] = mfma32(tf[ks % 3][c], xf[ks], acc[c]);
        SB();
        if (ks < 38) {
          if (c == 0 && (ks & 3) == 2) { BAR(); asm volatile("" ::: "memory"); }
          tf[(ks + 2) % 3][c] = ldsr<u32x4>(tb + ((ks + 2) * 3 + c) * 1024);
        }
        SB();
      }
    }
    if (wid == 4) STAMP(5);
    lds_char* eb = smem + (YV ? 0 : 122880) + w4 * 6656;
#pragma unroll
    for (int c = 0; c < 3; ++c) {
      ldsw<u32x4>(eb + r * 208 + c * 64 + h * 32, pack8(acc[c], 0));
      ldsw<u32x4>(eb + r * 208 + c * 64 + h * 32 + 16, pack8(acc[c], 1));
    }
    const __amdgpu_buffer_rsrc_t yr = __builtin_amdgcn_make_buffer_rsrc((void*)y, 0, 2048 * F_ * 2, 0x00020000);
    const unsigned yo = ((mb * 128 + w4 * 32) * F_ + nb * 96) * 2;
#pragma unroll
    for (int i = 0; i < 6; ++i) {
      int q = i * 64 + lane, row = q / 12, chn = q % 12;
      __builtin_amdgcn_raw_buffer_store_b128(ldsr<u32x4>(eb + row * 208 + chn * 16), yr, row * (F_ * 2) + chn * 16, yo, Y_AUX);
    }
    WAIT_V(0);
    if (wid == 4) STAMP(6);
    if (wid == 4 && blockIdx.x == AMP_BLK) {
      WAIT_L(0);
      unsigned d = __builtin_amdgcn_readfirstlane(sw[16 + AMP] - sw[16]);
      unsigned t0 = (unsigned)__builtin_amdgcn_s_memrealtime();
      while ((unsigned)__builtin_amdgcn_s_memrealtime() - t0 < AMPF * d + 300) __builtin_amdgcn_s_sleep(1);
    }
  }
}

extern "C" void kernel_launch(void* const* d_in, const int* in_sizes, int n_in, void* d_out, int out_size, void* d_ws,
                              size_t ws_size, hipStream_t stream) {
  (void)hipFuncSetAttribute((const void*)quip, hipFuncAttributeMaxDynamicSharedMemorySize, LDS_BYTES);
  quip<<<256, 512, LDS_BYTES, stream>>>((const __hip_bfloat16*)d_in[0], (const int*)d_in[1], (const __hip_bfloat16*)d_in[2],
                                        (const __hip_bfloat16*)d_in[3], (const __hip_bfloat16*)d_in[4], (const __hip_bfloat16*)d_in[5],
                                        (char*)d_ws, (__hip_bfloat16*)d_out);
}
```

```cpp
#include <hip/hip_runtime.h>
#include <hip/hip_bf16.h>

typedef __attribute__((ext_vector_type(8))) __bf16 bf16x8;
typedef __attribute__((ext_vector_type(2))) __bf16 bf16x2;
typedef __attribute__((ext_vector_type(4))) short s16x4;
typedef __attribute__((ext_vector_type(16))) float f32x16;
typedef __attribute__((ext_vector_type(4))) float f32x4;
typedef __attribute__((ext_vector_type(4))) unsigned u32x4;
typedef __attribute__((ext_vector_type(2))) unsigned u32x2;
typedef __attribute__((address_space(3))) s16x4 lds_s16x4;
typedef __attribute__((address_space(3))) char lds_char;
typedef __attribute__((address_space(3))) unsigned lds_u32;
typedef __attribute__((address_space(1))) unsigned long long gu64;
typedef unsigned long long u64;

#define STAMP(i)
#define Y_AUX 2
#define XCD_PERM 0x76543210
#define YPART 1
#define PFV 1
#define PFW 2
#define XRV 0
#define YV 1
#define TGATE 0
#define XTOUCH 0
#define POLL_SLEEP 4
constexpr int D_ = 640, F_ = 1536;

#define WAIT_V(n) asm volatile("s_waitcnt vmcnt(" #n ")" ::: "memory")
#define WAIT_L(n) asm volatile("s_waitcnt lgkmcnt(" #n ")" ::: "memory")
#define BAR() __builtin_amdgcn_s_barrier()
#define SB() __builtin_amdgcn_sched_barrier(0)
#define RLX_AGENT __ATOMIC_RELAXED, __HIP_MEMORY_SCOPE_AGENT
#define RLX_WG __ATOMIC_RELAXED, __HIP_MEMORY_SCOPE_WORKGROUP

__device__ __forceinline__ void set_m0(unsigned ldsaddr) { asm volatile("s_mov_b32 m0, %0\n\ts_nop 0" ::"s"(ldsaddr) : "memory"); }
template <int OFF> __device__ __forceinline__ void glds(const void* sbase, unsigned voff) {
  asm volatile("global_load_lds_dwordx4 %0, %1 offset:%2" ::"v"(voff), "s"(sbase), "i"(OFF) : "memory");
}
__device__ __forceinline__ void touch(const void* sbase, unsigned voff) {
  asm volatile("global_load_lds_dword %0, %1" ::"v"(voff), "s"(sbase) : "memory");
}
__device__ __forceinline__ unsigned pack2(float a, float b) {
  bf16x2 p; p[0] = (__bf16)a; p[1] = (__bf16)b;
  return __builtin_bit_cast(unsigned, p);
}
__device__ __forceinline__ float bflo(unsigned u) { return __builtin_bit_cast(float, u << 16); }
__device__ __forceinline__ float bfhi(unsigned u) { return __builtin_bit_cast(float, u & 0xffff0000u); }
__device__ __forceinline__ u32x4 trread2(lds_char* p0, lds_char* p1) {
  u32x2 lo = __builtin_bit_cast(u32x2, __builtin_amdgcn_ds_read_tr16_b64_v4i16((lds_s16x4*)p0));
  u32x2 hi = __builtin_bit_cast(u32x2, __builtin_amdgcn_ds_read_tr16_b64_v4i16((lds_s16x4*)p1));
  return u32x4{lo[0], lo[1], hi[0], hi[1]};
}
template <typename T> __device__ __forceinline__ T ldsr(lds_char* p) { return *(__attribute__((address_space(3))) T*)p; }
template <typename T> __device__ __forceinline__ void ldsw(lds_char* p, T v) { *(__attribute__((address_space(3))) T*)p = v; }
__device__ __forceinline__ f32x16 mfma32(u32x4 a, u32x4 b, f32x16 c) {
  return __builtin_amdgcn_mfma_f32_32x32x16_bf16(__builtin_bit_cast(bf16x8, a), __builtin_bit_cast(bf16x8, b), c, 0, 0, 0);
}
__device__ __forceinline__ u32x4 pack8(const f32x16& a, int s) {
  u32x4 r;
  r[0] = pack2(a[8 * s + 0], a[8 * s + 1]); r[1] = pack2(a[8 * s + 2], a[8 * s + 3]);
  r[2] = pack2(a[8 * s + 4], a[8 * s + 5]); r[3] = pack2(a[8 * s + 6], a[8 * s + 7]);
  return r;
}
__device__ __forceinline__ u32x2 deq4(u32x4 q, f32x4 s, f32x4 z) {
  float w0, w1, w2, w3;
  asm("v_cvt_f32_i32 %0, %4\n\tv_cvt_f32_i32 %1, %5\n\tv_cvt_f32_i32 %2, %6\n\tv_cvt_f32_i32 %3, %7\n\t"
      "v_fma_f32 %0, %0, %8, %12\n\tv_fma_f32 %1, %1, %9, %13\n\tv_fma_f32 %2, %2, %10, %14\n\tv_fma_f32 %3, %3, %11, %15\n\t"
      "v_cvt_pk_bf16_f32 %0, %0, %1\n\tv_cvt_pk_bf16_f32 %1, %2, %3"
      : "=&v"(w0), "=&v"(w1), "=&v"(w2), "=&v"(w3)
      : "v"(q[0]), "v"(q[1]), "v"(q[2]), "v"(q[3]), "v"(s[0]), "v"(s[1]), "v"(s[2]), "v"(s[3]), "v"(z[0]), "v"(z[1]), "v"(z[2]), "v"(z[3]));
  return u32x2{__builtin_bit_cast(unsigned, w0), __builtin_bit_cast(unsigned, w1)};
}
__device__ __forceinline__ void lds_spin(lds_u32* w, unsigned want) {
  while ((unsigned)__builtin_amdgcn_readfirstlane(__hip_atomic_load(w, RLX_WG)) < want) __builtin_amdgcn_s_sleep(1);
  asm volatile("" ::: "memory");
}
__device__ __forceinline__ void poll_flags(gu64* f, u64 tok) {
#pragma unroll 1
  for (unsigned spins = 0; spins < (1u << 20); ++spins) {
    if (__all(__hip_atomic_load(f, RLX_AGENT) == tok)) break;
    __builtin_amdgcn_s_sleep(POLL_SLEEP);
  }
  __builtin_amdgcn_fence(__ATOMIC_ACQUIRE, "wavefront");
}

constexpr int XRF_OFF = 2 << 20, SYNC_OFF = 6 << 20;
#define FS 16
constexpr int FT = 1, FX = 121;
constexpr int FA = 185 * FS;
constexpr int CODE_TOUCH_MAX = 14464;
constexpr int L_SYNC = 156160, L_DUMP = 156416, LDS_BYTES = 156672;

__global__ __launch_bounds__(512, 2) void quip(const __hip_bfloat16* __restrict__ x, const int* __restrict__ qw,
                                               const __hip_bfloat16* __restrict__ scales, const __hip_bfloat16* __restrict__ zeros,
                                               const __hip_bfloat16* __restrict__ U, const __hip_bfloat16* __restrict__ V,
                                               char* ws, __hip_bfloat16* __restrict__ y) {
  extern __shared__ __attribute__((aligned(16))) char smem_[];
  lds_char* smem = (lds_char*)smem_;
  const unsigned lb = (unsigned)(size_t)smem;
  const int wid = __builtin_amdgcn_readfirstlane(threadIdx.x >> 6), lane = threadIdx.x & 63;
  const int xcd = (XCD_PERM >> ((blockIdx.x & 7) * 4)) & 7, idx = blockIdx.x >> 3;
  const int h = lane >> 5, r = lane & 31;
  const int blk = (lane >> 4) & 1, q4 = (lane & 15) >> 2, p4 = lane & 3;
  lds_u32* sw = (lds_u32*)(smem + L_SYNC);
  gu64* sync = (gu64*)(ws + SYNC_OFF);
  const __amdgpu_buffer_rsrc_t wsr = __builtin_amdgcn_make_buffer_rsrc(ws, 0, SYNC_OFF, 0x00020000);
  const bool is_pf = idx < 12 && (XRV == 1 || xcd < 6);
  if (wid == 0) STAMP(0);

  auto put_token = [&](u64 epoch) {
    if (wid == 3 && lane == 0) {
      const u64 e = epoch + 0x9E3779B97F4A7C15ull;
      sw[0] = (unsigned)e; sw[1] = (unsigned)(e >> 32);
      __hip_atomic_store(sw + 2, 1u, RLX_WG);
      __hip_atomic_store(sync + FA + blockIdx.x, e, RLX_AGENT);
    }
  };
  if (wid == 3) {
    if (lane < 16) sw[lane] = ((lane == 3 || lane == 8) && is_pf) ? 4u : 0u;
  } else if (wid == 2 && idx < 2) {
    u64 pc;
    asm volatile("s_getpc_b64 %0" : "=s"(pc));
    set_m0(lb + L_DUMP);
    touch((const char*)pc, min(idx * 8192 + lane * 128, CODE_TOUCH_MAX));
  }
  int myflag = 0;

  if (is_pf) {
    if (wid < PFW) {
      set_m0(lb + L_DUMP);
      const int ga = xcd & 1, gb = xcd >> 1;
      const bool isq = idx < 3 || (idx >= 7 && idx < 10), far = idx < 7;
      const unsigned vo_ = isq ? (lane >> 1) * (F_ * 4) + (lane & 1) * 128 : (lane >> 3) * (F_ * 2) + (lane & 7) * 128;
      const char* gbase = isq ? (const char*)qw + (size_t)(ga * 320) * (F_ * 4) : (const char*)V + gb * 1024;
      const int nc = isq ? 3 : (far ? 4 : 2), c0 = idx - (idx < 3 ? 0 : idx < 7 ? 3 : idx < 10 ? 7 : 10);
      const int ne = isq ? (far ? 80 : 160) : 96;
      __builtin_amdgcn_s_waitcnt(0x0F70);
      int n = 0;
#pragma unroll 1
      for (int e = c0 + nc * wid; e < ne; e += nc * PFW) {
        if (isq) {
          int t = far ? e * 3 + gb : (e >> 1) * 3 + (gb + 1 + (e & 1)) % 3;
          const int pp = t / 10, j = t - pp * 10;
          touch(gbase + j * (32 * F_ * 4) + pp * 256, vo_);
        } else {
          touch(gbase + (size_t)(e * 2 + (far ? ga : ga ^ 1)) * (8 * F_ * 2), vo_);
        }
        if (++n == 2) BAR();
      }
      if (wid == 0) STAMP(7);
    } else {
      WAIT_L(0);
      BAR();
      if (wid == 3) put_token(__hip_atomic_load(sync, RLX_AGENT));
      if (blockIdx.x == 1 && wid == 2) {
        lds_spin(sw + 2, 1);
        const u64 tok = (u64)sw[0] | ((u64)sw[1] << 32);
#pragma unroll 1
        for (int i = 0; i < 4; ++i) poll_flags(sync + FA + i * 64 + lane, tok);
        if (lane == 0) __hip_atomic_store(sync, tok, RLX_AGENT);
      }
    }
  } else if (xcd < 6) {
    const int t = idx - 12;
    const int mt = (xcd & 1) * 5 + t % 5, nt = (xcd >> 1) * 4 + t / 5;
    myflag = FT + nt * 10 + mt;
    if (wid >= 4) {
      const int w4 = wid - 4, rr = lane >> 4, c16 = lane & 15;
      unsigned ag[4], bg[4];
#pragma unroll
      for (int i = 0; i < 4; ++i) {
        int row = i * 4 + rr;
        ag[i] = row * (F_ * 4) + ((c16 ^ row) * 16) - i * 1024;
        bg[i] = row * (F_ * 2) + ((c16 ^ (rr << 2)) * 16) - i * 1024;
      }
      const char* ap = (const char*)qw + (size_t)(mt * 64 + w4 * 16) * (F_ * 4);
      const char* bp = (const char*)V + nt * 256 + (size_t)(w4 * 16) * (F_ * 2);
      unsigned so = lb + w4 * 4096;
      auto issue = [&](int p) {
        unsigned base = so + (p & 3) * 32768;
        set_m0(base);
        glds<0>(ap, ag[0]); glds<1024>(ap, ag[1]); glds<2048>(ap, ag[2]); glds<3072>(ap, ag[3]);
        set_m0(base + 16384);
        glds<0>(bp, bg[0]); glds<1024>(bp, bg[1]); glds<2048>(bp, bg[2]); glds<3072>(bp, bg[3]);
        ap += 256; bp += 64 * F_ * 2;
      };
      issue(0);
      issue(1);
      BAR();
#pragma unroll 1
      for (int p = 2; p < 24; ++p) {
        WAIT_V(8);
        BAR();
        issue(p);
      }
      WAIT_V(8);
      BAR();
      WAIT_V(0);
      BAR();
    } else {
      const int m = wid & 1, kl = wid >> 1;
      {
        const size_t o = (size_t)((mt * 2 + m) * F_ + kl * 768) * 2;
        lds_char* T = smem + 131072 + m * 12288 + kl * 3072;
        u32x4 s8[2], z8[2];
#pragma unroll
        for (int j = 0; j < 2; ++j) {
          int e = j ? 64 + (lane & 31) : lane;
          s8[j] = *(const u32x4*)((const char*)scales + o + e * 16);
          z8[j] = *(const u32x4*)((const char*)zeros + o + e * 16);
        }
        const u64 epoch = __hip_atomic_load(sync, RLX_AGENT);
#pragma unroll
        for (int j = 0; j < 2; ++j) {
          int e = j ? 64 + (lane & 31) : lane;
          f32x4 sa, sb, za, zb;
          sa[0] = bflo(s8[j][0]); sa[1] = bfhi(s8[j][0]); sa[2] = bflo(s8[j][1]); sa[3] = bfhi(s8[j][1]);
          sb[0] = bflo(s8[j][2]); sb[1] = bfhi(s8[j][2]); sb[2] = bflo(s8[j][3]); sb[3] = bfhi(s8[j][3]);
          za[0] = -bflo(z8[j][0]) * sa[0]; za[1] = -bfhi(z8[j][0]) * sa[1]; za[2] = -bflo(z8[j][1]) * sa[2]; za[3] = -bfhi(z8[j][1]) * sa[3];
          zb[0] = -bflo(z8[j][2]) * sb[0]; zb[1] = -bfhi(z8[j][2]) * sb[1]; zb[2] = -bflo(z8[j][3]) * sb[2]; zb[3] = -bfhi(z8[j][3]) * sb[3];
          ldsw<f32x4>(T + e * 32, sa); ldsw<f32x4>(T + e * 32 + 16, sb);
          ldsw<f32x4>(T + 6144 + e * 32, za); ldsw<f32x4>(T + 6144 + e * 32 + 16, zb);
        }
        put_token(epoch);
        WAIT_L(0);
        BAR();
      }
      int aoff[4], boff[4];
#pragma unroll
      for (int c = 0; c < 4; ++c) {
        int ch = 8 * kl + 4 * (c >> 1) + 2 * h + (c & 1);
        aoff[c] = (m * 32 + r) * 256 + ((ch ^ (r & 15)) * 16);
        boff[c] = 16384 + (32 * kl + 8 * h + q4) * 256 + ((c ^ (2 * kl) ^ q4) * 64) + 32 * (p4 & 1) + 16 * blk + 8 * (p4 >> 1);
      }
      lds_char* tp = smem + 131072 + m * 12288 + (32 * kl + 8 * h) * 4;
      f32x16 acc[4] = {};
      f32x4 ts[4], tz[4];
      u32x4 av[4];
      u32x4 af[2][2] = {}, bf[2][2][4] = {};
      auto rdtab = [&](int o, int c) {
        ts[c] = ldsr<f32x4>(tp + o + (c >> 1) * 64 + (c & 1) * 16);
        tz[c] = ldsr<f32x4>(tp + o + 6144 + (c >> 1) * 64 + (c & 1) * 16);
      };
      auto step = [&](int slot) {
        const int buf = slot & 1;
        lds_char* sl = smem + slot * 32768;
        BAR();
#pragma unroll
        for (int c = 0; c < 4; ++c) av[c] = ldsr<u32x4>(sl + aoff[c]);
        SB();
#pragma unroll
        for (int u = 0; u < 8; ++u) {
          acc[u & 3] = mfma32(af[buf ^ 1][u >> 2], bf[buf ^ 1][u >> 2][u & 3], acc[u & 3]);
          SB();
          if (u < 4) {
#pragma unroll
            for (int v = 2 * u; v < 2 * u + 2; ++v)
              bf[buf][v >> 2][v & 3] = trread2(sl + boff[v & 3] + (16 * (v >> 2)) * 256, sl + boff[v & 3] + (16 * (v >> 2) + 4) * 256);
          } else {
            const int c = u - 4;
            u32x2 o = deq4(av[c], ts[c], tz[c]);
            af[buf][c >> 1][(c & 1) * 2] = o[0]; af[buf][c >> 1][(c & 1) * 2 + 1] = o[1];
            SB();
            rdtab((slot + 1) * 256, c);
          }
          SB();
        }
      };
#pragma unroll
      for (int c = 0; c < 4; ++c) rdtab(0, c);
#pragma unroll 1
      for (int p = 0; p < 24; p += 4) {
        step(0); step(1); step(2); step(3);
        tp += 1024;
      }
      if (wid == 0) STAMP(1);
      lds_char* RED = smem + m * 16384 + lane * 16;
      const int n32 = nt * 4 + kl * 2;
      const unsigned oo0 = (((n32 / 3) * 40 + (mt * 2 + m) * 2) * 3 + n32 % 3) * 1024;
      const unsigned oo1 = ((((n32 + 1) / 3) * 40 + (mt * 2 + m) * 2) * 3 + (n32 + 1) % 3) * 1024;
      SB();
#pragma unroll
      for (int u = 0; u < 8; ++u) {
        const int c = (u < 4 ? 2 : 0) + ((u >> 1) & 1), s2 = u & 1;
        acc[c] = mfma32(af[1][s2], bf[1][s2][c], acc[c]);
        SB();
        if (u >= 3 && u < 7) {
#pragma unroll
          for (int j = 2 * (u - 3); j < 2 * (u - 3) + 2; ++j) {
            const f32x16& g = acc[2 + (j >> 2)];
            const int q = 4 * (j & 3);
            ldsw<f32x4>(RED + kl * 8192 + j * 1024, f32x4{g[q], g[q + 1], g[q + 2], g[q + 3]});
          }
          SB();
        }
      }
      asm volatile("" ::: "memory");
      __hip_atomic_store(sw + 9 + wid, 1u, RLX_WG);
      while (__builtin_amdgcn_readfirstlane(__hip_atomic_load(sw + 9 + (wid ^ 2), RLX_WG)) == 0) {}
      asm volatile("" ::: "memory");
      f32x4 pv[8];
#pragma unroll
      for (int j = 0; j < 8; ++j) pv[j] = ldsr<f32x4>(RED + (kl ^ 1) * 8192 + j * 1024);
      SB();
#pragma unroll
      for (int j = 0; j < 4; ++j) {
        f32x16& o = acc[j >> 1];
#pragma unroll
        for (int e = 0; e < 8; ++e) o[(j & 1) * 8 + e] += pv[2 * j + (e >> 2)][e & 3];
        __builtin_amdgcn_raw_buffer_store_b128(pack8(o, j & 1), wsr, lane * 16 + (j & 1) * 3072, j < 2 ? oo0 : oo1, 16);
      }
    }
  } else {
    const int mi = idx & 7, eb = idx >> 3;
    const int xmb = (xcd - 6) * 8 + mi;
    myflag = FX + xmb * 4 + eb;
    if (wid >= 4) {
      const int w4 = wid - 4;
      unsigned xg[4], ug[5];
#pragma unroll
      for (int i = 0; i < 4; ++i) {
        int row = i * 8 + (lane >> 3);
        xg[i] = row * (D_ * 2) + (((lane & 7) ^ ((row >> 1) & 7)) * 16) - i * 1024;
      }
#pragma unroll
      for (int i = 0; i < 5; ++i) {
        int off = i * 1024 + lane * 16;
        ug[i] = (off / 320) * (D_ * 2) + off % 320 - (i & 3) * 1024;
      }
      const char* xb = (const char*)x + (size_t)(xmb * 128 + w4 * 32) * (D_ * 2);
      const char* ub = (const char*)U + eb * 320 + (size_t)(w4 * 16) * (D_ * 2);
      int ks = mi + ((eb * 5 + 1) >> 1);
      if (ks >= 10) ks -= 10;
      auto issue = [&](int st) {
        unsigned base = lb + (st & 3) * 36864;
        const char* xp = xb + ks * 128;
        const char* up = ub + (size_t)ks * 64 * (D_ * 2);
        set_m0(base + w4 * 4096);
        glds<0>(xp, xg[0]); glds<1024>(xp, xg[1]); glds<2048>(xp, xg[2]); glds<3072>(xp, xg[3]);
        set_m0(base + 16384 + w4 * 5120);
        glds<0>(up, ug[0]); glds<1024>(up, ug[1]); glds<2048>(up, ug[2]); glds<3072>(up, ug[3]);
        set_m0(base + 16384 + w4 * 5120 + 4096);
        glds<0>(up, ug[4]);
        if (++ks == 10) ks = 0;
      };
      issue(0);
      if (XTOUCH) {
        set_m0(lb + L_DUMP);
        int kt = ks + (w4 >> 1);
        if (kt >= 10) kt -= 10;
        if (w4 < 2 || !(eb & 1)) touch((const char*)x + (size_t)(xmb * 128 + (w4 & 1) * 64) * (D_ * 2) + kt * 128, lane * (D_ * 2));
        if (w4 == 3 && mi < 6) {
          int ku = 8 + mi / 3 + ((eb * 5 + 1) >> 1);
          if (ku >= 10) ku -= 10;
          touch((const char*)U + (size_t)ku * 64 * (D_ * 2) + ((eb * 320) & ~127) + (mi % 3) * 128, lane * (D_ * 2));
        }
      }
#pragma unroll 1
      for (int st = 1; st < 10; ++st) {
        issue(st);
        WAIT_V(9);
        BAR();
      }
      WAIT_V(0);
      BAR();
    } else {
      int xoff[4];
#pragma unroll
      for (int s = 0; s < 4; ++s) xoff[s] = (wid * 32 + r) * 128 + (((2 * s + h) ^ ((r >> 1) & 7)) * 16);
      const int uoff = 16384 + (8 * h + q4) * 320 + blk * 32 + p4 * 8;
      f32x16 acc[5] = {};
      u32x4 xf[2], uf[2][5];
      auto rdu = [&](lds_char* base, int s, int buf, int c) {
        uf[buf][c] = trread2(base + uoff + c * 64 + (16 * s) * 320, base + uoff + c * 64 + (16 * s + 4) * 320);
      };
      if (wid == 3) put_token(__hip_atomic_load(sync, RLX_AGENT));
      WAIT_L(0);
      BAR();
      xf[0] = ldsr<u32x4>(smem + xoff[0]);
#pragma unroll
      for (int c = 0; c < 5; ++c) rdu(smem, 0, 0, c);
#pragma unroll 1
      for (int st = 0; st < 10; ++st) {
#pragma unroll
        for (int s = 0; s < 4; ++s) {
          lds_char* base = smem + ((s < 3 ? st : st + 1) & 3) * 36864;
          const int nx = (s + 1) & 3, nb_ = (s + 1) & 1;
#pragma unroll
          for (int c = 0; c < 5; ++c) {
            acc[c] = mfma32(uf[s & 1][c], xf[s & 1], acc[c]);
            SB();
            if (c == 0) {
              if (s == 3 && st < 9) { BAR(); asm volatile("" ::: "memory"); }
              xf[nb_] = ldsr<u32x4>(base + xoff[nx]);
              rdu(base, nx, nb_, 0);
            } else if (c < 4) {
              rdu(base, nx, nb_, c);
              if (c == 3) rdu(base, nx, nb_, 4);
            }
            SB();
          }
        }
      }
#pragma unroll
      for (int c = 0; c < 5; ++c) {
        const unsigned o = XRF_OFF + ((xmb * 4 + wid) * 40 + (eb * 5 + c) * 2) * 1024;
        __builtin_amdgcn_raw_buffer_store_b128(pack8(acc[c], 0), wsr, lane * 16, o, 16);
        __builtin_amdgcn_raw_buffer_store_b128(pack8(acc[c], 1), wsr, lane * 16 + 1024, o, 16);
      }
      if (wid == 0) STAMP(3);
    }
  }

  const int mb = YPART == 0 ? (xcd >> 1) * 4 + (idx & 3) : YPART == 1 ? (xcd >> 2) * 8 + (idx & 7) : idx & 15;
  const int nb = YPART == 0 ? (xcd & 1) * 8 + (idx >> 2) : YPART == 1 ? (xcd & 3) * 4 + (idx >> 3) : xcd * 2 + (idx >> 4);
  const unsigned vo = lane * 16;
  const char* xsrc = ws + XRF_OFF + (mb * 4 + (wid & 3)) * 40960;
  auto ldx = [&](int ch) {
    asm volatile("s_mov_b32 m0, %2\n\ts_nop 0\n\tglobal_load_lds_dwordx4 %0, %1 sc1\n\tglobal_load_lds_dwordx4 %0, %1 offset:1024 sc1\n\t"
                 "global_load_lds_dwordx4 %0, %1 offset:2048 sc1\n\tglobal_load_lds_dwordx4 %0, %1 offset:3072 sc1"
                 ::"v"(vo), "s"(xsrc + ch * 4096), "s"(lb + (ch % 5) * 28672 + 12288 + (wid & 3) * 4096) : "memory");
  };
  if (wid < 4) {
    if (!is_pf) {
      if (wid == 0) STAMP(10);
      if (lane == 0) __hip_atomic_fetch_add(sw + 8, 1u, RLX_WG);
    }
    lds_spin(sw + 2, 1);
    const u64 tok = (u64)sw[0] | ((u64)sw[1] << 32);
    if (!is_pf) {
      asm volatile("s_waitcnt vmcnt(0) lgkmcnt(0)" ::: "memory");
      if (lane == 0) {
        if (__hip_atomic_fetch_add(sw + 3, 1u, RLX_WG) == 3) __hip_atomic_store(sync + myflag * FS, tok, RLX_AGENT);
      }
      if (wid == 0) STAMP(2);
    }
    if (wid == 0) {
      lds_spin(sw + 3, 4);
      if (TGATE) lds_spin(sw + 4, 1);
      const int n0 = (nb * 3) >> 2, n1 = (nb * 3 + 2) >> 2;
      poll_flags(sync + (FT + n0 * 10 + min(lane, (n1 - n0) * 10 + 9)) * FS, tok);
      if (lane == 0) __hip_atomic_store(sw + 5, 1u, RLX_WG);
      STAMP(4);
    } else {
      lds_spin(sw + 5, 1);
    }
    const char* tsrc = ws + nb * 122880 + wid * 3072;
    auto ldt = [&](int ch) {
      asm volatile("s_mov_b32 m0, %2\n\ts_nop 0\n\tglobal_load_lds_dwordx4 %0, %1 sc1\n\tglobal_load_lds_dwordx4 %0, %1 offset:1024 sc1\n\t"
                   "global_load_lds_dwordx4 %0, %1 offset:2048 sc1" ::"v"(vo), "s"(tsrc + ch * 12288), "s"(lb + (ch % 5) * 28672 + wid * 3072) : "memory");
    };
    ldt(0); ldt(1); ldt(2);
    WAIT_V(6); BAR(); ldt(3);
    WAIT_V(6); BAR(); ldt(4);
    WAIT_V(6); BAR(); ldt(5); ldx(5);
    WAIT_V(10); BAR(); ldt(6); ldx(6);
    WAIT_V(14); BAR(); ldt(7); ldx(7);
    WAIT_V(14); BAR(); ldt(8); ldx(8);
    WAIT_V(14); BAR(); ldt(9); ldx(9);
    WAIT_V(14); BAR();
    WAIT_V(7); BAR();
    WAIT_V(0); BAR();
  } else {
    const int w4 = wid - 4;
    if (wid == 4) {
      lds_spin(sw + 2, 1);
      const u64 tok = (u64)sw[0] | ((u64)sw[1] << 32);
      poll_flags(sync + (XRV ? FX + (mb >> 1) * 5 + min(lane, 4) : FX + mb * 4 + (lane & 3)) * FS, tok);
      lds_spin(sw + 8, 4);
      if (lane == 0) __hip_atomic_store(sw + 4, 1u, RLX_WG);
      STAMP(8);
    } else {
      lds_spin(sw + 4, 1);
    }
#pragma unroll
    for (int ch = 0; ch < 5; ++ch) ldx(ch);
    f32x16 acc[3] = {};
    u32x4 tf[3][3], xf[3];
    lds_char* tb = smem + lane * 16;
    lds_char* xb = smem + lane * 16 + 12288 + w4 * 4096;
    auto rdf = [&](int ks, int c) {
      const int o = ((ks >> 2) % 5) * 28672;
      if (c < 3) tf[ks % 3][c] = ldsr<u32x4>(tb + o + ((ks & 3) * 3 + c) * 1024);
      else xf[ks % 3] = ldsr<u32x4>(xb + o + (ks & 3) * 1024);
    };
    WAIT_V(16);
    BAR();
#pragma unroll
    for (int c = 0; c < 8; ++c) rdf(c >> 2, 3 - (c & 3));
#pragma unroll
    for (int ks = 0; ks < 40; ++ks) {
#pragma unroll
      for (int c = 0; c < 3; ++c) {
        acc[c] = mfma32(tf[ks % 3][c], xf[ks % 3], acc[c]);
        SB();
        if (ks < 38) {
          if (c == 0) {
            if ((ks & 3) == 2) {
              if (ks == 2) WAIT_V(12);
              if (ks == 6) WAIT_V(8);
              if (ks == 10) WAIT_V(4);
              if (ks == 14) WAIT_V(0);
              BAR(); asm volatile("" ::: "memory");
            }
            rdf(ks + 2, 3);
          }
          rdf(ks + 2, c);
        }
        SB();
      }
    }
    if (wid == 4) STAMP(5);
    lds_char* eb = smem + (YV ? 0 : 122880) + w4 * 6656;
#pragma unroll
    for (int c = 0; c < 3; ++c) {
      ldsw<u32x4>(eb + r * 208 + c * 64 + h * 32, pack8(acc[c], 0));
      ldsw<u32x4>(eb + r * 208 + c * 64 + h * 32 + 16, pack8(acc[c], 1));
    }
    const __amdgpu_buffer_rsrc_t yr = __builtin_amdgcn_make_buffer_rsrc((void*)y, 0, 2048 * F_ * 2, 0x00020000);
    const unsigned yo = ((mb * 128 + w4 * 32) * F_ + nb * 96) * 2;
#pragma unroll
    for (int i = 0; i < 6; ++i) {
      int q = i * 64 + lane, row = q / 12, chn = q % 12;
      __builtin_amdgcn_raw_buffer_store_b128(ldsr<u32x4>(eb + row * 208 + chn * 16), yr, row * (F_ * 2) + chn * 16, yo, Y_AUX);
    }
  }
}

extern "C" void kernel_launch(void* const* d_in, const int* in_sizes, int n_in, void* d_out, int out_size, void* d_ws,
                              size_t ws_size, hipStream_t stream) {
  (void)hipFuncSetAttribute((const void*)quip, hipFuncAttributeMaxDynamicSharedMemorySize, LDS_BYTES);
  quip<<<256, 512, LDS_BYTES, stream>>>((const __hip_bfloat16*)d_in[0], (const int*)d_in[1], (const __hip_bfloat16*)d_in[2],
                                        (const __hip_bfloat16*)d_in[3], (const __hip_bfloat16*)d_in[4], (const __hip_bfloat16*)d_in[5],
                                        (char*)d_ws, (__hip_bfloat16*)d_out);
}
```

```cpp
#define AMP 0
#define AMP_BLK 125
#define AMPF 3
#include <hip/hip_runtime.h>
#include <hip/hip_bf16.h>

typedef __attribute__((ext_vector_type(8))) __bf16 bf16x8;
typedef __attribute__((ext_vector_type(2))) __bf16 bf16x2;
typedef __attribute__((ext_vector_type(4))) short s16x4;
typedef __attribute__((ext_vector_type(16))) float f32x16;
typedef __attribute__((ext_vector_type(4))) float f32x4;
typedef __attribute__((ext_vector_type(4))) unsigned u32x4;
typedef __attribute__((ext_vector_type(2))) unsigned u32x2;
typedef __attribute__((address_space(3))) s16x4 lds_s16x4;
typedef __attribute__((address_space(3))) char lds_char;
typedef __attribute__((address_space(3))) unsigned lds_u32;
typedef __attribute__((address_space(1))) unsigned long long gu64;
typedef unsigned long long u64;

#define STAMP(i) do { if ((threadIdx.x & 63) == 0) sw[16 + (i)] = (unsigned)__builtin_amdgcn_s_memrealtime(); } while (0)
#define Y_AUX 2
#define XCD_PERM 0x54321706
#define YPART 1
#define PFV 1
#define PFW 2
#define XRV 0
#define YV 0
#define TGATE 0
#define XTOUCH 0
#define POLL_SLEEP 4
constexpr int D_ = 640, F_ = 1536;

#define WAIT_V(n) asm volatile("s_waitcnt vmcnt(" #n ")" ::: "memory")
#define WAIT_L(n) asm volatile("s_waitcnt lgkmcnt(" #n ")" ::: "memory")
#define BAR() __builtin_amdgcn_s_barrier()
#define SB() __builtin_amdgcn_sched_barrier(0)
#define RLX_AGENT __ATOMIC_RELAXED, __HIP_MEMORY_SCOPE_AGENT
#define RLX_WG __ATOMIC_RELAXED, __HIP_MEMORY_SCOPE_WORKGROUP

__device__ __forceinline__ void set_m0(unsigned ldsaddr) { asm volatile("s_mov_b32 m0, %0\n\ts_nop 0" ::"s"(ldsaddr) : "memory"); }
template <int OFF> __device__ __forceinline__ void glds(const void* sbase, unsigned voff) {
  asm volatile("global_load_lds_dwordx4 %0, %1 offset:%2" ::"v"(voff), "s"(sbase), "i"(OFF) : "memory");
}
__device__ __forceinline__ void touch(const void* sbase, unsigned voff) {
  asm volatile("global_load_lds_dword %0, %1" ::"v"(voff), "s"(sbase) : "memory");
}
__device__ __forceinline__ unsigned pack2(float a, float b) {
  bf16x2 p; p[0] = (__bf16)a; p[1] = (__bf16)b;
  return __builtin_bit_cast(unsigned, p);
}
__device__ __forceinline__ float bflo(unsigned u) { return __builtin_bit_cast(float, u << 16); }
__device__ __forceinline__ float bfhi(unsigned u) { return __builtin_bit_cast(float, u & 0xffff0000u); }
__device__ __forceinline__ u32x4 trread2(lds_char* p0, lds_char* p1) {
  u32x2 lo = __builtin_bit_cast(u32x2, __builtin_amdgcn_ds_read_tr16_b64_v4i16((lds_s16x4*)p0));
  u32x2 hi = __builtin_bit_cast(u32x2, __builtin_amdgcn_ds_read_tr16_b64_v4i16((lds_s16x4*)p1));
  return u32x4{lo[0], lo[1], hi[0], hi[1]};
}
template <typename T> __device__ __forceinline__ T ldsr(lds_char* p) { return *(__attribute__((address_space(3))) T*)p; }
template <typename T> __device__ __forceinline__ void ldsw(lds_char* p, T v) { *(__attribute__((address_space(3))) T*)p = v; }
__device__ __forceinline__ f32x16 mfma32(u32x4 a, u32x4 b, f32x16 c) {
  return __builtin_amdgcn_mfma_f32_32x32x16_bf16(__builtin_bit_cast(bf16x8, a), __builtin_bit_cast(bf16x8, b), c, 0, 0, 0);
}
__device__ __forceinline__ u32x4 pack8(const f32x16& a, int s) {
  u32x4 r;
  r[0] = pack2(a[8 * s + 0], a[8 * s + 1]); r[1] = pack2(a[8 * s + 2], a[8 * s + 3]);
  r[2] = pack2(a[8 * s + 4], a[8 * s + 5]); r[3] = pack2(a[8 * s + 6], a[8 * s + 7]);
  return r;
}
__device__ __forceinline__ u32x2 deq4(u32x4 q, f32x4 s, f32x4 z) {
  float w0, w1, w2, w3;
  asm("v_cvt_f32_i32 %0, %4\n\tv_cvt_f32_i32 %1, %5\n\tv_cvt_f32_i32 %2, %6\n\tv_cvt_f32_i32 %3, %7\n\t"
      "v_fma_f32 %0, %0, %8, %12\n\tv_fma_f32 %1, %1, %9, %13\n\tv_fma_f32 %2, %2, %10, %14\n\tv_fma_f32 %3, %3, %11, %15\n\t"
      "v_cvt_pk_bf16_f32 %0, %0, %1\n\tv_cvt_pk_bf16_f32 %1, %2, %3"
      : "=&v"(w0), "=&v"(w1), "=&v"(w2), "=&v"(w3)
      : "v"(q[0]), "v"(q[1]), "v"(q[2]), "v"(q[3]), "v"(s[0]), "v"(s[1]), "v"(s[2]), "v"(s[3]), "v"(z[0]), "v"(z[1]), "v"(z[2]), "v"(z[3]));
  return u32x2{__builtin_bit_cast(unsigned, w0), __builtin_bit_cast(unsigned, w1)};
}
__device__ __forceinline__ void lds_spin(lds_u32* w, unsigned want) {
  while ((unsigned)__builtin_amdgcn_readfirstlane(__hip_atomic_load(w, RLX_WG)) < want) __builtin_amdgcn_s_sleep(1);
  asm volatile("" ::: "memory");
}
__device__ __forceinline__ void poll_flags(gu64* f, u64 tok) {
#pragma unroll 1
  for (unsigned spins = 0; spins < (1u << 20); ++spins) {
    if (__all(__hip_atomic_load(f, RLX_AGENT) == tok)) break;
    __builtin_amdgcn_s_sleep(POLL_SLEEP);
  }
  __builtin_amdgcn_fence(__ATOMIC_ACQUIRE, "wavefront");
}

constexpr int XRF_OFF = 2 << 20, SYNC_OFF = 6 << 20;
#define FS 16
constexpr int FT = 1, FX = 121;
constexpr int FA = 185 * FS;
constexpr int CODE_TOUCH_MAX = 14464;
constexpr int L_SYNC = 156160, L_DUMP = 156416, LDS_BYTES = 156672;

__global__ __launch_bounds__(512, 2) void quip(const __hip_bfloat16* __restrict__ x, const int* __restrict__ qw,
                                               const __hip_bfloat16* __restrict__ scales, const __hip_bfloat16* __restrict__ zeros,
                                               const __hip_bfloat16* __restrict__ U, const __hip_bfloat16* __restrict__ V,
                                               char* ws, __hip_bfloat16* __restrict__ y) {
  extern __shared__ __attribute__((aligned(16))) char smem_[];
  lds_char* smem = (lds_char*)smem_;
  const unsigned lb = (unsigned)(size_t)smem;
  const int wid = __builtin_amdgcn_readfirstlane(threadIdx.x >> 6), lane = threadIdx.x & 63;
  const int xcd = (XCD_PERM >> ((blockIdx.x & 7) * 4)) & 7, idx = blockIdx.x >> 3;
  const int h = lane >> 5, r = lane & 31;
  const int blk = (lane >> 4) & 1, q4 = (lane & 15) >> 2, p4 = lane & 3;
  lds_u32* sw = (lds_u32*)(smem + L_SYNC);
  gu64* sync = (gu64*)(ws + SYNC_OFF);
  const __amdgpu_buffer_rsrc_t wsr = __builtin_amdgcn_make_buffer_rsrc(ws, 0, SYNC_OFF, 0x00020000);
  const bool is_pf = idx < 12 && (XRV == 1 || xcd < 6);
  if (wid == 0) STAMP(0);

  auto put_token = [&](u64 epoch) {
    if (wid == 3 && lane == 0) {
      const u64 e = epoch + 0x9E3779B97F4A7C15ull;
      sw[0] = (unsigned)e; sw[1] = (unsigned)(e >> 32);
      __hip_atomic_store(sw + 2, 1u, RLX_WG);
      __hip_atomic_store(sync + FA + blockIdx.x, e, RLX_AGENT);
    }
  };
  if (wid == 3) {
    if (lane < 16) sw[lane] = ((lane == 3 || lane == 8) && is_pf) ? 4u : 0u;
  } else if (wid == 2 && idx < 2) {
    u64 pc;
    asm volatile("s_getpc_b64 %0" : "=s"(pc));
    set_m0(lb + L_DUMP);
    touch((const char*)pc, min(idx * 8192 + lane * 128, CODE_TOUCH_MAX));
  }
  int myflag = 0;

  if (is_pf) {
    if (wid < PFW) {
      set_m0(lb + L_DUMP);
      const int ga = xcd & 1, gb = xcd >> 1;
      const bool isq = idx < 3 || (idx >= 7 && idx < 10), far = idx < 7;
      const unsigned vo_ = isq ? (lane >> 1) * (F_ * 4) + (lane & 1) * 128 : (lane >> 3) * (F_ * 2) + (lane & 7) * 128;
      const char* gbase = isq ? (const char*)qw + (size_t)(ga * 320) * (F_ * 4) : (const char*)V + gb * 1024;
      const int nc = isq ? 3 : (far ? 4 : 2), c0 = idx - (idx < 3 ? 0 : idx < 7 ? 3 : idx < 10 ? 7 : 10);
      const int ne = isq ? (far ? 80 : 160) : 96;
      __builtin_amdgcn_s_waitcnt(0x0F70);
      int n = 0;
#pragma unroll 1
      for (int e = c0 + nc * wid; e < ne; e += nc * PFW) {
        if (isq) {
          int t = far ? e * 3 + gb : (e >> 1) * 3 + (gb + 1 + (e & 1)) % 3;
          const int pp = t / 10, j = t - pp * 10;
          touch(gbase + j * (32 * F_ * 4) + pp * 256, vo_);
        } else {
          touch(gbase + (size_t)(e * 2 + (far ? ga : ga ^ 1)) * (8 * F_ * 2), vo_);
        }
        if (++n == 2) BAR();
      }
      if (wid == 0) STAMP(7);
    } else {
      WAIT_L(0);
      BAR();
      if (wid == 3) put_token(__hip_atomic_load(sync, RLX_AGENT));
      if (blockIdx.x == 1 && wid == 2) {
        lds_spin(sw + 2, 1);
        const u64 tok = (u64)sw[0] | ((u64)sw[1] << 32);
#pragma unroll 1
        for (int i = 0; i < 4; ++i) poll_flags(sync + FA + i * 64 + lane, tok);
        if (lane == 0) __hip_atomic_store(sync, tok, RLX_AGENT);
      }
    }
  } else if (xcd < 6) {
    const int t = idx - 12;
    const int mt = (xcd & 1) * 5 + t % 5, nt = (xcd >> 1) * 4 + t / 5;
    myflag = FT + nt * 10 + mt;
    if (wid >= 4) {
      const int w4 = wid - 4, rr = lane >> 4, c16 = lane & 15;
      unsigned ag[4], bg[4];
#pragma unroll
      for (int i = 0; i < 4; ++i) {
        int row = i * 4 + rr;
        ag[i] = row * (F_ * 4) + ((c16 ^ row) * 16) - i * 1024;
        bg[i] = row * (F_ * 2) + ((c16 ^ (rr << 2)) * 16) - i * 1024;
      }
      const char* ap = (const char*)qw + (size_t)(mt * 64 + w4 * 16) * (F_ * 4);
      const char* bp = (const char*)V + nt * 256 + (size_t)(w4 * 16) * (F_ * 2);
      unsigned so = lb + w4 * 4096;
      auto issue = [&](int p) {
        unsigned base = so + (p & 3) * 32768;
        set_m0(base);
        glds<0>(ap, ag[0]); glds<1024>(ap, ag[1]); glds<2048>(ap, ag[2]); glds<3072>(ap, ag[3]);
        set_m0(base + 16384);
        glds<0>(bp, bg[0]); glds<1024>(bp, bg[1]); glds<2048>(bp, bg[2]); glds<3072>(bp, bg[3]);
        ap += 256; bp += 64 * F_ * 2;
      };
      issue(0);
      issue(1);
      BAR();
#pragma unroll 1
      for (int p = 2; p < 24; ++p) {
        WAIT_V(8);
        BAR();
        issue(p);
      }
      WAIT_V(8);
      BAR();
      WAIT_V(0);
      BAR();
    } else {
      const int m = wid & 1, kl = wid >> 1;
      {
        const size_t o = (size_t)((mt * 2 + m) * F_ + kl * 768) * 2;
        lds_char* T = smem + 131072 + m * 12288 + kl * 3072;
        u32x4 s8[2], z8[2];
#pragma unroll
        for (int j = 0; j < 2; ++j) {
          int e = j ? 64 + (lane & 31) : lane;
          s8[j] = *(const u32x4*)((const char*)scales + o + e * 16);
          z8[j] = *(const u32x4*)((const char*)zeros + o + e * 16);
        }
        const u64 epoch = __hip_atomic_load(sync, RLX_AGENT);
#pragma unroll
        for (int j = 0; j < 2; ++j) {
          int e = j ? 64 + (lane & 31) : lane;
          f32x4 sa, sb, za, zb;
          sa[0] = bflo(s8[j][0]); sa[1] = bfhi(s8[j][0]); sa[2] = bflo(s8[j][1]); sa[3] = bfhi(s8[j][1]);
          sb[0] = bflo(s8[j][2]); sb[1] = bfhi(s8[j][2]); sb[2] = bflo(s8[j][3]); sb[3] = bfhi(s8[j][3]);
          za[0] = -bflo(z8[j][0]) * sa[0]; za[1] = -bfhi(z8[j][0]) * sa[1]; za[2] = -bflo(z8[j][1]) * sa[2]; za[3] = -bfhi(z8[j][1]) * sa[3];
          zb[0] = -bflo(z8[j][2]) * sb[0]; zb[1] = -bfhi(z8[j][2]) * sb[1]; zb[2] = -bflo(z8[j][3]) * sb[2]; zb[3] = -bfhi(z8[j][3]) * sb[3];
          ldsw<f32x4>(T + e * 32, sa); ldsw<f32x4>(T + e * 32 + 16, sb);
          ldsw<f32x4>(T + 6144 + e * 32, za); ldsw<f32x4>(T + 6144 + e * 32 + 16, zb);
        }
        put_token(epoch);
        WAIT_L(0);
        BAR();
      }
      int aoff[4], boff[4];
#pragma unroll
      for (int c = 0; c < 4; ++c) {
        int ch = 8 * kl + 4 * (c >> 1) + 2 * h + (c & 1);
        aoff[c] = (m * 32 + r) * 256 + ((ch ^ (r & 15)) * 16);
        boff[c] = 16384 + (32 * kl + 8 * h + q4) * 256 + ((c ^ (2 * kl) ^ q4) * 64) + 32 * (p4 & 1) + 16 * blk + 8 * (p4 >> 1);
      }
      lds_char* tp = smem + 131072 + m * 12288 + (32 * kl + 8 * h) * 4;
      f32x16 acc[4] = {};
      f32x4 ts[4], tz[4];
      u32x4 av[4];
      u32x4 af[2][2] = {}, bf[2][2][4] = {};
      auto rdtab = [&](int o, int c) {
        ts[c] = ldsr<f32x4>(tp + o + (c >> 1) * 64 + (c & 1) * 16);
        tz[c] = ldsr<f32x4>(tp + o + 6144 + (c >> 1) * 64 + (c & 1) * 16);
      };
      auto step = [&](int slot) {
        const int buf = slot & 1;
        lds_char* sl = smem + slot * 32768;
        BAR();
#pragma unroll
        for (int c = 0; c < 4; ++c) av[c] = ldsr<u32x4>(sl + aoff[c]);
        SB();
#pragma unroll
        for (int u = 0; u < 8; ++u) {
          acc[u & 3] = mfma32(af[buf ^ 1][u >> 2], bf[buf ^ 1][u >> 2][u & 3], acc[u & 3]);
          SB();
          if (u < 4) {
#pragma unroll
            for (int v = 2 * u; v < 2 * u + 2; ++v)
              bf[buf][v >> 2][v & 3] = trread2(sl + boff[v & 3] + (16 * (v >> 2)) * 256, sl + boff[v & 3] + (16 * (v >> 2) + 4) * 256);
          } else {
            const int c = u - 4;
            u32x2 o = deq4(av[c], ts[c], tz[c]);
            af[buf][c >> 1][(c & 1) * 2] = o[0]; af[buf][c >> 1][(c & 1) * 2 + 1] = o[1];
            SB();
            rdtab((slot + 1) * 256, c);
          }
          SB();
        }
      };
#pragma unroll
      for (int c = 0; c < 4; ++c) rdtab(0, c);
#pragma unroll 1
      for (int p = 0; p < 24; p += 4) {
        if (wid == 0) STAMP(12 + (p >> 2));
        step(0); step(1); step(2); step(3);
        tp += 1024;
      }
      if (wid == 0) STAMP(1);
      lds_char* RED = smem + m * 16384 + lane * 16;
      const int n32 = nt * 4 + kl * 2;
      const unsigned oo0 = (((n32 / 3) * 40 + (mt * 2 + m) * 2) * 3 + n32 % 3) * 1024;
      const unsigned oo1 = ((((n32 + 1) / 3) * 40 + (mt * 2 + m) * 2) * 3 + (n32 + 1) % 3) * 1024;
      SB();
#pragma unroll
      for (int u = 0; u < 8; ++u) {
        const int c = (u < 4 ? 2 : 0) + ((u >> 1) & 1), s2 = u & 1;
        acc[c] = mfma32(af[1][s2], bf[1][s2][c], acc[c]);
        SB();
        if (u >= 3 && u < 7) {
#pragma unroll
          for (int j = 2 * (u - 3); j < 2 * (u - 3) + 2; ++j) {
            const f32x16& g = acc[2 + (j >> 2)];
            const int q = 4 * (j & 3);
            ldsw<f32x4>(RED + kl * 8192 + j * 1024, f32x4{g[q], g[q + 1], g[q + 2], g[q + 3]});
          }
          SB();
        }
      }
      asm volatile("" ::: "memory");
      __hip_atomic_store(sw + 9 + wid, 1u, RLX_WG);
      while (__builtin_amdgcn_readfirstlane(__hip_atomic_load(sw + 9 + (wid ^ 2), RLX_WG)) == 0) {}
      asm volatile("" ::: "memory");
      f32x4 pv[8];
#pragma unroll
      for (int j = 0; j < 8; ++j) pv[j] = ldsr<f32x4>(RED + (kl ^ 1) * 8192 + j * 1024);
      SB();
#pragma unroll
      for (int j = 0; j < 4; ++j) {
        f32x16& o = acc[j >> 1];
#pragma unroll
        for (int e = 0; e < 8; ++e) o[(j & 1) * 8 + e] += pv[2 * j + (e >> 2)][e & 3];
        __builtin_amdgcn_raw_buffer_store_b128(pack8(o, j & 1), wsr, lane * 16 + (j & 1) * 3072, j < 2 ? oo0 : oo1, 16);
      }
    }
  } else {
    const int mi = idx & 7, eb = idx >> 3;
    const int xmb = (xcd - 6) * 8 + mi;
    myflag = FX + xmb * 4 + eb;
    if (wid >= 4) {
      const int w4 = wid - 4;
      unsigned xg[4], ug[5];
#pragma unroll
      for (int i = 0; i < 4; ++i) {
        int row = i * 8 + (lane >> 3);
        xg[i] = row * (D_ * 2) + (((lane & 7) ^ ((row >> 1) & 7)) * 16) - i * 1024;
      }
#pragma unroll
      for (int i = 0; i < 5; ++i) {
        int off = i * 1024 + lane * 16;
        ug[i] = (off / 320) * (D_ * 2) + off % 320 - (i & 3) * 1024;
      }
      const char* xb = (const char*)x + (size_t)(xmb * 128 + w4 * 32) * (D_ * 2);
      const char* ub = (const char*)U + eb * 320 + (size_t)(w4 * 16) * (D_ * 2);
      int ks = mi + ((eb * 5 + 1) >> 1);
      if (ks >= 10) ks -= 10;
      auto issue = [&](int st) {
        unsigned base = lb + (st & 3) * 36864;
        const char* xp = xb + ks * 128;
        const char* up = ub + (size_t)ks * 64 * (D_ * 2);
        set_m0(base + w4 * 4096);
        glds<0>(xp, xg[0]); glds<1024>(xp, xg[1]); glds<2048>(xp, xg[2]); glds<3072>(xp, xg[3]);
        set_m0(base + 16384 + w4 * 5120);
        glds<0>(up, ug[0]); glds<1024>(up, ug[1]); glds<2048>(up, ug[2]); glds<3072>(up, ug[3]);
        set_m0(base + 16384 + w4 * 5120 + 4096);
        glds<0>(up, ug[4]);
        if (++ks == 10) ks = 0;
      };
      issue(0);
      if (XTOUCH) {
        set_m0(lb + L_DUMP);
        int kt = ks + (w4 >> 1);
        if (kt >= 10) kt -= 10;
        if (w4 < 2 || !(eb & 1)) touch((const char*)x + (size_t)(xmb * 128 + (w4 & 1) * 64) * (D_ * 2) + kt * 128, lane * (D_ * 2));
        if (w4 == 3 && mi < 6) {
          int ku = 8 + mi / 3 + ((eb * 5 + 1) >> 1);
          if (ku >= 10) ku -= 10;
          touch((const char*)U + (size_t)ku * 64 * (D_ * 2) + ((eb * 320) & ~127) + (mi % 3) * 128, lane * (D_ * 2));
        }
      }
#pragma unroll 1
      for (int st = 1; st < 10; ++st) {
        issue(st);
        WAIT_V(9);
        BAR();
      }
      WAIT_V(0);
      BAR();
    } else {
      int xoff[4];
#pragma unroll
      for (int s = 0; s < 4; ++s) xoff[s] = (wid * 32 + r) * 128 + (((2 * s + h) ^ ((r >> 1) & 7)) * 16);
      const int uoff = 16384 + (8 * h + q4) * 320 + blk * 32 + p4 * 8;
      f32x16 acc[5] = {};
      u32x4 xf[2], uf[2][5];
      auto rdu = [&](lds_char* base, int s, int buf, int c) {
        uf[buf][c] = trread2(base + uoff + c * 64 + (16 * s) * 320, base + uoff + c * 64 + (16 * s + 4) * 320);
      };
      if (wid == 3) put_token(__hip_atomic_load(sync, RLX_AGENT));
      WAIT_L(0);
      BAR();
      xf[0] = ldsr<u32x4>(smem + xoff[0]);
#pragma unroll
      for (int c = 0; c < 5; ++c) rdu(smem, 0, 0, c);
#pragma unroll 1
      for (int st = 0; st < 10; ++st) {
#pragma unroll
        for (int s = 0; s < 4; ++s) {
          lds_char* base = smem + ((s < 3 ? st : st + 1) & 3) * 36864;
          const int nx = (s + 1) & 3, nb_ = (s + 1) & 1;
#pragma unroll
          for (int c = 0; c < 5; ++c) {
            acc[c] = mfma32(uf[s & 1][c], xf[s & 1], acc[c]);
            SB();
            if (c == 0) {
              if (s == 3 && st < 9) { BAR(); asm volatile("" ::: "memory"); }
              xf[nb_] = ldsr<u32x4>(base + xoff[nx]);
              rdu(base, nx, nb_, 0);
            } else if (c < 4) {
              rdu(base, nx, nb_, c);
              if (c == 3) rdu(base, nx, nb_, 4);
            }
            SB();
          }
        }
      }
#pragma unroll
      for (int c = 0; c < 5; ++c) {
        const unsigned o = XRF_OFF + ((xmb * 4 + wid) * 40 + (eb * 5 + c) * 2) * 1024;
        __builtin_amdgcn_raw_buffer_store_b128(pack8(acc[c], 0), wsr, lane * 16, o, 16);
        __builtin_amdgcn_raw_buffer_store_b128(pack8(acc[c], 1), wsr, lane * 16 + 1024, o, 16);
      }
      if (wid == 0) STAMP(3);
    }
  }

  const int mb = YPART == 0 ? (xcd >> 1) * 4 + (idx & 3) : YPART == 1 ? (xcd >> 2) * 8 + (idx & 7) : idx & 15;
  const int nb = YPART == 0 ? (xcd & 1) * 8 + (idx >> 2) : YPART == 1 ? (xcd & 3) * 4 + (idx >> 3) : xcd * 2 + (idx >> 4);
  const unsigned vo = lane * 16;
  const char* xsrc = ws + XRF_OFF + (mb * 4 + (wid & 3)) * 40960;
  auto ldx = [&](int ch) {
    asm volatile("s_mov_b32 m0, %2\n\ts_nop 0\n\tglobal_load_lds_dwordx4 %0, %1 sc1\n\tglobal_load_lds_dwordx4 %0, %1 offset:1024 sc1\n\t"
                 "global_load_lds_dwordx4 %0, %1 offset:2048 sc1\n\tglobal_load_lds_dwordx4 %0, %1 offset:3072 sc1"
                 ::"v"(vo), "s"(xsrc + ch * 4096), "s"(lb + (ch % 5) * 28672 + 12288 + (wid & 3) * 4096) : "memory");
  };
  if (wid < 4) {
    if (!is_pf) {
      if (wid == 0) STAMP(10);
      if (lane == 0) __hip_atomic_fetch_add(sw + 8, 1u, RLX_WG);
    }
    lds_spin(sw + 2, 1);
    const u64 tok = (u64)sw[0] | ((u64)sw[1] << 32);
    if (!is_pf) {
      asm volatile("s_waitcnt vmcnt(0) lgkmcnt(0)" ::: "memory");
      if (lane == 0) {
        if (__hip_atomic_fetch_add(sw + 3, 1u, RLX_WG) == 3) __hip_atomic_store(sync + myflag * FS, tok, RLX_AGENT);
      }
      if (wid == 0) STAMP(2);
    }
    if (wid == 0) {
      lds_spin(sw + 3, 4);
      if (TGATE) lds_spin(sw + 4, 1);
      const int n0 = (nb * 3) >> 2, n1 = (nb * 3 + 2) >> 2;
      poll_flags(sync + (FT + n0 * 10 + min(lane, (n1 - n0) * 10 + 9)) * FS, tok);
      if (lane == 0) __hip_atomic_store(sw + 5, 1u, RLX_WG);
      STAMP(4);
    } else {
      lds_spin(sw + 5, 1);
    }
    const char* tsrc = ws + nb * 122880 + wid * 3072;
    unsigned tdst = lb + wid * 3072;
    auto issue = [&]() {
      asm volatile("s_mov_b32 m0, %2\n\ts_nop 0\n\tglobal_load_lds_dwordx4 %0, %1 sc1\n\tglobal_load_lds_dwordx4 %0, %1 offset:1024 sc1\n\t"
                   "global_load_lds_dwordx4 %0, %1 offset:2048 sc1" ::"v"(vo), "s"(tsrc), "s"(tdst) : "memory");
      tsrc += 12288; tdst += 12288;
    };
    issue(); issue(); issue(); issue();
#pragma unroll 1
    for (int ch = 0; ch < 6; ++ch) {
      WAIT_V(9);
      BAR();
      issue();
    }
    WAIT_V(9); BAR(); WAIT_V(6); BAR(); WAIT_V(3); BAR(); WAIT_V(0); BAR();
  } else {
    const int w4 = wid - 4;
    if (wid == 4) {
      lds_spin(sw + 2, 1);
      const u64 tok = (u64)sw[0] | ((u64)sw[1] << 32);
      poll_flags(sync + (XRV ? FX + (mb >> 1) * 5 + min(lane, 4) : FX + mb * 4 + (lane & 3)) * FS, tok);
      lds_spin(sw + 8, 4);
      if (lane == 0) __hip_atomic_store(sw + 4, 1u, RLX_WG);
      STAMP(8);
    } else {
      lds_spin(sw + 4, 1);
    }
    u32x4 xf[40];
    const unsigned xo = XRF_OFF + (mb * 4 + w4) * 40960;
#pragma unroll
    for (int ch = 0; ch < 10; ++ch) {
#pragma unroll
      for (int i = 0; i < 4; ++i) xf[ch * 4 + i] = __builtin_amdgcn_raw_buffer_load_b128(wsr, lane * 16 + i * 1024, xo + ch * 4096, 16);
    }
    SB();
    if (wid == 4) STAMP(11);
    f32x16 acc[3] = {};
    u32x4 tf[3][3];
    lds_char* tb = smem + lane * 16;
    BAR();
    if (wid == 4) STAMP(9);
#pragma unroll
    for (int c = 0; c < 6; ++c) tf[c / 3][c % 3] = ldsr<u32x4>(tb + c * 1024);
#pragma unroll
    for (int ks = 0; ks < 40; ++ks) {
#pragma unroll
      for (int c = 0; c < 3; ++c) {
        acc[c] = mfma32(tf[ks % 3][c], xf[ks], acc[c]);
        SB();
        if (ks < 38) {
          if (c == 0 && (ks & 3) == 2) { BAR(); asm volatile("" ::: "memory"); }
          tf[(ks + 2) % 3][c] = ldsr<u32x4>(tb + ((ks + 2) * 3 + c) * 1024);
        }
        SB();
      }
    }
    if (wid == 4) STAMP(5);
    lds_char* eb = smem + (YV ? 0 : 122880) + w4 * 6656;
#pragma unroll
    for (int c = 0; c < 3; ++c) {
      ldsw<u32x4>(eb + r * 208 + c * 64 + h * 32, pack8(acc[c], 0));
      ldsw<u32x4>(eb + r * 208 + c * 64 + h * 32 + 16, pack8(acc[c], 1));
    }
    const __amdgpu_buffer_rsrc_t yr = __builtin_amdgcn_make_buffer_rsrc((void*)y, 0, 2048 * F_ * 2, 0x00020000);
    const unsigned yo = ((mb * 128 + w4 * 32) * F_ + nb * 96) * 2;
#pragma unroll
    for (int i = 0; i < 6; ++i) {
      int q = i * 64 + lane, row = q / 12, chn = q % 12;
      __builtin_amdgcn_raw_buffer_store_b128(ldsr<u32x4>(eb + row * 208 + chn * 16), yr, row * (F_ * 2) + chn * 16, yo, Y_AUX);
    }
    WAIT_V(0);
    if (wid == 4) STAMP(6);
    if (wid == 4 && blockIdx.x == AMP_BLK) {
      WAIT_L(0);
      unsigned d = __builtin_amdgcn_readfirstlane(sw[16 + AMP] - sw[16]);
      unsigned t0 = (unsigned)__builtin_amdgcn_s_memrealtime();
      while ((unsigned)__builtin_amdgcn_s_memrealtime() - t0 < AMPF * d + 300) __builtin_amdgcn_s_sleep(1);
    }
  }
}

extern "C" void kernel_launch(void* const* d_in, const int* in_sizes, int n_in, void* d_out, int out_size, void* d_ws,
                              size_t ws_size, hipStream_t stream) {
  (void)hipFuncSetAttribute((const void*)quip, hipFuncAttributeMaxDynamicSharedMemorySize, LDS_BYTES);
  quip<<<256, 512, LDS_BYTES, stream>>>((const __hip_bfloat16*)d_in[0], (const int*)d_in[1], (const __hip_bfloat16*)d_in[2],
                                        (const __hip_bfloat16*)d_in[3], (const __hip_bfloat16*)d_in[4], (const __hip_bfloat16*)d_in[5],
                                        (char*)d_ws, (__hip_bfloat16*)d_out);
}
```

```cpp
#include <hip/hip_runtime.h>
#include <hip/hip_bf16.h>

typedef __attribute__((ext_vector_type(8))) __bf16 bf16x8;
typedef __attribute__((ext_vector_type(2))) __bf16 bf16x2;
typedef __attribute__((ext_vector_type(4))) short s16x4;
typedef __attribute__((ext_vector_type(16))) float f32x16;
typedef __attribute__((ext_vector_type(4))) float f32x4;
typedef __attribute__((ext_vector_type(4))) unsigned u32x4;
typedef __attribute__((ext_vector_type(2))) unsigned u32x2;
typedef __attribute__((address_space(3))) s16x4 lds_s16x4;
typedef __attribute__((address_space(3))) char lds_char;
typedef __attribute__((address_space(3))) unsigned lds_u32;
typedef __attribute__((address_space(1))) unsigned long long gu64;
typedef unsigned long long u64;

#define STAMP(i)
#define Y_AUX 2
#define XCD_PERM 0x54321706
#define YPART 1
#define XRV 0
#define KICK 2
#define YV 0
#define TGATE 0
#define POLL_SLEEP 4
constexpr int D_ = 640, F_ = 1536;

#define WAIT_V(n) asm volatile("s_waitcnt vmcnt(" #n ")" ::: "memory")
#define WAIT_L(n) asm volatile("s_waitcnt lgkmcnt(" #n ")" ::: "memory")
#define BAR() __builtin_amdgcn_s_barrier()
#define SB() __builtin_amdgcn_sched_barrier(0)
#define RLX_AGENT __ATOMIC_RELAXED, __HIP_MEMORY_SCOPE_AGENT
#define RLX_WG __ATOMIC_RELAXED, __HIP_MEMORY_SCOPE_WORKGROUP

__device__ __forceinline__ void set_m0(unsigned ldsaddr) { asm volatile("s_mov_b32 m0, %0\n\ts_nop 0" ::"s"(ldsaddr) : "memory"); }
template <int OFF> __device__ __forceinline__ void glds(const void* sbase, unsigned voff) {
  asm volatile("global_load_lds_dwordx4 %0, %1 offset:%2" ::"v"(voff), "s"(sbase), "i"(OFF) : "memory");
}
__device__ __forceinline__ void touch(const void* sbase, unsigned voff) {
  asm volatile("global_load_lds_dword %0, %1" ::"v"(voff), "s"(sbase) : "memory");
}
__device__ __forceinline__ unsigned pack2(float a, float b) {
  bf16x2 p; p[0] = (__bf16)a; p[1] = (__bf16)b;
  return __builtin_bit_cast(unsigned, p);
}
__device__ __forceinline__ float bflo(unsigned u) { return __builtin_bit_cast(float, u << 16); }
__device__ __forceinline__ float bfhi(unsigned u) { return __builtin_bit_cast(float, u & 0xffff0000u); }
__device__ __forceinline__ u32x4 trread2(lds_char* p0, lds_char* p1) {
  u32x2 lo = __builtin_bit_cast(u32x2, __builtin_amdgcn_ds_read_tr16_b64_v4i16((lds_s16x4*)p0));
  u32x2 hi = __builtin_bit_cast(u32x2, __builtin_amdgcn_ds_read_tr16_b64_v4i16((lds_s16x4*)p1));
  return u32x4{lo[0], lo[1], hi[0], hi[1]};
}
template <typename T> __device__ __forceinline__ T ldsr(lds_char* p) { return *(__attribute__((address_space(3))) T*)p; }
template <typename T> __device__ __forceinline__ void ldsw(lds_char* p, T v) { *(__attribute__((address_space(3))) T*)p = v; }
__device__ __forceinline__ f32x16 mfma32(u32x4 a, u32x4 b, f32x16 c) {
  return __builtin_amdgcn_mfma_f32_32x32x16_bf16(__builtin_bit_cast(bf16x8, a), __builtin_bit_cast(bf16x8, b), c, 0, 0, 0);
}
__device__ __forceinline__ u32x4 pack8(const f32x16& a, int s) {
  u32x4 r;
  r[0] = pack2(a[8 * s + 0], a[8 * s + 1]); r[1] = pack2(a[8 * s + 2], a[8 * s + 3]);
  r[2] = pack2(a[8 * s + 4], a[8 * s + 5]); r[3] = pack2(a[8 * s + 6], a[8 * s + 7]);
  return r;
}
__device__ __forceinline__ u32x2 deq4(u32x4 q, f32x4 s, f32x4 z) {
  float w0, w1, w2, w3;
  asm("v_cvt_f32_i32 %0, %4\n\tv_cvt_f32_i32 %1, %5\n\tv_cvt_f32_i32 %2, %6\n\tv_cvt_f32_i32 %3, %7\n\t"
      "v_fma_f32 %0, %0, %8, %12\n\tv_fma_f32 %1, %1, %9, %13\n\tv_fma_f32 %2, %2, %10, %14\n\tv_fma_f32 %3, %3, %11, %15\n\t"
      "v_cvt_pk_bf16_f32 %0, %0, %1\n\tv_cvt_pk_bf16_f32 %1, %2, %3"
      : "=&v"(w0), "=&v"(w1), "=&v"(w2), "=&v"(w3)
      : "v"(q[0]), "v"(q[1]), "v"(q[2]), "v"(q[3]), "v"(s[0]), "v"(s[1]), "v"(s[2]), "v"(s[3]), "v"(z[0]), "v"(z[1]), "v"(z[2]), "v"(z[3]));
  return u32x2{__builtin_bit_cast(unsigned, w0), __builtin_bit_cast(unsigned, w1)};
}
__device__ __forceinline__ void lds_spin(lds_u32* w, unsigned want) {
  while ((unsigned)__builtin_amdgcn_readfirstlane(__hip_atomic_load(w, RLX_WG)) < want) __builtin_amdgcn_s_sleep(1);
  asm volatile("" ::: "memory");
}
__device__ __forceinline__ void poll_flags(gu64* f, u64 tok) {
#pragma unroll 1
  for (unsigned spins = 0; spins < (1u << 20); ++spins) {
    if (__all(__hip_atomic_load(f, RLX_AGENT) == tok)) break;
    __builtin_amdgcn_s_sleep(POLL_SLEEP);
  }
  __builtin_amdgcn_fence(__ATOMIC_ACQUIRE, "wavefront");
}

constexpr int XRF_OFF = 2 << 20, SYNC_OFF = 6 << 20;
#define FS 16
constexpr int FT = 1, FX = 121;
constexpr int FA = 185 * FS;
constexpr unsigned CODE_TOUCH_MAX = 14464;
constexpr int L_SYNC = 156160, L_DUMP = 156416, LDS_BYTES = 156672;

__global__ __launch_bounds__(512, 2) void quip(const __hip_bfloat16* __restrict__ x, const int* __restrict__ qw,
                                               const __hip_bfloat16* __restrict__ scales, const __hip_bfloat16* __restrict__ zeros,
                                               const __hip_bfloat16* __restrict__ U, const __hip_bfloat16* __restrict__ V,
                                               char* ws, __hip_bfloat16* __restrict__ y) {
  extern __shared__ __attribute__((aligned(16))) char smem_[];
  lds_char* smem = (lds_char*)smem_;
  const unsigned lb = (unsigned)(size_t)smem;
  const int wid = __builtin_amdgcn_readfirstlane(threadIdx.x >> 6), lane = threadIdx.x & 63;
  const int xcd = (XCD_PERM >> ((blockIdx.x & 7) * 4)) & 7, idx = blockIdx.x >> 3;
  {
    u64 pc;
    asm volatile("s_mov_b32 m0, %3\n\ts_cmp_gt_u32 %2, 7\n\ts_cbranch_scc1 1f\n\ts_getpc_b64 %0\n\ts_nop 0\n\tglobal_load_lds_dword %1, %0\n1:"
                 : "=&s"(pc) : "v"(min(threadIdx.x * 128, CODE_TOUCH_MAX)), "s"(blockIdx.x), "s"(lb + L_DUMP) : "memory", "scc");
    SB();
  }
  if (KICK && wid < (xcd < 6 ? (idx < 12 ? 4 : 0) : (KICK == 2 ? 5 : 0))) {
    const char* kb;
    unsigned ko, sh, rs, lim = 0xffffffffu;
    if (xcd < 6) {
      const int t = wid * 12 + idx, u = t - 8, p = (u * 57) >> 10, j = u - p * 18;
      kb = t < 4 ? (const char*)scales : t < 8 ? (const char*)zeros : j < 10 ? (const char*)qw : (const char*)V;
      ko = t < 8 ? (xcd & 1) * 30720 + (t & 3) * 8192
                 : j < 10 ? ((xcd & 1) * 320 + j * 32) * (F_ * 4) + p * 256 : (p * 64 + (j - 10) * 8) * (F_ * 2) + (xcd >> 1) * 1024;
      sh = t < 8 ? 6 : j < 10 ? 1 : 3;
      rs = j < 10 ? F_ * 4 : F_ * 2;
      if (t < 8 && (t & 3) == 3) lim = 6016;
    } else {
      int k0 = (idx & 7) + (((idx >> 3) * 5 + 1) >> 1);
      if (k0 >= 10) k0 -= 10;
      kb = wid < 2 ? (const char*)x : (const char*)U;
      ko = wid < 2 ? (((xcd - 6) * 8 + (idx & 7)) * 128 + wid * 64) * (D_ * 2) + k0 * 128
                   : k0 * 64 * (D_ * 2) + (((idx >> 3) * 320) & ~127) + (wid - 2) * 128;
      sh = 0; rs = D_ * 2;
    }
    touch(kb + ko, min((lane >> sh) * rs + ((lane & ((1u << sh) - 1)) << 7), lim));
  }
  const int h = lane >> 5, r = lane & 31;
  const int blk = (lane >> 4) & 1, q4 = (lane & 15) >> 2, p4 = lane & 3;
  lds_u32* sw = (lds_u32*)(smem + L_SYNC);
  gu64* sync = (gu64*)(ws + SYNC_OFF);
  const __amdgpu_buffer_rsrc_t wsr = __builtin_amdgcn_make_buffer_rsrc(ws, 0, SYNC_OFF, 0x00020000);
  const bool is_pf = idx < 12 && xcd < 6;
  if (wid == 0) STAMP(0);

  auto put_token = [&](u64 epoch) {
    if (wid == 3 && lane == 0) {
      const u64 e = epoch + 0x9E3779B97F4A7C15ull;
      sw[0] = (unsigned)e; sw[1] = (unsigned)(e >> 32);
      __hip_atomic_store(sw + 2, 1u, RLX_WG);
      __hip_atomic_store(sync + FA + blockIdx.x, e, RLX_AGENT);
    }
  };
  if (wid == 3 && lane < 16) sw[lane] = ((lane == 3 || lane == 8) && is_pf) ? 4u : 0u;
  int myflag = 0;

  if (is_pf) {
    if (wid == 0) {
      const unsigned qo = (lane >> 1) * (F_ * 4) + (lane & 1) * 128, vo_ = (lane >> 3) * (F_ * 2) + (lane & 7) * 128;
      const char* qb = (const char*)qw + (size_t)((xcd & 1) * 320) * (F_ * 4);
      const char* vb = (const char*)V + (xcd >> 1) * 1024;
      int p = KICK ? 2 : 0, j = (KICK ? 4 : 0) + idx;
      BAR();
#pragma unroll 1
      while (true) {
        if (j >= 18) { j -= 18; if (++p == 24) break; }
        if (j < 10) touch(qb + j * (32 * F_ * 4) + p * 256, qo);
        else touch(vb + (size_t)(p * 64 + (j - 10) * 8) * (F_ * 2), vo_);
        j += 12;
      }
      STAMP(7);
    } else {
      WAIT_L(0);
      BAR();
      if (wid == 3) put_token(__hip_atomic_load(sync, RLX_AGENT));
      if (blockIdx.x == 1 && wid == 2) {
        lds_spin(sw + 2, 1);
        const u64 tok = (u64)sw[0] | ((u64)sw[1] << 32);
#pragma unroll 1
        for (int i = 0; i < 4; ++i) poll_flags(sync + FA + i * 64 + lane, tok);
        if (lane == 0) __hip_atomic_store(sync, tok, RLX_AGENT);
      }
    }
  } else if (xcd < 6) {
    const int t = idx - 12;
    const int mt = (xcd & 1) * 5 + t % 5, nt = (xcd >> 1) * 4 + t / 5;
    myflag = FT + nt * 10 + mt;
    if (wid >= 4) {
      const int w4 = wid - 4, rr = lane >> 4, c16 = lane & 15;
      unsigned ag[4], bg[4];
#pragma unroll
      for (int i = 0; i < 4; ++i) {
        int row = i * 4 + rr;
        ag[i] = row * (F_ * 4) + ((c16 ^ row) * 16) - i * 1024;
        bg[i] = row * (F_ * 2) + ((c16 ^ (rr << 2)) * 16) - i * 1024;
      }
      const char* ap = (const char*)qw + (size_t)(mt * 64 + w4 * 16) * (F_ * 4);
      const char* bp = (const char*)V + nt * 256 + (size_t)(w4 * 16) * (F_ * 2);
      unsigned so = lb + w4 * 4096;
      auto issue = [&](int p) {
        unsigned base = so + (p & 3) * 32768;
        set_m0(base);
        glds<0>(ap, ag[0]); glds<1024>(ap, ag[1]); glds<2048>(ap, ag[2]); glds<3072>(ap, ag[3]);
        set_m0(base + 16384);
        glds<0>(bp, bg[0]); glds<1024>(bp, bg[1]); glds<2048>(bp, bg[2]); glds<3072>(bp, bg[3]);
        ap += 256; bp += 64 * F_ * 2;
      };
      issue(0);
      issue(1);
      BAR();
#pragma unroll 1
      for (int p = 2; p < 24; ++p) {
        WAIT_V(8);
        BAR();
        issue(p);
      }
      WAIT_V(8);
      BAR();
      WAIT_V(0);
      BAR();
    } else {
      const int m = wid & 1, kl = wid >> 1;
      {
        const size_t o = (size_t)((mt * 2 + m) * F_ + kl * 768) * 2;
        lds_char* T = smem + 131072 + m * 12288 + kl * 3072;
        u32x4 s8[2], z8[2];
#pragma unroll
        for (int j = 0; j < 2; ++j) {
          int e = j ? 64 + (lane & 31) : lane;
          s8[j] = *(const u32x4*)((const char*)scales + o + e * 16);
          z8[j] = *(const u32x4*)((const char*)zeros + o + e * 16);
        }
        const u64 epoch = __hip_atomic_load(sync, RLX_AGENT);
#pragma unroll
        for (int j = 0; j < 2; ++j) {
          int e = j ? 64 + (lane & 31) : lane;
          f32x4 sa, sb, za, zb;
          sa[0] = bflo(s8[j][0]); sa[1] = bfhi(s8[j][0]); sa[2] = bflo(s8[j][1]); sa[3] = bfhi(s8[j][1]);
          sb[0] = bflo(s8[j][2]); sb[1] = bfhi(s8[j][2]); sb[2] = bflo(s8[j][3]); sb[3] = bfhi(s8[j][3]);
          za[0] = -bflo(z8[j][0]) * sa[0]; za[1] = -bfhi(z8[j][0]) * sa[1]; za[2] = -bflo(z8[j][1]) * sa[2]; za[3] = -bfhi(z8[j][1]) * sa[3];
          zb[0] = -bflo(z8[j][2]) * sb[0]; zb[1] = -bfhi(z8[j][2]) * sb[1]; zb[2] = -bflo(z8[j][3]) * sb[2]; zb[3] = -bfhi(z8[j][3]) * sb[3];
          ldsw<f32x4>(T + e * 32, sa); ldsw<f32x4>(T + e * 32 + 16, sb);
          ldsw<f32x4>(T + 6144 + e * 32, za); ldsw<f32x4>(T + 6144 + e * 32 + 16, zb);
        }
        put_token(epoch);
        WAIT_L(0);
        BAR();
      }
      int aoff[4], boff[4];
#pragma unroll
      for (int c = 0; c < 4; ++c) {
        int ch = 8 * kl + 4 * (c >> 1) + 2 * h + (c & 1);
        aoff[c] = (m * 32 + r) * 256 + ((ch ^ (r & 15)) * 16);
        boff[c] = 16384 + (32 * kl + 8 * h + q4) * 256 + ((c ^ (2 * kl) ^ q4) * 64) + 32 * (p4 & 1) + 16 * blk + 8 * (p4 >> 1);
      }
      lds_char* tp = smem + 131072 + m * 12288 + (32 * kl + 8 * h) * 4;
      f32x16 acc[4] = {};
      f32x4 ts[4], tz[4];
      u32x4 av[4];
      u32x4 af[2][2] = {}, bf[2][2][4] = {};
      auto rdtab = [&](int o, int c) {
        ts[c] = ldsr<f32x4>(tp + o + (c >> 1) * 64 + (c & 1) * 16);
        tz[c] = ldsr<f32x4>(tp + o + 6144 + (c >> 1) * 64 + (c & 1) * 16);
      };
      auto step = [&](int slot) {
        const int buf = slot & 1;
        lds_char* sl = smem + slot * 32768;
        BAR();
#pragma unroll
        for (int c = 0; c < 4; ++c) av[c] = ldsr<u32x4>(sl + aoff[c]);
        SB();
#pragma unroll
        for (int u = 0; u < 8; ++u) {
          acc[u & 3] = mfma32(af[buf ^ 1][u >> 2], bf[buf ^ 1][u >> 2][u & 3], acc[u & 3]);
          SB();
          if (u < 4) {
#pragma unroll
            for (int v = 2 * u; v < 2 * u + 2; ++v)
              bf[buf][v >> 2][v & 3] = trread2(sl + boff[v & 3] + (16 * (v >> 2)) * 256, sl + boff[v & 3] + (16 * (v >> 2) + 4) * 256);
          } else {
            const int c = u - 4;
            u32x2 o = deq4(av[c], ts[c], tz[c]);
            af[buf][c >> 1][(c & 1) * 2] = o[0]; af[buf][c >> 1][(c & 1) * 2 + 1] = o[1];
            SB();
            rdtab((slot + 1) * 256, c);
          }
          SB();
        }
      };
      if (wid == 0) STAMP(9);
#pragma unroll
      for (int c = 0; c < 4; ++c) rdtab(0, c);
#pragma unroll 1
      for (int p = 0; p < 24; p += 4) {
        step(0); step(1); step(2); step(3);
        tp += 1024;
      }
      if (wid == 0) STAMP(1);
      lds_char* RED = smem + m * 16384 + lane * 16;
      const int n32 = nt * 4 + kl * 2;
      const unsigned oo0 = (((n32 / 3) * 40 + (mt * 2 + m) * 2) * 3 + n32 % 3) * 1024;
      const unsigned oo1 = ((((n32 + 1) / 3) * 40 + (mt * 2 + m) * 2) * 3 + (n32 + 1) % 3) * 1024;
      SB();
#pragma unroll
      for (int u = 0; u < 8; ++u) {
        const int c = (u < 4 ? 2 : 0) + ((u >> 1) & 1), s2 = u & 1;
        acc[c] = mfma32(af[1][s2], bf[1][s2][c], acc[c]);
        SB();
        if (u >= 3 && u < 7) {
#pragma unroll
          for (int j = 2 * (u - 3); j < 2 * (u - 3) + 2; ++j) {
            const f32x16& g = acc[2 + (j >> 2)];
            const int q = 4 * (j & 3);
            ldsw<f32x4>(RED + kl * 8192 + j * 1024, f32x4{g[q], g[q + 1], g[q + 2], g[q + 3]});
          }
          SB();
        }
      }
      asm volatile("" ::: "memory");
      __hip_atomic_store(sw + 9 + wid, 1u, RLX_WG);
      while (__builtin_amdgcn_readfirstlane(__hip_atomic_load(sw + 9 + (wid ^ 2), RLX_WG)) == 0) {}
      asm volatile("" ::: "memory");
      f32x4 pv[8];
#pragma unroll
      for (int j = 0; j < 8; ++j) pv[j] = ldsr<f32x4>(RED + (kl ^ 1) * 8192 + j * 1024);
      SB();
#pragma unroll
      for (int j = 0; j < 4; ++j) {
        f32x16& o = acc[j >> 1];
#pragma unroll
        for (int e = 0; e < 8; ++e) o[(j & 1) * 8 + e] += pv[2 * j + (e >> 2)][e & 3];
        __builtin_amdgcn_raw_buffer_store_b128(pack8(o, j & 1), wsr, lane * 16 + (j & 1) * 3072, j < 2 ? oo0 : oo1, 16);
      }
    }
  } else {
    const int mi = idx & 7, eb = idx >> 3;
    const int xmb = (xcd - 6) * 8 + mi;
    myflag = FX + xmb * 4 + eb;
    if (wid >= 4) {
      const int w4 = wid - 4;
      unsigned xg[4], ug[5];
#pragma unroll
      for (int i = 0; i < 4; ++i) {
        int row = i * 8 + (lane >> 3);
        xg[i] = row * (D_ * 2) + (((lane & 7) ^ ((row >> 1) & 7)) * 16) - i * 1024;
      }
#pragma unroll
      for (int i = 0; i < 5; ++i) {
        int off = i * 1024 + lane * 16;
        ug[i] = (off / 320) * (D_ * 2) + off % 320 - (i & 3) * 1024;
      }
      const char* xb = (const char*)x + (size_t)(xmb * 128 + w4 * 32) * (D_ * 2);
      const char* ub = (const char*)U + eb * 320 + (size_t)(w4 * 16) * (D_ * 2);
      int ks = mi + ((eb * 5 + 1) >> 1);
      if (ks >= 10) ks -= 10;
      auto issue = [&](int st) {
        unsigned base = lb + (st & 3) * 36864;
        const char* xp = xb + ks * 128;
        const char* up = ub + (size_t)ks * 64 * (D_ * 2);
        set_m0(base + w4 * 4096);
        glds<0>(xp, xg[0]); glds<1024>(xp, xg[1]); glds<2048>(xp, xg[2]); glds<3072>(xp, xg[3]);
        set_m0(base + 16384 + w4 * 5120);
        glds<0>(up, ug[0]); glds<1024>(up, ug[1]); glds<2048>(up, ug[2]); glds<3072>(up, ug[3]);
        set_m0(base + 16384 + w4 * 5120 + 4096);
        glds<0>(up, ug[4]);
        if (++ks == 10) ks = 0;
      };
      issue(0);
#pragma unroll 1
      for (int st = 1; st < 10; ++st) {
        issue(st);
        WAIT_V(9);
        BAR();
      }
      WAIT_V(0);
      BAR();
    } else {
      int xoff[4];
#pragma unroll
      for (int s = 0; s < 4; ++s) xoff[s] = (wid * 32 + r) * 128 + (((2 * s + h) ^ ((r >> 1) & 7)) * 16);
      const int uoff = 16384 + (8 * h + q4) * 320 + blk * 32 + p4 * 8;
      f32x16 acc[5] = {};
      u32x4 xf[2], uf[2][5];
      auto rdu = [&](lds_char* base, int s, int buf, int c) {
        uf[buf][c] = trread2(base + uoff + c * 64 + (16 * s) * 320, base + uoff + c * 64 + (16 * s + 4) * 320);
      };
      if (wid == 3) put_token(__hip_atomic_load(sync, RLX_AGENT));
      WAIT_L(0);
      BAR();
      xf[0] = ldsr<u32x4>(smem + xoff[0]);
#pragma unroll
      for (int c = 0; c < 5; ++c) rdu(smem, 0, 0, c);
#pragma unroll 1
      for (int st = 0; st < 10; ++st) {
#pragma unroll
        for (int s = 0; s < 4; ++s) {
          lds_char* base = smem + ((s < 3 ? st : st + 1) & 3) * 36864;
          const int nx = (s + 1) & 3, nb_ = (s + 1) & 1;
#pragma unroll
          for (int c = 0; c < 5; ++c) {
            acc[c] = mfma32(uf[s & 1][c], xf[s & 1], acc[c]);
            SB();
            if (c == 0) {
              if (s == 3 && st < 9) { BAR(); asm volatile("" ::: "memory"); }
              xf[nb_] = ldsr<u32x4>(base + xoff[nx]);
              rdu(base, nx, nb_, 0);
            } else if (c < 4) {
              rdu(base, nx, nb_, c);
              if (c == 3) rdu(base, nx, nb_, 4);
            }
            SB();
          }
        }
      }
#pragma unroll
      for (int c = 0; c < 5; ++c) {
        const unsigned o = XRF_OFF + ((xmb * 4 + wid) * 40 + (eb * 5 + c) * 2) * 1024;
        __builtin_amdgcn_raw_buffer_store_b128(pack8(acc[c], 0), wsr, lane * 16, o, 16);
        __builtin_amdgcn_raw_buffer_store_b128(pack8(acc[c], 1), wsr, lane * 16 + 1024, o, 16);
      }
      if (wid == 0) STAMP(3);
    }
  }

  const int mb = YPART == 0 ? (xcd >> 1) * 4 + (idx & 3) : YPART == 1 ? (xcd >> 2) * 8 + (idx & 7) : idx & 15;
  const int nb = YPART == 0 ? (xcd & 1) * 8 + (idx >> 2) : YPART == 1 ? (xcd & 3) * 4 + (idx >> 3) : xcd * 2 + (idx >> 4);
  const unsigned vo = lane * 16;
  const char* xsrc = ws + XRF_OFF + (mb * 4 + (wid & 3)) * 40960;
  auto ldx = [&](int ch) {
    asm volatile("s_mov_b32 m0, %2\n\ts_nop 0\n\tglobal_load_lds_dwordx4 %0, %1 sc1\n\tglobal_load_lds_dwordx4 %0, %1 offset:1024 sc1\n\t"
                 "global_load_lds_dwordx4 %0, %1 offset:2048 sc1\n\tglobal_load_lds_dwordx4 %0, %1 offset:3072 sc1"
                 ::"v"(vo), "s"(xsrc + ch * 4096), "s"(lb + (ch % 5) * 28672 + 12288 + (wid & 3) * 4096) : "memory");
  };
  if (wid < 4) {
    if (!is_pf) {
      if (wid == 0) STAMP(10);
      if (lane == 0) __hip_atomic_fetch_add(sw + 8, 1u, RLX_WG);
    }
    lds_spin(sw + 2, 1);
    const u64 tok = (u64)sw[0] | ((u64)sw[1] << 32);
    if (!is_pf) {
      asm volatile("s_waitcnt vmcnt(0) lgkmcnt(0)" ::: "memory");
      if (lane == 0) {
        if (__hip_atomic_fetch_add(sw + 3, 1u, RLX_WG) == 3) __hip_atomic_store(sync + myflag * FS, tok, RLX_AGENT);
      }
      if (wid == 0) STAMP(2);
    }
    if (wid == 0) {
      lds_spin(sw + 3, 4);
      if (TGATE) lds_spin(sw + 4, 1);
      const int n0 = (nb * 3) >> 2, n1 = (nb * 3 + 2) >> 2;
      poll_flags(sync + (FT + n0 * 10 + min(lane, (n1 - n0) * 10 + 9)) * FS, tok);
      if (lane == 0) __hip_atomic_store(sw + 5, 1u, RLX_WG);
      STAMP(4);
    } else {
      lds_spin(sw + 5, 1);
    }
    const char* tsrc = ws + nb * 122880 + wid * 3072;
    unsigned tdst = lb + wid * 3072;
    auto issue = [&]() {
      asm volatile("s_mov_b32 m0, %2\n\ts_nop 0\n\tglobal_load_lds_dwordx4 %0, %1 sc1\n\tglobal_load_lds_dwordx4 %0, %1 offset:1024 sc1\n\t"
                   "global_load_lds_dwordx4 %0, %1 offset:2048 sc1" ::"v"(vo), "s"(tsrc), "s"(tdst) : "memory");
      tsrc += 12288; tdst += 12288;
    };
    issue(); issue(); issue(); issue();
#pragma unroll 1
    for (int ch = 0; ch < 6; ++ch) {
      WAIT_V(9);
      BAR();
      issue();
    }
    WAIT_V(9); BAR(); WAIT_V(6); BAR(); WAIT_V(3); BAR(); WAIT_V(0); BAR();
  } else {
    const int w4 = wid - 4;
    if (wid == 4) {
      lds_spin(sw + 2, 1);
      const u64 tok = (u64)sw[0] | ((u64)sw[1] << 32);
      poll_flags(sync + (FX + mb * 4 + (lane & 3)) * FS, tok);
      lds_spin(sw + 8, 4);
      if (lane == 0) __hip_atomic_store(sw + 4, 1u, RLX_WG);
      STAMP(8);
    } else {
      lds_spin(sw + 4, 1);
    }
    u32x4 xf[40];
    const unsigned xo = XRF_OFF + (mb * 4 + w4) * 40960;
#pragma unroll
    for (int ch = 0; ch < 10; ++ch) {
#pragma unroll
      for (int i = 0; i < 4; ++i) xf[ch * 4 + i] = __builtin_amdgcn_raw_buffer_load_b128(wsr, lane * 16 + i * 1024, xo + ch * 4096, 16);
    }
    SB();
    if (wid == 4) STAMP(11);
    f32x16 acc[3] = {};
    u32x4 tf[3][3];
    lds_char* tb = smem + lane * 16;
    BAR();
#pragma unroll
    for (int c = 0; c < 6; ++c) tf[c / 3][c % 3] = ldsr<u32x4>(tb + c * 1024);
#pragma unroll
    for (int ks = 0; ks < 40; ++ks) {
#pragma unroll
      for (int c = 0; c < 3; ++c) {
        acc[c] = mfma32(tf[ks % 3][c], xf[ks], acc[c]);
        SB();
        if (ks < 38) {
          if (c == 0 && (ks & 3) == 2) { BAR(); asm volatile("" ::: "memory"); }
          tf[(ks + 2) % 3][c] = ldsr<u32x4>(tb + ((ks + 2) * 3 + c) * 1024);
        }
        SB();
      }
    }
    if (wid == 4) STAMP(5);
    lds_char* eb = smem + (YV ? 0 : 122880) + w4 * 6656;
#pragma unroll
    for (int c = 0; c < 3; ++c) {
      ldsw<u32x4>(eb + r * 208 + c * 64 + h * 32, pack8(acc[c], 0));
      ldsw<u32x4>(eb + r * 208 + c * 64 + h * 32 + 16, pack8(acc[c], 1));
    }
    const __amdgpu_buffer_rsrc_t yr = __builtin_amdgcn_make_buffer_rsrc((void*)y, 0, 2048 * F_ * 2, 0x00020000);
    const unsigned yo = ((mb * 128 + w4 * 32) * F_ + nb * 96) * 2;
#pragma unroll
    for (int i = 0; i < 6; ++i) {
      int q = i * 64 + lane, row = q / 12, chn = q % 12;
      __builtin_amdgcn_raw_buffer_store_b128(ldsr<u32x4>(eb + row * 208 + chn * 16), yr, row * (F_ * 2) + chn * 16, yo, Y_AUX);
    }
  }
}

extern "C" void kernel_launch(void* const* d_in, const int* in_sizes, int n_in, void* d_out, int out_size, void* d_ws,
                              size_t ws_size, hipStream_t stream) {
  (void)hipFuncSetAttribute((const void*)quip, hipFuncAttributeMaxDynamicSharedMemorySize, LDS_BYTES);
  quip<<<256, 512, LDS_BYTES, stream>>>((const __hip_bfloat16*)d_in[0], (const int*)d_in[1], (const __hip_bfloat16*)d_in[2],
                                        (const __hip_bfloat16*)d_in[3], (const __hip_bfloat16*)d_in[4], (const __hip_bfloat16*)d_in[5],
                                        (char*)d_ws, (__hip_bfloat16*)d_out);
}
```

```cpp
#define AMP 0
#define AMP_BLK 125
#define AMPF 5
#include <hip/hip_runtime.h>
#include <hip/hip_bf16.h>

typedef __attribute__((ext_vector_type(8))) __bf16 bf16x8;
typedef __attribute__((ext_vector_type(2))) __bf16 bf16x2;
typedef __attribute__((ext_vector_type(4))) short s16x4;
typedef __attribute__((ext_vector_type(16))) float f32x16;
typedef __attribute__((ext_vector_type(4))) float f32x4;
typedef __attribute__((ext_vector_type(4))) unsigned u32x4;
typedef __attribute__((ext_vector_type(2))) unsigned u32x2;
typedef __attribute__((address_space(3))) s16x4 lds_s16x4;
typedef __attribute__((address_space(3))) char lds_char;
typedef __attribute__((address_space(3))) unsigned lds_u32;
typedef __attribute__((address_space(1))) unsigned long long gu64;
typedef unsigned long long u64;

#define STAMP(i) do { if ((threadIdx.x & 63) == 0) sw[16 + (i)] = (unsigned)__builtin_amdgcn_s_memrealtime(); } while (0)
#define Y_AUX 2
#define XCD_PERM 0x54321706
#define YPART 1
#define XRV 0
#define KICK 0
#define YV 0
#define TGATE 0
#define POLL_SLEEP 4
constexpr int D_ = 640, F_ = 1536;

#define WAIT_V(n) asm volatile("s_waitcnt vmcnt(" #n ")" ::: "memory")
#define WAIT_L(n) asm volatile("s_waitcnt lgkmcnt(" #n ")" ::: "memory")
#define BAR() __builtin_amdgcn_s_barrier()
#define SB() __builtin_amdgcn_sched_barrier(0)
#define RLX_AGENT __ATOMIC_RELAXED, __HIP_MEMORY_SCOPE_AGENT
#define RLX_WG __ATOMIC_RELAXED, __HIP_MEMORY_SCOPE_WORKGROUP

__device__ __forceinline__ void set_m0(unsigned ldsaddr) { asm volatile("s_mov_b32 m0, %0\n\ts_nop 0" ::"s"(ldsaddr) : "memory"); }
template <int OFF> __device__ __forceinline__ void glds(const void* sbase, unsigned voff) {
  asm volatile("global_load_lds_dwordx4 %0, %1 offset:%2" ::"v"(voff), "s"(sbase), "i"(OFF) : "memory");
}
__device__ __forceinline__ void touch(const void* sbase, unsigned voff) {
  asm volatile("global_load_lds_dword %0, %1" ::"v"(voff), "s"(sbase) : "memory");
}
__device__ __forceinline__ unsigned pack2(float a, float b) {
  bf16x2 p; p[0] = (__bf16)a; p[1] = (__bf16)b;
  return __builtin_bit_cast(unsigned, p);
}
__device__ __forceinline__ float bflo(unsigned u) { return __builtin_bit_cast(float, u << 16); }
__device__ __forceinline__ float bfhi(unsigned u) { return __builtin_bit_cast(float, u & 0xffff0000u); }
__device__ __forceinline__ u32x4 trread2(lds_char* p0, lds_char* p1) {
  u32x2 lo = __builtin_bit_cast(u32x2, __builtin_amdgcn_ds_read_tr16_b64_v4i16((lds_s16x4*)p0));
  u32x2 hi = __builtin_bit_cast(u32x2, __builtin_amdgcn_ds_read_tr16_b64_v4i16((lds_s16x4*)p1));
  return u32x4{lo[0], lo[1], hi[0], hi[1]};
}
template <typename T> __device__ __forceinline__ T ldsr(lds_char* p) { return *(__attribute__((address_space(3))) T*)p; }
template <typename T> __device__ __forceinline__ void ldsw(lds_char* p, T v) { *(__attribute__((address_space(3))) T*)p = v; }
__device__ __forceinline__ f32x16 mfma32(u32x4 a, u32x4 b, f32x16 c) {
  return __builtin_amdgcn_mfma_f32_32x32x16_bf16(__builtin_bit_cast(bf16x8, a), __builtin_bit_cast(bf16x8, b), c, 0, 0, 0);
}
__device__ __forceinline__ u32x4 pack8(const f32x16& a, int s) {
  u32x4 r;
  r[0] = pack2(a[8 * s + 0], a[8 * s + 1]); r[1] = pack2(a[8 * s + 2], a[8 * s + 3]);
  r[2] = pack2(a[8 * s + 4], a[8 * s + 5]); r[3] = pack2(a[8 * s + 6], a[8 * s + 7]);
  return r;
}
__device__ __forceinline__ u32x2 deq4(u32x4 q, f32x4 s, f32x4 z) {
  float w0, w1, w2, w3;
  asm("v_cvt_f32_i32 %0, %4\n\tv_cvt_f32_i32 %1, %5\n\tv_cvt_f32_i32 %2, %6\n\tv_cvt_f32_i32 %3, %7\n\t"
      "v_fma_f32 %0, %0, %8, %12\n\tv_fma_f32 %1, %1, %9, %13\n\tv_fma_f32 %2, %2, %10, %14\n\tv_fma_f32 %3, %3, %11, %15\n\t"
      "v_cvt_pk_bf16_f32 %0, %0, %1\n\tv_cvt_pk_bf16_f32 %1, %2, %3"
      : "=&v"(w0), "=&v"(w1), "=&v"(w2), "=&v"(w3)
      : "v"(q[0]), "v"(q[1]), "v"(q[2]), "v"(q[3]), "v"(s[0]), "v"(s[1]), "v"(s[2]), "v"(s[3]), "v"(z[0]), "v"(z[1]), "v"(z[2]), "v"(z[3]));
  return u32x2{__builtin_bit_cast(unsigned, w0), __builtin_bit_cast(unsigned, w1)};
}
__device__ __forceinline__ void lds_spin(lds_u32* w, unsigned want) {
  while ((unsigned)__builtin_amdgcn_readfirstlane(__hip_atomic_load(w, RLX_WG)) < want) __builtin_amdgcn_s_sleep(1);
  asm volatile("" ::: "memory");
}
__device__ __forceinline__ void poll_flags(gu64* f, u64 tok) {
#pragma unroll 1
  for (unsigned spins = 0; spins < (1u << 20); ++spins) {
    if (__all(__hip_atomic_load(f, RLX_AGENT) == tok)) break;
    __builtin_amdgcn_s_sleep(POLL_SLEEP);
  }
  __builtin_amdgcn_fence(__ATOMIC_ACQUIRE, "wavefront");
}

constexpr int XRF_OFF = 2 << 20, SYNC_OFF = 6 << 20;
#define FS 16
constexpr int FT = 1, FX = 121;
constexpr int FA = 185 * FS;
constexpr unsigned CODE_TOUCH_MAX = 14464;
constexpr int L_SYNC = 156160, L_DUMP = 156416, LDS_BYTES = 156672;

__global__ __launch_bounds__(512, 2) void quip(const __hip_bfloat16* __restrict__ x, const int* __restrict__ qw,
                                               const __hip_bfloat16* __restrict__ scales, const __hip_bfloat16* __restrict__ zeros,
                                               const __hip_bfloat16* __restrict__ U, const __hip_bfloat16* __restrict__ V,
                                               char* ws, __hip_bfloat16* __restrict__ y) {
  extern __shared__ __attribute__((aligned(16))) char smem_[];
  lds_char* smem = (lds_char*)smem_;
  const unsigned lb = (unsigned)(size_t)smem;
  const int wid = __builtin_amdgcn_readfirstlane(threadIdx.x >> 6), lane = threadIdx.x & 63;
  const int xcd = (XCD_PERM >> ((blockIdx.x & 7) * 4)) & 7, idx = blockIdx.x >> 3;
  {
    u64 pc;
    asm volatile("s_mov_b32 m0, %3\n\ts_cmp_gt_u32 %2, 7\n\ts_cbranch_scc1 1f\n\ts_getpc_b64 %0\n\ts_nop 0\n\tglobal_load_lds_dword %1, %0\n1:"
                 : "=&s"(pc) : "v"(min(threadIdx.x * 128, CODE_TOUCH_MAX)), "s"(blockIdx.x), "s"(lb + L_DUMP) : "memory", "scc");
    SB();
  }
  if (KICK && wid < (xcd < 6 ? (idx < 12 ? 4 : 0) : (KICK == 2 ? 5 : 0))) {
    const char* kb;
    unsigned ko, sh, rs, lim = 0xffffffffu;
    if (xcd < 6) {
      const int t = wid * 12 + idx, u = t - 8, p = (u * 57) >> 10, j = u - p * 18;
      kb = t < 4 ? (const char*)scales : t < 8 ? (const char*)zeros : j < 10 ? (const char*)qw : (const char*)V;
      ko = t < 8 ? (xcd & 1) * 30720 + (t & 3) * 8192
                 : j < 10 ? ((xcd & 1) * 320 + j * 32) * (F_ * 4) + p * 256 : (p * 64 + (j - 10) * 8) * (F_ * 2) + (xcd >> 1) * 1024;
      sh = t < 8 ? 6 : j < 10 ? 1 : 3;
      rs = j < 10 ? F_ * 4 : F_ * 2;
      if (t < 8 && (t & 3) == 3) lim = 6016;
    } else {
      int k0 = (idx & 7) + (((idx >> 3) * 5 + 1) >> 1);
      if (k0 >= 10) k0 -= 10;
      kb = wid < 2 ? (const char*)x : (const char*)U;
      ko = wid < 2 ? (((xcd - 6) * 8 + (idx & 7)) * 128 + wid * 64) * (D_ * 2) + k0 * 128
                   : k0 * 64 * (D_ * 2) + (((idx >> 3) * 320) & ~127) + (wid - 2) * 128;
      sh = 0; rs = D_ * 2;
    }
    touch(kb + ko, min((lane >> sh) * rs + ((lane & ((1u << sh) - 1)) << 7), lim));
  }
  const int h = lane >> 5, r = lane & 31;
  const int blk = (lane >> 4) & 1, q4 = (lane & 15) >> 2, p4 = lane & 3;
  lds_u32* sw = (lds_u32*)(smem + L_SYNC);
  gu64* sync = (gu64*)(ws + SYNC_OFF);
  const __amdgpu_buffer_rsrc_t wsr = __builtin_amdgcn_make_buffer_rsrc(ws, 0, SYNC_OFF, 0x00020000);
  const bool is_pf = idx < 12 && xcd < 6;
  if (wid == 0) STAMP(0);

  auto put_token = [&](u64 epoch) {
    if (wid == 3 && lane == 0) {
      const u64 e = epoch + 0x9E3779B97F4A7C15ull;
      sw[0] = (unsigned)e; sw[1] = (unsigned)(e >> 32);
      __hip_atomic_store(sw + 2, 1u, RLX_WG);
      __hip_atomic_store(sync + FA + blockIdx.x, e, RLX_AGENT);
    }
  };
  if (wid == 3 && lane < 16) sw[lane] = ((lane == 3 || lane == 8) && is_pf) ? 4u : 0u;
  int myflag = 0;

  if (is_pf) {
    if (wid == 0) {
      const unsigned qo = (lane >> 1) * (F_ * 4) + (lane & 1) * 128, vo_ = (lane >> 3) * (F_ * 2) + (lane & 7) * 128;
      const char* qb = (const char*)qw + (size_t)((xcd & 1) * 320) * (F_ * 4);
      const char* vb = (const char*)V + (xcd >> 1) * 1024;
      int p = KICK ? 2 : 0, j = (KICK ? 4 : 0) + idx;
      BAR();
#pragma unroll 1
      while (true) {
        if (j >= 18) { j -= 18; if (++p == 24) break; }
        if (j < 10) touch(qb + j * (32 * F_ * 4) + p * 256, qo);
        else touch(vb + (size_t)(p * 64 + (j - 10) * 8) * (F_ * 2), vo_);
        j += 12;
      }
      STAMP(7);
    } else {
      WAIT_L(0);
      BAR();
      if (wid == 3) put_token(__hip_atomic_load(sync, RLX_AGENT));
      if (blockIdx.x == 1 && wid == 2) {
        lds_spin(sw + 2, 1);
        const u64 tok = (u64)sw[0] | ((u64)sw[1] << 32);
#pragma unroll 1
        for (int i = 0; i < 4; ++i) poll_flags(sync + FA + i * 64 + lane, tok);
        if (lane == 0) __hip_atomic_store(sync, tok, RLX_AGENT);
      }
    }
  } else if (xcd < 6) {
    const int t = idx - 12;
    const int mt = (xcd & 1) * 5 + t % 5, nt = (xcd >> 1) * 4 + t / 5;
    myflag = FT + nt * 10 + mt;
    if (wid >= 4) {
      const int w4 = wid - 4, rr = lane >> 4, c16 = lane & 15;
      unsigned ag[4], bg[4];
#pragma unroll
      for (int i = 0; i < 4; ++i) {
        int row = i * 4 + rr;
        ag[i] = row * (F_ * 4) + ((c16 ^ row) * 16) - i * 1024;
        bg[i] = row * (F_ * 2) + ((c16 ^ (rr << 2)) * 16) - i * 1024;
      }
      const char* ap = (const char*)qw + (size_t)(mt * 64 + w4 * 16) * (F_ * 4);
      const char* bp = (const char*)V + nt * 256 + (size_t)(w4 * 16) * (F_ * 2);
      unsigned so = lb + w4 * 4096;
      auto issue = [&](int p) {
        unsigned base = so + (p & 3) * 32768;
        set_m0(base);
        glds<0>(ap, ag[0]); glds<1024>(ap, ag[1]); glds<2048>(ap, ag[2]); glds<3072>(ap, ag[3]);
        set_m0(base + 16384);
        glds<0>(bp, bg[0]); glds<1024>(bp, bg[1]); glds<2048>(bp, bg[2]); glds<3072>(bp, bg[3]);
        ap += 256; bp += 64 * F_ * 2;
      };
      if (wid == 4) STAMP(16);
      issue(0);
      issue(1);
      if (wid == 4) STAMP(17);
      BAR();
#pragma unroll 1
      for (int p = 2; p < 24; ++p) {
        WAIT_V(8);
        BAR();
        issue(p);
      }
      WAIT_V(8);
      BAR();
      WAIT_V(0);
      BAR();
    } else {
      const int m = wid & 1, kl = wid >> 1;
      {
        const size_t o = (size_t)((mt * 2 + m) * F_ + kl * 768) * 2;
        lds_char* T = smem + 131072 + m * 12288 + kl * 3072;
        u32x4 s8[2], z8[2];
        asm volatile("" :: "s"(scales), "s"(zeros) : "memory");
        if (wid == 0) STAMP(13);
#pragma unroll
        for (int j = 0; j < 2; ++j) {
          int e = j ? 64 + (lane & 31) : lane;
          s8[j] = *(const u32x4*)((const char*)scales + o + e * 16);
          z8[j] = *(const u32x4*)((const char*)zeros + o + e * 16);
        }
        const u64 epoch = __hip_atomic_load(sync, RLX_AGENT);
        SB(); WAIT_V(0); if (wid == 0) STAMP(14); SB();
#pragma unroll
        for (int j = 0; j < 2; ++j) {
          int e = j ? 64 + (lane & 31) : lane;
          f32x4 sa, sb, za, zb;
          sa[0] = bflo(s8[j][0]); sa[1] = bfhi(s8[j][0]); sa[2] = bflo(s8[j][1]); sa[3] = bfhi(s8[j][1]);
          sb[0] = bflo(s8[j][2]); sb[1] = bfhi(s8[j][2]); sb[2] = bflo(s8[j][3]); sb[3] = bfhi(s8[j][3]);
          za[0] = -bflo(z8[j][0]) * sa[0]; za[1] = -bfhi(z8[j][0]) * sa[1]; za[2] = -bflo(z8[j][1]) * sa[2]; za[3] = -bfhi(z8[j][1]) * sa[3];
          zb[0] = -bflo(z8[j][2]) * sb[0]; zb[1] = -bfhi(z8[j][2]) * sb[1]; zb[2] = -bflo(z8[j][3]) * sb[2]; zb[3] = -bfhi(z8[j][3]) * sb[3];
          ldsw<f32x4>(T + e * 32, sa); ldsw<f32x4>(T + e * 32 + 16, sb);
          ldsw<f32x4>(T + 6144 + e * 32, za); ldsw<f32x4>(T + 6144 + e * 32 + 16, zb);
        }
        put_token(epoch);
        WAIT_L(0);
        if (wid == 0) STAMP(15);
        BAR();
      }
      int aoff[4], boff[4];
#pragma unroll
      for (int c = 0; c < 4; ++c) {
        int ch = 8 * kl + 4 * (c >> 1) + 2 * h + (c & 1);
        aoff[c] = (m * 32 + r) * 256 + ((ch ^ (r & 15)) * 16);
        boff[c] = 16384 + (32 * kl + 8 * h + q4) * 256 + ((c ^ (2 * kl) ^ q4) * 64) + 32 * (p4 & 1) + 16 * blk + 8 * (p4 >> 1);
      }
      lds_char* tp = smem + 131072 + m * 12288 + (32 * kl + 8 * h) * 4;
      f32x16 acc[4] = {};
      f32x4 ts[4], tz[4];
      u32x4 av[4];
      u32x4 af[2][2] = {}, bf[2][2][4] = {};
      auto rdtab = [&](int o, int c) {
        ts[c] = ldsr<f32x4>(tp + o + (c >> 1) * 64 + (c & 1) * 16);
        tz[c] = ldsr<f32x4>(tp + o + 6144 + (c >> 1) * 64 + (c & 1) * 16);
      };
      auto step = [&](int slot) {
        const int buf = slot & 1;
        lds_char* sl = smem + slot * 32768;
        BAR();
#pragma unroll
        for (int c = 0; c < 4; ++c) av[c] = ldsr<u32x4>(sl + aoff[c]);
        SB();
#pragma unroll
        for (int u = 0; u < 8; ++u) {
          acc[u & 3] = mfma32(af[buf ^ 1][u >> 2], bf[buf ^ 1][u >> 2][u & 3], acc[u & 3]);
          SB();
          if (u < 4) {
#pragma unroll
            for (int v = 2 * u; v < 2 * u + 2; ++v)
              bf[buf][v >> 2][v & 3] = trread2(sl + boff[v & 3] + (16 * (v >> 2)) * 256, sl + boff[v & 3] + (16 * (v >> 2) + 4) * 256);
          } else {
            const int c = u - 4;
            u32x2 o = deq4(av[c], ts[c], tz[c]);
            af[buf][c >> 1][(c & 1) * 2] = o[0]; af[buf][c >> 1][(c & 1) * 2 + 1] = o[1];
            SB();
            rdtab((slot + 1) * 256, c);
          }
          SB();
        }
      };
      if (wid == 0) STAMP(9);
#pragma unroll
      for (int c = 0; c < 4; ++c) rdtab(0, c);
#pragma unroll 1
      for (int p = 0; p < 24; p += 4) {
        step(0); step(1); step(2); step(3);
        tp += 1024;
      }
      if (wid == 0) STAMP(1);
      lds_char* RED = smem + m * 16384 + lane * 16;
      const int n32 = nt * 4 + kl * 2;
      const unsigned oo0 = (((n32 / 3) * 40 + (mt * 2 + m) * 2) * 3 + n32 % 3) * 1024;
      const unsigned oo1 = ((((n32 + 1) / 3) * 40 + (mt * 2 + m) * 2) * 3 + (n32 + 1) % 3) * 1024;
      SB();
#pragma unroll
      for (int u = 0; u < 8; ++u) {
        const int c = (u < 4 ? 2 : 0) + ((u >> 1) & 1), s2 = u & 1;
        acc[c] = mfma32(af[1][s2], bf[1][s2][c], acc[c]);
        SB();
        if (u >= 3 && u < 7) {
#pragma unroll
          for (int j = 2 * (u - 3); j < 2 * (u - 3) + 2; ++j) {
            const f32x16& g = acc[2 + (j >> 2)];
            const int q = 4 * (j & 3);
            ldsw<f32x4>(RED + kl * 8192 + j * 1024, f32x4{g[q], g[q + 1], g[q + 2], g[q + 3]});
          }
          SB();
        }
      }
      asm volatile("" ::: "memory");
      __hip_atomic_store(sw + 9 + wid, 1u, RLX_WG);
      while (__builtin_amdgcn_readfirstlane(__hip_atomic_load(sw + 9 + (wid ^ 2), RLX_WG)) == 0) {}
      asm volatile("" ::: "memory");
      f32x4 pv[8];
#pragma unroll
      for (int j = 0; j < 8; ++j) pv[j] = ldsr<f32x4>(RED + (kl ^ 1) * 8192 + j * 1024);
      SB();
#pragma unroll
      for (int j = 0; j < 4; ++j) {
        f32x16& o = acc[j >> 1];
#pragma unroll
        for (int e = 0; e < 8; ++e) o[(j & 1) * 8 + e] += pv[2 * j + (e >> 2)][e & 3];
        __builtin_amdgcn_raw_buffer_store_b128(pack8(o, j & 1), wsr, lane * 16 + (j & 1) * 3072, j < 2 ? oo0 : oo1, 16);
      }
    }
  } else {
    const int mi = idx & 7, eb = idx >> 3;
    const int xmb = (xcd - 6) * 8 + mi;
    myflag = FX + xmb * 4 + eb;
    if (wid >= 4) {
      const int w4 = wid - 4;
      unsigned xg[4], ug[5];
#pragma unroll
      for (int i = 0; i < 4; ++i) {
        int row = i * 8 + (lane >> 3);
        xg[i] = row * (D_ * 2) + (((lane & 7) ^ ((row >> 1) & 7)) * 16) - i * 1024;
      }
#pragma unroll
      for (int i = 0; i < 5; ++i) {
        int off = i * 1024 + lane * 16;
        ug[i] = (off / 320) * (D_ * 2) + off % 320 - (i & 3) * 1024;
      }
      const char* xb = (const char*)x + (size_t)(xmb * 128 + w4 * 32) * (D_ * 2);
      const char* ub = (const char*)U + eb * 320 + (size_t)(w4 * 16) * (D_ * 2);
      int ks = mi + ((eb * 5 + 1) >> 1);
      if (ks >= 10) ks -= 10;
      auto issue = [&](int st) {
        unsigned base = lb + (st & 3) * 36864;
        const char* xp = xb + ks * 128;
        const char* up = ub + (size_t)ks * 64 * (D_ * 2);
        set_m0(base + w4 * 4096);
        glds<0>(xp, xg[0]); glds<1024>(xp, xg[1]); glds<2048>(xp, xg[2]); glds<3072>(xp, xg[3]);
        set_m0(base + 16384 + w4 * 5120);
        glds<0>(up, ug[0]); glds<1024>(up, ug[1]); glds<2048>(up, ug[2]); glds<3072>(up, ug[3]);
        set_m0(base + 16384 + w4 * 5120 + 4096);
        glds<0>(up, ug[4]);
        if (++ks == 10) ks = 0;
      };
      issue(0);
#pragma unroll 1
      for (int st = 1; st < 10; ++st) {
        issue(st);
        WAIT_V(9);
        BAR();
      }
      WAIT_V(0);
      BAR();
    } else {
      int xoff[4];
#pragma unroll
      for (int s = 0; s < 4; ++s) xoff[s] = (wid * 32 + r) * 128 + (((2 * s + h) ^ ((r >> 1) & 7)) * 16);
      const int uoff = 16384 + (8 * h + q4) * 320 + blk * 32 + p4 * 8;
      f32x16 acc[5] = {};
      u32x4 xf[2], uf[2][5];
      auto rdu = [&](lds_char* base, int s, int buf, int c) {
        uf[buf][c] = trread2(base + uoff + c * 64 + (16 * s) * 320, base + uoff + c * 64 + (16 * s + 4) * 320);
      };
      if (wid == 3) put_token(__hip_atomic_load(sync, RLX_AGENT));
      WAIT_L(0);
      BAR();
      xf[0] = ldsr<u32x4>(smem + xoff[0]);
#pragma unroll
      for (int c = 0; c < 5; ++c) rdu(smem, 0, 0, c);
#pragma unroll 1
      for (int st = 0; st < 10; ++st) {
#pragma unroll
        for (int s = 0; s < 4; ++s) {
          lds_char* base = smem + ((s < 3 ? st : st + 1) & 3) * 36864;
          const int nx = (s + 1) & 3, nb_ = (s + 1) & 1;
#pragma unroll
          for (int c = 0; c < 5; ++c) {
            acc[c] = mfma32(uf[s & 1][c], xf[s & 1], acc[c]);
            SB();
            if (c == 0) {
              if (s == 3 && st < 9) { BAR(); asm volatile("" ::: "memory"); }
              xf[nb_] = ldsr<u32x4>(base + xoff[nx]);
              rdu(base, nx, nb_, 0);
            } else if (c < 4) {
              rdu(base, nx, nb_, c);
              if (c == 3) rdu(base, nx, nb_, 4);
            }
            SB();
          }
        }
      }
#pragma unroll
      for (int c = 0; c < 5; ++c) {
        const unsigned o = XRF_OFF + ((xmb * 4 + wid) * 40 + (eb * 5 + c) * 2) * 1024;
        __builtin_amdgcn_raw_buffer_store_b128(pack8(acc[c], 0), wsr, lane * 16, o, 16);
        __builtin_amdgcn_raw_buffer_store_b128(pack8(acc[c], 1), wsr, lane * 16 + 1024, o, 16);
      }
      if (wid == 0) STAMP(3);
    }
  }

  const int mb = YPART == 0 ? (xcd >> 1) * 4 + (idx & 3) : YPART == 1 ? (xcd >> 2) * 8 + (idx & 7) : idx & 15;
  const int nb = YPART == 0 ? (xcd & 1) * 8 + (idx >> 2) : YPART == 1 ? (xcd & 3) * 4 + (idx >> 3) : xcd * 2 + (idx >> 4);
  const unsigned vo = lane * 16;
  const char* xsrc = ws + XRF_OFF + (mb * 4 + (wid & 3)) * 40960;
  auto ldx = [&](int ch) {
    asm volatile("s_mov_b32 m0, %2\n\ts_nop 0\n\tglobal_load_lds_dwordx4 %0, %1 sc1\n\tglobal_load_lds_dwordx4 %0, %1 offset:1024 sc1\n\t"
                 "global_load_lds_dwordx4 %0, %1 offset:2048 sc1\n\tglobal_load_lds_dwordx4 %0, %1 offset:3072 sc1"
                 ::"v"(vo), "s"(xsrc + ch * 4096), "s"(lb + (ch % 5) * 28672 + 12288 + (wid & 3) * 4096) : "memory");
  };
  if (wid < 4) {
    if (!is_pf) {
      if (wid == 0) STAMP(10);
      if (lane == 0) __hip_atomic_fetch_add(sw + 8, 1u, RLX_WG);
    }
    lds_spin(sw + 2, 1);
    const u64 tok = (u64)sw[0] | ((u64)sw[1] << 32);
    if (!is_pf) {
      asm volatile("s_waitcnt vmcnt(0) lgkmcnt(0)" ::: "memory");
      if (lane == 0) {
        if (__hip_atomic_fetch_add(sw + 3, 1u, RLX_WG) == 3) __hip_atomic_store(sync + myflag * FS, tok, RLX_AGENT);
      }
      if (wid == 0) STAMP(2);
    }
    if (wid == 0) {
      lds_spin(sw + 3, 4);
      if (TGATE) lds_spin(sw + 4, 1);
      const int n0 = (nb * 3) >> 2, n1 = (nb * 3 + 2) >> 2;
      poll_flags(sync + (FT + n0 * 10 + min(lane, (n1 - n0) * 10 + 9)) * FS, tok);
      if (lane == 0) __hip_atomic_store(sw + 5, 1u, RLX_WG);
      STAMP(4);
    } else {
      lds_spin(sw + 5, 1);
    }
    const char* tsrc = ws + nb * 122880 + wid * 3072;
    unsigned tdst = lb + wid * 3072;
    auto issue = [&]() {
      asm volatile("s_mov_b32 m0, %2\n\ts_nop 0\n\tglobal_load_lds_dwordx4 %0, %1 sc1\n\tglobal_load_lds_dwordx4 %0, %1 offset:1024 sc1\n\t"
                   "global_load_lds_dwordx4 %0, %1 offset:2048 sc1" ::"v"(vo), "s"(tsrc), "s"(tdst) : "memory");
      tsrc += 12288; tdst += 12288;
    };
    issue(); issue(); issue(); issue();
#pragma unroll 1
    for (int ch = 0; ch < 6; ++ch) {
      WAIT_V(9);
      BAR();
      issue();
    }
    WAIT_V(9); BAR(); WAIT_V(6); BAR(); WAIT_V(3); BAR(); WAIT_V(0); BAR();
  } else {
    const int w4 = wid - 4;
    if (wid == 4) {
      lds_spin(sw + 2, 1);
      const u64 tok = (u64)sw[0] | ((u64)sw[1] << 32);
      poll_flags(sync + (FX + mb * 4 + (lane & 3)) * FS, tok);
      lds_spin(sw + 8, 4);
      if (lane == 0) __hip_atomic_store(sw + 4, 1u, RLX_WG);
      STAMP(8);
    } else {
      lds_spin(sw + 4, 1);
    }
    u32x4 xf[40];
    const unsigned xo = XRF_OFF + (mb * 4 + w4) * 40960;
#pragma unroll
    for (int ch = 0; ch < 10; ++ch) {
#pragma unroll
      for (int i = 0; i < 4; ++i) xf[ch * 4 + i] = __builtin_amdgcn_raw_buffer_load_b128(wsr, lane * 16 + i * 1024, xo + ch * 4096, 16);
    }
    SB();
    if (wid == 4) STAMP(11);
    f32x16 acc[3] = {};
    u32x4 tf[3][3];
    lds_char* tb = smem + lane * 16;
    BAR();
#pragma unroll
    for (int c = 0; c < 6; ++c) tf[c / 3][c % 3] = ldsr<u32x4>(tb + c * 1024);
#pragma unroll
    for (int ks = 0; ks < 40; ++ks) {
#pragma unroll
      for (int c = 0; c < 3; ++c) {
        acc[c] = mfma32(tf[ks % 3][c], xf[ks], acc[c]);
        SB();
        if (ks < 38) {
          if (c == 0 && (ks & 3) == 2) { BAR(); asm volatile("" ::: "memory"); }
          tf[(ks + 2) % 3][c] = ldsr<u32x4>(tb + ((ks + 2) * 3 + c) * 1024);
        }
        SB();
      }
    }
    if (wid == 4) STAMP(5);
    lds_char* eb = smem + (YV ? 0 : 122880) + w4 * 6656;
#pragma unroll
    for (int c = 0; c < 3; ++c) {
      ldsw<u32x4>(eb + r * 208 + c * 64 + h * 32, pack8(acc[c], 0));
      ldsw<u32x4>(eb + r * 208 + c * 64 + h * 32 + 16, pack8(acc[c], 1));
    }
    const __amdgpu_buffer_rsrc_t yr = __builtin_amdgcn_make_buffer_rsrc((void*)y, 0, 2048 * F_ * 2, 0x00020000);
    const unsigned yo = ((mb * 128 + w4 * 32) * F_ + nb * 96) * 2;
#pragma unroll
    for (int i = 0; i < 6; ++i) {
      int q = i * 64 + lane, row = q / 12, chn = q % 12;
      __builtin_amdgcn_raw_buffer_store_b128(ldsr<u32x4>(eb + row * 208 + chn * 16), yr, row * (F_ * 2) + chn * 16, yo, Y_AUX);
    }
    WAIT_V(0);
    if (wid == 4) STAMP(6);
    if (wid == 4 && blockIdx.x == AMP_BLK) {
      WAIT_L(0);
      unsigned d = __builtin_amdgcn_readfirstlane(sw[16 + AMP] - sw[16]);
      unsigned t0 = (unsigned)__builtin_amdgcn_s_memrealtime();
      while ((unsigned)__builtin_amdgcn_s_memrealtime() - t0 < AMPF * d + 300) __builtin_amdgcn_s_sleep(1);
    }
  }
}

extern "C" void kernel_launch(void* const* d_in, const int* in_sizes, int n_in, void* d_out, int out_size, void* d_ws,
                              size_t ws_size, hipStream_t stream) {
  (void)hipFuncSetAttribute((const void*)quip, hipFuncAttributeMaxDynamicSharedMemorySize, LDS_BYTES);
  quip<<<256, 512, LDS_BYTES, stream>>>((const __hip_bfloat16*)d_in[0], (const int*)d_in[1], (const __hip_bfloat16*)d_in[2],
                                        (const __hip_bfloat16*)d_in[3], (const __hip_bfloat16*)d_in[4], (const __hip_bfloat16*)d_in[5],
                                        (char*)d_ws, (__hip_bfloat16*)d_out);
}
```

```cpp
#include <hip/hip_runtime.h>
#include <hip/hip_bf16.h>

typedef __attribute__((ext_vector_type(8))) __bf16 bf16x8;
typedef __attribute__((ext_vector_type(2))) __bf16 bf16x2;
typedef __attribute__((ext_vector_type(4))) short s16x4;
typedef __attribute__((ext_vector_type(16))) float f32x16;
typedef __attribute__((ext_vector_type(4))) float f32x4;
typedef __attribute__((ext_vector_type(4))) unsigned u32x4;
typedef __attribute__((ext_vector_type(2))) unsigned u32x2;
typedef __attribute__((address_space(3))) s16x4 lds_s16x4;
typedef __attribute__((address_space(3))) char lds_char;
typedef __attribute__((address_space(3))) unsigned lds_u32;
typedef __attribute__((address_space(1))) unsigned long long gu64;
typedef unsigned long long u64;

#define STAMP(i)
#define Y_AUX 2
#define XCD_PERM 0x54321706
#define YPART 1
#define XRV 0
#define KICK 0
#define YV 0
#define TGATE 0
#define POLL_SLEEP 4
constexpr int D_ = 640, F_ = 1536;

#define WAIT_V(n) asm volatile("s_waitcnt vmcnt(" #n ")" ::: "memory")
#define WAIT_L(n) asm volatile("s_waitcnt lgkmcnt(" #n ")" ::: "memory")
#define BAR() __builtin_amdgcn_s_barrier()
#define SB() __builtin_amdgcn_sched_barrier(0)
#define RLX_AGENT __ATOMIC_RELAXED, __HIP_MEMORY_SCOPE_AGENT
#define RLX_WG __ATOMIC_RELAXED, __HIP_MEMORY_SCOPE_WORKGROUP

__device__ __forceinline__ void set_m0(unsigned ldsaddr) { asm volatile("s_mov_b32 m0, %0\n\ts_nop 0" ::"s"(ldsaddr) : "memory"); }
template <int OFF> __device__ __forceinline__ void glds(const void* sbase, unsigned voff) {
  asm volatile("global_load_lds_dwordx4 %0, %1 offset:%2" ::"v"(voff), "s"(sbase), "i"(OFF) : "memory");
}
__device__ __forceinline__ void touch(const void* sbase, unsigned voff) {
  asm volatile("global_load_lds_dword %0, %1" ::"v"(voff), "s"(sbase) : "memory");
}
__device__ __forceinline__ unsigned pack2(float a, float b) {
  bf16x2 p; p[0] = (__bf16)a; p[1] = (__bf16)b;
  return __builtin_bit_cast(unsigned, p);
}
__device__ __forceinline__ float bflo(unsigned u) { return __builtin_bit_cast(float, u << 16); }
__device__ __forceinline__ float bfhi(unsigned u) { return __builtin_bit_cast(float, u & 0xffff0000u); }
__device__ __forceinline__ u32x4 trread2(lds_char* p0, lds_char* p1) {
  u32x2 lo = __builtin_bit_cast(u32x2, __builtin_amdgcn_ds_read_tr16_b64_v4i16((lds_s16x4*)p0));
  u32x2 hi = __builtin_bit_cast(u32x2, __builtin_amdgcn_ds_read_tr16_b64_v4i16((lds_s16x4*)p1));
  return u32x4{lo[0], lo[1], hi[0], hi[1]};
}
template <typename T> __device__ __forceinline__ T ldsr(lds_char* p) { return *(__attribute__((address_space(3))) T*)p; }
template <typename T> __device__ __forceinline__ void ldsw(lds_char* p, T v) { *(__attribute__((address_space(3))) T*)p = v; }
__device__ __forceinline__ f32x16 mfma32(u32x4 a, u32x4 b, f32x16 c) {
  return __builtin_amdgcn_mfma_f32_32x32x16_bf16(__builtin_bit_cast(bf16x8, a), __builtin_bit_cast(bf16x8, b), c, 0, 0, 0);
}
__device__ __forceinline__ u32x4 pack8(const f32x16& a, int s) {
  u32x4 r;
  r[0] = pack2(a[8 * s + 0], a[8 * s + 1]); r[1] = pack2(a[8 * s + 2], a[8 * s + 3]);
  r[2] = pack2(a[8 * s + 4], a[8 * s + 5]); r[3] = pack2(a[8 * s + 6], a[8 * s + 7]);
  return r;
}
__device__ __forceinline__ u32x2 deq4(u32x4 q, f32x4 s, f32x4 z) {
  float w0, w1, w2, w3;
  asm("v_cvt_f32_i32 %0, %4\n\tv_cvt_f32_i32 %1, %5\n\tv_cvt_f32_i32 %2, %6\n\tv_cvt_f32_i32 %3, %7\n\t"
      "v_fma_f32 %0, %0, %8, %12\n\tv_fma_f32 %1, %1, %9, %13\n\tv_fma_f32 %2, %2, %10, %14\n\tv_fma_f32 %3, %3, %11, %15\n\t"
      "v_cvt_pk_bf16_f32 %0, %0, %1\n\tv_cvt_pk_bf16_f32 %1, %2, %3"
      : "=&v"(w0), "=&v"(w1), "=&v"(w2), "=&v"(w3)
      : "v"(q[0]), "v"(q[1]), "v"(q[2]), "v"(q[3]), "v"(s[0]), "v"(s[1]), "v"(s[2]), "v"(s[3]), "v"(z[0]), "v"(z[1]), "v"(z[2]), "v"(z[3]));
  return u32x2{__builtin_bit_cast(unsigned, w0), __builtin_bit_cast(unsigned, w1)};
}
__device__ __forceinline__ void lds_spin(lds_u32* w, unsigned want) {
  while ((unsigned)__builtin_amdgcn_readfirstlane(__hip_atomic_load(w, RLX_WG)) < want) __builtin_amdgcn_s_sleep(1);
  asm volatile("" ::: "memory");
}
__device__ __forceinline__ void poll_flags(gu64* f, u64 tok) {
#pragma unroll 1
  for (unsigned spins = 0; spins < (1u << 20); ++spins) {
    if (__all(__hip_atomic_load(f, RLX_AGENT) == tok)) break;
    __builtin_amdgcn_s_sleep(POLL_SLEEP);
  }
  __builtin_amdgcn_fence(__ATOMIC_ACQUIRE, "wavefront");
}

constexpr int XRF_OFF = 2 << 20, SYNC_OFF = 6 << 20;
#define FS 16
constexpr int FT = 1, FX = 121;
constexpr int FA = 185 * FS;
constexpr unsigned CODE_TOUCH_MAX = 14080;
constexpr int L_SYNC = 156160, L_DUMP = 156416, LDS_BYTES = 156672;

__global__ __launch_bounds__(512, 2) void quip(const __hip_bfloat16* __restrict__ x, const int* __restrict__ qw,
                                               const __hip_bfloat16* __restrict__ scales, const __hip_bfloat16* __restrict__ zeros,
                                               const __hip_bfloat16* __restrict__ U, const __hip_bfloat16* __restrict__ V,
                                               char* ws, __hip_bfloat16* __restrict__ y) {
  extern __shared__ __attribute__((aligned(16))) char smem_[];
  lds_char* smem = (lds_char*)smem_;
  const unsigned lb = (unsigned)(size_t)smem;
  const int wid = __builtin_amdgcn_readfirstlane(threadIdx.x >> 6), lane = threadIdx.x & 63;
  const int xcd = (XCD_PERM >> ((blockIdx.x & 7) * 4)) & 7, idx = blockIdx.x >> 3;
  {
    u64 pc;
    asm volatile("s_mov_b32 m0, %3\n\ts_cmp_gt_u32 %2, 7\n\ts_cbranch_scc1 1f\n\ts_getpc_b64 %0\n\ts_nop 0\n\tglobal_load_lds_dword %1, %0\n1:"
                 : "=&s"(pc) : "v"(min(threadIdx.x * 128, CODE_TOUCH_MAX)), "s"(blockIdx.x), "s"(lb + L_DUMP) : "memory", "scc");
    SB();
  }
  if (KICK && wid < (xcd < 6 ? (idx < 12 ? 4 : 0) : (KICK == 2 ? 5 : 0))) {
    const char* kb;
    unsigned ko, sh, rs, lim = 0xffffffffu;
    if (xcd < 6) {
      const int t = wid * 12 + idx, u = t - 8, p = (u * 57) >> 10, j = u - p * 18;
      kb = t < 4 ? (const char*)scales : t < 8 ? (const char*)zeros : j < 10 ? (const char*)qw : (const char*)V;
      ko = t < 8 ? (xcd & 1) * 30720 + (t & 3) * 8192
                 : j < 10 ? ((xcd & 1) * 320 + j * 32) * (F_ * 4) + p * 256 : (p * 64 + (j - 10) * 8) * (F_ * 2) + (xcd >> 1) * 1024;
      sh = t < 8 ? 6 : j < 10 ? 1 : 3;
      rs = j < 10 ? F_ * 4 : F_ * 2;
      if (t < 8 && (t & 3) == 3) lim = 6016;
    } else {
      int k0 = (idx & 7) + (((idx >> 3) * 5 + 1) >> 1);
      if (k0 >= 10) k0 -= 10;
      kb = wid < 2 ? (const char*)x : (const char*)U;
      ko = wid < 2 ? (((xcd - 6) * 8 + (idx & 7)) * 128 + wid * 64) * (D_ * 2) + k0 * 128
                   : k0 * 64 * (D_ * 2) + (((idx >> 3) * 320) & ~127) + (wid - 2) * 128;
      sh = 0; rs = D_ * 2;
    }
    touch(kb + ko, min((lane >> sh) * rs + ((lane & ((1u << sh) - 1)) << 7), lim));
  }
  const int h = lane >> 5, r = lane & 31;
  const int blk = (lane >> 4) & 1, q4 = (lane & 15) >> 2, p4 = lane & 3;
  lds_u32* sw = (lds_u32*)(smem + L_SYNC);
  gu64* sync = (gu64*)(ws + SYNC_OFF);
  const __amdgpu_buffer_rsrc_t wsr = __builtin_amdgcn_make_buffer_rsrc(ws, 0, SYNC_OFF, 0x00020000);
  const bool is_pf = idx < 12 && xcd < 6;
  if (wid == 0) STAMP(0);

  auto get_token = [&]() {
    if (wid == 7 && lane == 0) {
      const u64 e = __hip_atomic_load(sync, RLX_AGENT) + 0x9E3779B97F4A7C15ull;
      sw[0] = (unsigned)e; sw[1] = (unsigned)(e >> 32);
      __hip_atomic_store(sw + 2, 1u, RLX_WG);
      __hip_atomic_store(sync + FA + blockIdx.x, e, RLX_AGENT);
    }
  };
  if (wid == 3 && lane < 16) sw[lane] = ((lane == 3 || lane == 8) && is_pf) ? 4u : 0u;
  int myflag = 0;

  if (is_pf) {
    if (wid == 0) {
      const unsigned qo = (lane >> 1) * (F_ * 4) + (lane & 1) * 128, vo_ = (lane >> 3) * (F_ * 2) + (lane & 7) * 128;
      const char* qb = (const char*)qw + (size_t)((xcd & 1) * 320) * (F_ * 4);
      const char* vb = (const char*)V + (xcd >> 1) * 1024;
      int p = KICK ? 2 : 0, j = (KICK ? 4 : 0) + idx;
      BAR();
#pragma unroll 1
      while (true) {
        if (j >= 18) { j -= 18; if (++p == 24) break; }
        if (j < 10) touch(qb + j * (32 * F_ * 4) + p * 256, qo);
        else touch(vb + (size_t)(p * 64 + (j - 10) * 8) * (F_ * 2), vo_);
        j += 12;
      }
      STAMP(7);
    } else {
      WAIT_L(0);
      BAR();
      get_token();
    }
  } else if (xcd < 6) {
    const int t = idx - 12;
    const int mt = (xcd & 1) * 5 + t % 5, nt = (xcd >> 1) * 4 + t / 5;
    myflag = FT + nt * 10 + mt;
    if (wid >= 4) {
      const int w4 = wid - 4, rr = lane >> 4, c16 = lane & 15;
      unsigned ag[4], bg[4];
#pragma unroll
      for (int i = 0; i < 4; ++i) {
        int row = i * 4 + rr;
        ag[i] = row * (F_ * 4) + ((c16 ^ row) * 16) - i * 1024;
        bg[i] = row * (F_ * 2) + ((c16 ^ (rr << 2)) * 16) - i * 1024;
      }
      const char* ap = (const char*)qw + (size_t)(mt * 64 + w4 * 16) * (F_ * 4);
      const char* bp = (const char*)V + nt * 256 + (size_t)(w4 * 16) * (F_ * 2);
      unsigned so = lb + w4 * 4096;
      auto issue = [&](int p) {
        unsigned base = so + (p & 3) * 32768;
        set_m0(base);
        glds<0>(ap, ag[0]); glds<1024>(ap, ag[1]); glds<2048>(ap, ag[2]); glds<3072>(ap, ag[3]);
        set_m0(base + 16384);
        glds<0>(bp, bg[0]); glds<1024>(bp, bg[1]); glds<2048>(bp, bg[2]); glds<3072>(bp, bg[3]);
        ap += 256; bp += 64 * F_ * 2;
      };
      issue(0);
      issue(1);
#pragma unroll 1
      for (int p = 2; p < 24; ++p) {
        WAIT_V(8);
        BAR();
        issue(p);
      }
      WAIT_V(8);
      BAR();
      WAIT_V(0);
      BAR();
      get_token();
    } else {
      const int m = wid & 1, kl = wid >> 1;
      lds_char* TB = smem + 131072 + wid * 6144;
      u32x2 sr[3], zr[3];
      {
        const unsigned o = ((mt * 2 + m) * F_ + (lane >> 3) * 64 + kl * 32 + (lane & 7) * 4) * 2;
#pragma unroll
        for (int j = 0; j < 3; ++j) {
          sr[j] = *(const u32x2*)((const char*)scales + o + j * 1024);
          zr[j] = *(const u32x2*)((const char*)zeros + o + j * 1024);
        }
      }
      auto cvt = [&](int j) {
        f32x4 sa, za;
        sa[0] = bflo(sr[j][0]); sa[1] = bfhi(sr[j][0]); sa[2] = bflo(sr[j][1]); sa[3] = bfhi(sr[j][1]);
        za[0] = -bflo(zr[j][0]) * sa[0]; za[1] = -bfhi(zr[j][0]) * sa[1]; za[2] = -bflo(zr[j][1]) * sa[2]; za[3] = -bfhi(zr[j][1]) * sa[3];
        lds_char* d = TB + j * 2048 + (lane >> 3) * 256 + (lane & 7) * 16;
        ldsw<f32x4>(d, sa); ldsw<f32x4>(d + 128, za);
      };
      int aoff[4], boff[4];
#pragma unroll
      for (int c = 0; c < 4; ++c) {
        int ch = 8 * kl + 4 * (c >> 1) + 2 * h + (c & 1);
        aoff[c] = (m * 32 + r) * 256 + ((ch ^ (r & 15)) * 16);
        boff[c] = 16384 + (32 * kl + 8 * h + q4) * 256 + ((c ^ (2 * kl) ^ q4) * 64) + 32 * (p4 & 1) + 16 * blk + 8 * (p4 >> 1);
      }
      lds_char* tp = TB + h * 32;
      f32x16 acc[4] = {};
      f32x4 ts[4], tz[4];
      u32x4 av[4];
      u32x4 af[2][2] = {}, bf[2][2][4] = {};
      auto rdtab = [&](int o, int c) {
        ts[c] = ldsr<f32x4>(tp + o + (c >> 1) * 64 + (c & 1) * 16);
        tz[c] = ldsr<f32x4>(tp + o + 128 + (c >> 1) * 64 + (c & 1) * 16);
      };
      auto step = [&](int slot) {
        const int buf = slot & 1;
        lds_char* sl = smem + slot * 32768;
        BAR();
#pragma unroll
        for (int c = 0; c < 4; ++c) av[c] = ldsr<u32x4>(sl + aoff[c]);
        SB();
#pragma unroll
        for (int u = 0; u < 8; ++u) {
          acc[u & 3] = mfma32(af[buf ^ 1][u >> 2], bf[buf ^ 1][u >> 2][u & 3], acc[u & 3]);
          SB();
          if (u < 4) {
#pragma unroll
            for (int v = 2 * u; v < 2 * u + 2; ++v)
              bf[buf][v >> 2][v & 3] = trread2(sl + boff[v & 3] + (16 * (v >> 2)) * 256, sl + boff[v & 3] + (16 * (v >> 2) + 4) * 256);
          } else {
            const int c = u - 4;
            u32x2 o = deq4(av[c], ts[c], tz[c]);
            af[buf][c >> 1][(c & 1) * 2] = o[0]; af[buf][c >> 1][(c & 1) * 2 + 1] = o[1];
            SB();
            rdtab((slot + 1) * 256, c);
          }
          SB();
        }
      };
#pragma unroll
      for (int c = 0; c < 4; ++c)
        asm volatile("" : "+v"(acc[c]), "+v"(bf[1][0][c]), "+v"(bf[1][1][c]), "+v"(aoff[c]), "+v"(boff[c]));
      asm volatile("" : "+v"(af[1][0]), "+v"(af[1][1]));
      SB();
      cvt(0);
#pragma unroll
      for (int c = 0; c < 4; ++c) rdtab(0, c);
      cvt(1); cvt(2);
      if (wid == 0) STAMP(9);
#pragma unroll 1
      for (int p = 0; p < 24; p += 4) {
        step(0); step(1); step(2); step(3);
        tp += 1024;
      }
      if (wid == 0) STAMP(1);
      lds_char* RED = smem + m * 16384 + lane * 16;
      const int n32 = nt * 4 + kl * 2;
      const unsigned oo0 = (((n32 / 3) * 40 + (mt * 2 + m) * 2) * 3 + n32 % 3) * 1024;
      const unsigned oo1 = ((((n32 + 1) / 3) * 40 + (mt * 2 + m) * 2) * 3 + (n32 + 1) % 3) * 1024;
      SB();
#pragma unroll
      for (int u = 0; u < 8; ++u) {
        const int c = (u < 4 ? 2 : 0) + ((u >> 1) & 1), s2 = u & 1;
        acc[c] = mfma32(af[1][s2], bf[1][s2][c], acc[c]);
        SB();
        if (u >= 3 && u < 7) {
#pragma unroll
          for (int j = 2 * (u - 3); j < 2 * (u - 3) + 2; ++j) {
            const f32x16& g = acc[2 + (j >> 2)];
            const int q = 4 * (j & 3);
            ldsw<f32x4>(RED + kl * 8192 + j * 1024, f32x4{g[q], g[q + 1], g[q + 2], g[q + 3]});
          }
          SB();
        }
      }
      asm volatile("" ::: "memory");
      __hip_atomic_store(sw + 9 + wid, 1u, RLX_WG);
      while (__builtin_amdgcn_readfirstlane(__hip_atomic_load(sw + 9 + (wid ^ 2), RLX_WG)) == 0) {}
      asm volatile("" ::: "memory");
      f32x4 pv[8];
#pragma unroll
      for (int j = 0; j < 8; ++j) pv[j] = ldsr<f32x4>(RED + (kl ^ 1) * 8192 + j * 1024);
      SB();
#pragma unroll
      for (int j = 0; j < 4; ++j) {
        f32x16& o = acc[j >> 1];
#pragma unroll
        for (int e = 0; e < 8; ++e) o[(j & 1) * 8 + e] += pv[2 * j + (e >> 2)][e & 3];
        __builtin_amdgcn_raw_buffer_store_b128(pack8(o, j & 1), wsr, lane * 16 + (j & 1) * 3072, j < 2 ? oo0 : oo1, 16);
      }
    }
  } else {
    const int mi = idx & 7, eb = idx >> 3;
    const int xmb = (xcd - 6) * 8 + mi;
    myflag = FX + xmb * 4 + eb;
    if (wid >= 4) {
      const int w4 = wid - 4;
      unsigned xg[4], ug[5];
#pragma unroll
      for (int i = 0; i < 4; ++i) {
        int row = i * 8 + (lane >> 3);
        xg[i] = row * (D_ * 2) + (((lane & 7) ^ ((row >> 1) & 7)) * 16) - i * 1024;
      }
#pragma unroll
      for (int i = 0; i < 5; ++i) {
        int off = i * 1024 + lane * 16;
        ug[i] = (off / 320) * (D_ * 2) + off % 320 - (i & 3) * 1024;
      }
      const char* xb = (const char*)x + (size_t)(xmb * 128 + w4 * 32) * (D_ * 2);
      const char* ub = (const char*)U + eb * 320 + (size_t)(w4 * 16) * (D_ * 2);
      int ks = mi + ((eb * 5 + 1) >> 1);
      if (ks >= 10) ks -= 10;
      auto issue = [&](int st) {
        unsigned base = lb + (st & 3) * 36864;
        const char* xp = xb + ks * 128;
        const char* up = ub + (size_t)ks * 64 * (D_ * 2);
        set_m0(base + w4 * 4096);
        glds<0>(xp, xg[0]); glds<1024>(xp, xg[1]); glds<2048>(xp, xg[2]); glds<3072>(xp, xg[3]);
        set_m0(base + 16384 + w4 * 5120);
        glds<0>(up, ug[0]); glds<1024>(up, ug[1]); glds<2048>(up, ug[2]); glds<3072>(up, ug[3]);
        set_m0(base + 16384 + w4 * 5120 + 4096);
        glds<0>(up, ug[4]);
        if (++ks == 10) ks = 0;
      };
      issue(0);
#pragma unroll 1
      for (int st = 1; st < 10; ++st) {
        issue(st);
        WAIT_V(9);
        BAR();
      }
      WAIT_V(0);
      BAR();
      get_token();
    } else {
      int xoff[4];
#pragma unroll
      for (int s = 0; s < 4; ++s) xoff[s] = (wid * 32 + r) * 128 + (((2 * s + h) ^ ((r >> 1) & 7)) * 16);
      const int uoff = 16384 + (8 * h + q4) * 320 + blk * 32 + p4 * 8;
      f32x16 acc[5] = {};
      u32x4 xf[2], uf[2][5];
      auto rdu = [&](lds_char* base, int s, int buf, int c) {
        uf[buf][c] = trread2(base + uoff + c * 64 + (16 * s) * 320, base + uoff + c * 64 + (16 * s + 4) * 320);
      };
      WAIT_L(0);
      BAR();
      xf[0] = ldsr<u32x4>(smem + xoff[0]);
#pragma unroll
      for (int c = 0; c < 5; ++c) rdu(smem, 0, 0, c);
#pragma unroll 1
      for (int st = 0; st < 10; ++st) {
#pragma unroll
        for (int s = 0; s < 4; ++s) {
          lds_char* base = smem + ((s < 3 ? st : st + 1) & 3) * 36864;
          const int nx = (s + 1) & 3, nb_ = (s + 1) & 1;
#pragma unroll
          for (int c = 0; c < 5; ++c) {
            acc[c] = mfma32(uf[s & 1][c], xf[s & 1], acc[c]);
            SB();
            if (c == 0) {
              if (s == 3 && st < 9) { BAR(); asm volatile("" ::: "memory"); }
              xf[nb_] = ldsr<u32x4>(base + xoff[nx]);
              rdu(base, nx, nb_, 0);
            } else if (c < 4) {
              rdu(base, nx, nb_, c);
              if (c == 3) rdu(base, nx, nb_, 4);
            }
            SB();
          }
        }
      }
#pragma unroll
      for (int c = 0; c < 5; ++c) {
        const unsigned o = XRF_OFF + ((xmb * 4 + wid) * 40 + (eb * 5 + c) * 2) * 1024;
        __builtin_amdgcn_raw_buffer_store_b128(pack8(acc[c], 0), wsr, lane * 16, o, 16);
        __builtin_amdgcn_raw_buffer_store_b128(pack8(acc[c], 1), wsr, lane * 16 + 1024, o, 16);
      }
      if (wid == 0) STAMP(3);
    }
  }

  const int mb = YPART == 0 ? (xcd >> 1) * 4 + (idx & 3) : YPART == 1 ? (xcd >> 2) * 8 + (idx & 7) : idx & 15;
  const int nb = YPART == 0 ? (xcd & 1) * 8 + (idx >> 2) : YPART == 1 ? (xcd & 3) * 4 + (idx >> 3) : xcd * 2 + (idx >> 4);
  const unsigned vo = lane * 16;
  const char* xsrc = ws + XRF_OFF + (mb * 4 + (wid & 3)) * 40960;
  auto ldx = [&](int ch) {
    asm volatile("s_mov_b32 m0, %2\n\ts_nop 0\n\tglobal_load_lds_dwordx4 %0, %1 sc1\n\tglobal_load_lds_dwordx4 %0, %1 offset:1024 sc1\n\t"
                 "global_load_lds_dwordx4 %0, %1 offset:2048 sc1\n\tglobal_load_lds_dwordx4 %0, %1 offset:3072 sc1"
                 ::"v"(vo), "s"(xsrc + ch * 4096), "s"(lb + (ch % 5) * 28672 + 12288 + (wid & 3) * 4096) : "memory");
  };
  if (wid < 4) {
    if (!is_pf) {
      if (wid == 0) STAMP(10);
      if (lane == 0) __hip_atomic_fetch_add(sw + 8, 1u, RLX_WG);
    }
    lds_spin(sw + 2, 1);
    const u64 tok = (u64)sw[0] | ((u64)sw[1] << 32);
    if (!is_pf) {
      asm volatile("s_waitcnt vmcnt(0) lgkmcnt(0)" ::: "memory");
      if (lane == 0) {
        if (__hip_atomic_fetch_add(sw + 3, 1u, RLX_WG) == 3) __hip_atomic_store(sync + myflag * FS, tok, RLX_AGENT);
      }
      if (wid == 0) STAMP(2);
    }
    if (wid == 0) {
      lds_spin(sw + 3, 4);
      if (TGATE) lds_spin(sw + 4, 1);
      const int n0 = (nb * 3) >> 2, n1 = (nb * 3 + 2) >> 2;
      poll_flags(sync + (FT + n0 * 10 + min(lane, (n1 - n0) * 10 + 9)) * FS, tok);
      if (lane == 0) __hip_atomic_store(sw + 5, 1u, RLX_WG);
      STAMP(4);
    } else {
      lds_spin(sw + 5, 1);
    }
    const char* tsrc = ws + nb * 122880 + wid * 3072;
    unsigned tdst = lb + wid * 3072;
    auto issue = [&]() {
      asm volatile("s_mov_b32 m0, %2\n\ts_nop 0\n\tglobal_load_lds_dwordx4 %0, %1 sc1\n\tglobal_load_lds_dwordx4 %0, %1 offset:1024 sc1\n\t"
                   "global_load_lds_dwordx4 %0, %1 offset:2048 sc1" ::"v"(vo), "s"(tsrc), "s"(tdst) : "memory");
      tsrc += 12288; tdst += 12288;
    };
    issue(); issue(); issue(); issue();
#pragma unroll 1
    for (int ch = 0; ch < 6; ++ch) {
      WAIT_V(9);
      BAR();
      issue();
    }
    WAIT_V(9); BAR(); WAIT_V(6); BAR(); WAIT_V(3); BAR(); WAIT_V(0); BAR();
    if (blockIdx.x == 1 && wid == 2) {
#pragma unroll 1
      for (unsigned spins = 0; spins < (1u << 20); ++spins) {
        bool ok = true;
#pragma unroll
        for (int i = 0; i < 4; ++i) ok &= __hip_atomic_load(sync + FA + i * 64 + lane, RLX_AGENT) == tok;
        if (__all(ok)) break;
        __builtin_amdgcn_s_sleep(POLL_SLEEP);
      }
      if (lane == 0) __hip_atomic_store(sync, tok, RLX_AGENT);
    }
  } else {
    const int w4 = wid - 4;
    if (wid == 4) {
      lds_spin(sw + 2, 1);
      const u64 tok = (u64)sw[0] | ((u64)sw[1] << 32);
      poll_flags(sync + (FX + mb * 4 + (lane & 3)) * FS, tok);
      lds_spin(sw + 8, 4);
      if (lane == 0) __hip_atomic_store(sw + 4, 1u, RLX_WG);
      STAMP(8);
    } else {
      lds_spin(sw + 4, 1);
    }
    u32x4 xf[40];
    const unsigned xo = XRF_OFF + (mb * 4 + w4) * 40960;
#pragma unroll
    for (int ch = 0; ch < 10; ++ch) {
#pragma unroll
      for (int i = 0; i < 4; ++i) xf[ch * 4 + i] = __builtin_amdgcn_raw_buffer_load_b128(wsr, lane * 16 + i * 1024, xo + ch * 4096, 16);
    }
    SB();
    if (wid == 4) STAMP(11);
    f32x16 acc[3] = {};
    u32x4 tf[3][3];
    lds_char* tb = smem + lane * 16;
    BAR();
#pragma unroll
    for (int c = 0; c < 6; ++c) tf[c / 3][c % 3] = ldsr<u32x4>(tb + c * 1024);
#pragma unroll
    for (int ks = 0; ks < 40; ++ks) {
#pragma unroll
      for (int c = 0; c < 3; ++c) {
        acc[c] = mfma32(tf[ks % 3][c], xf[ks], acc[c]);
        SB();
        if (ks < 38) {
          if (c == 0 && (ks & 3) == 2) { BAR(); asm volatile("" ::: "memory"); }
          tf[(ks + 2) % 3][c] = ldsr<u32x4>(tb + ((ks + 2) * 3 + c) * 1024);
        }
        SB();
      }
    }
    if (wid == 4) STAMP(5);
    lds_char* eb = smem + (YV ? 0 : 122880) + w4 * 6656;
#pragma unroll
    for (int c = 0; c < 3; ++c) {
      ldsw<u32x4>(eb + r * 208 + c * 64 + h * 32, pack8(acc[c], 0));
      ldsw<u32x4>(eb + r * 208 + c * 64 + h * 32 + 16, pack8(acc[c], 1));
    }
    const __amdgpu_buffer_rsrc_t yr = __builtin_amdgcn_make_buffer_rsrc((void*)y, 0, 2048 * F_ * 2, 0x00020000);
    const unsigned yo = ((mb * 128 + w4 * 32) * F_ + nb * 96) * 2;
#pragma unroll
    for (int i = 0; i < 6; ++i) {
      int q = i * 64 + lane, row = q / 12, chn = q % 12;
      __builtin_amdgcn_raw_buffer_store_b128(ldsr<u32x4>(eb + row * 208 + chn * 16), yr, row * (F_ * 2) + chn * 16, yo, Y_AUX);
    }
  }
}

extern "C" void kernel_launch(void* const* d_in, const int* in_sizes, int n_in, void* d_out, int out_size, void* d_ws,
                              size_t ws_size, hipStream_t stream) {
  (void)hipFuncSetAttribute((const void*)quip, hipFuncAttributeMaxDynamicSharedMemorySize, LDS_BYTES);
  quip<<<256, 512, LDS_BYTES, stream>>>((const __hip_bfloat16*)d_in[0], (const int*)d_in[1], (const __hip_bfloat16*)d_in[2],
                                        (const __hip_bfloat16*)d_in[3], (const __hip_bfloat16*)d_in[4], (const __hip_bfloat16*)d_in[5],
                                        (char*)d_ws, (__hip_bfloat16*)d_out);
}
```

```cpp
#include <hip/hip_runtime.h>
#include <hip/hip_bf16.h>
#include <utility>

typedef __attribute__((ext_vector_type(8))) __bf16 bf16x8;
typedef __attribute__((ext_vector_type(2))) __bf16 bf16x2;
typedef __attribute__((ext_vector_type(4))) short s16x4;
typedef __attribute__((ext_vector_type(16))) float f32x16;
typedef __attribute__((ext_vector_type(4))) float f32x4;
typedef __attribute__((ext_vector_type(4))) unsigned u32x4;
typedef __attribute__((ext_vector_type(2))) unsigned u32x2;
typedef __attribute__((address_space(3))) s16x4 lds_s16x4;
typedef __attribute__((address_space(3))) char lds_char;
typedef __attribute__((address_space(3))) unsigned lds_u32;
typedef __attribute__((address_space(1))) unsigned long long gu64;
typedef unsigned long long u64;

#define STAMP(i)
#define Y_AUX 2
#define XCD_PERM 0x54321706
#define YPART 0
#define XRV 0
#define XP 1
#define POLL_SLEEP 4
constexpr int D_ = 640, F_ = 1536;

#define WAIT_V(n) asm volatile("s_waitcnt vmcnt(" #n ")" ::: "memory")
#define WAIT_L(n) asm volatile("s_waitcnt lgkmcnt(" #n ")" ::: "memory")
#define BAR() __builtin_amdgcn_s_barrier()
#define SB() __builtin_amdgcn_sched_barrier(0)
#define RLX_AGENT __ATOMIC_RELAXED, __HIP_MEMORY_SCOPE_AGENT
#define RLX_WG __ATOMIC_RELAXED, __HIP_MEMORY_SCOPE_WORKGROUP

__device__ __forceinline__ void set_m0(unsigned ldsaddr) { asm volatile("s_mov_b32 m0, %0\n\ts_nop 0" ::"s"(ldsaddr) : "memory"); }
template <int OFF> __device__ __forceinline__ void glds(const void* sbase, unsigned voff) {
  asm volatile("global_load_lds_dwordx4 %0, %1 offset:%2" ::"v"(voff), "s"(sbase), "i"(OFF) : "memory");
}
__device__ __forceinline__ void touch(const void* sbase, unsigned voff) {
  asm volatile("global_load_lds_dword %0, %1" ::"v"(voff), "s"(sbase) : "memory");
}
__device__ __forceinline__ unsigned pack2(float a, float b) {
  bf16x2 p; p[0] = (__bf16)a; p[1] = (__bf16)b;
  return __builtin_bit_cast(unsigned, p);
}
__device__ __forceinline__ float bflo(unsigned u) { return __builtin_bit_cast(float, u << 16); }
__device__ __forceinline__ float bfhi(unsigned u) { return __builtin_bit_cast(float, u & 0xffff0000u); }
__device__ __forceinline__ u32x4 trread2(lds_char* p0, lds_char* p1) {
  u32x2 lo = __builtin_bit_cast(u32x2, __builtin_amdgcn_ds_read_tr16_b64_v4i16((lds_s16x4*)p0));
  u32x2 hi = __builtin_bit_cast(u32x2, __builtin_amdgcn_ds_read_tr16_b64_v4i16((lds_s16x4*)p1));
  return u32x4{lo[0], lo[1], hi[0], hi[1]};
}
template <typename T> __device__ __forceinline__ T ldsr(lds_char* p) { return *(__attribute__((address_space(3))) T*)p; }
template <typename T> __device__ __forceinline__ void ldsw(lds_char* p, T v) { *(__attribute__((address_space(3))) T*)p = v; }
__device__ __forceinline__ f32x16 mfma32(u32x4 a, u32x4 b, f32x16 c) {
  return __builtin_amdgcn_mfma_f32_32x32x16_bf16(__builtin_bit_cast(bf16x8, a), __builtin_bit_cast(bf16x8, b), c, 0, 0, 0);
}
__device__ __forceinline__ u32x4 pack8(const f32x16& a, int s) {
  u32x4 r;
  r[0] = pack2(a[8 * s + 0], a[8 * s + 1]); r[1] = pack2(a[8 * s + 2], a[8 * s + 3]);
  r[2] = pack2(a[8 * s + 4], a[8 * s + 5]); r[3] = pack2(a[8 * s + 6], a[8 * s + 7]);
  return r;
}
__device__ __forceinline__ u32x2 deq4(u32x4 q, f32x4 s, f32x4 z) {
  float w0, w1, w2, w3;
  asm("v_cvt_f32_i32 %0, %4\n\tv_cvt_f32_i32 %1, %5\n\tv_cvt_f32_i32 %2, %6\n\tv_cvt_f32_i32 %3, %7\n\t"
      "v_fma_f32 %0, %0, %8, %12\n\tv_fma_f32 %1, %1, %9, %13\n\tv_fma_f32 %2, %2, %10, %14\n\tv_fma_f32 %3, %3, %11, %15\n\t"
      "v_cvt_pk_bf16_f32 %0, %0, %1\n\tv_cvt_pk_bf16_f32 %1, %2, %3"
      : "=&v"(w0), "=&v"(w1), "=&v"(w2), "=&v"(w3)
      : "v"(q[0]), "v"(q[1]), "v"(q[2]), "v"(q[3]), "v"(s[0]), "v"(s[1]), "v"(s[2]), "v"(s[3]), "v"(z[0]), "v"(z[1]), "v"(z[2]), "v"(z[3]));
  return u32x2{__builtin_bit_cast(unsigned, w0), __builtin_bit_cast(unsigned, w1)};
}
__device__ __forceinline__ void lds_spin(lds_u32* w, unsigned want) {
  while ((unsigned)__builtin_amdgcn_readfirstlane(__hip_atomic_load(w, RLX_WG)) < want) __builtin_amdgcn_s_sleep(1);
  asm volatile("" ::: "memory");
}
__device__ __forceinline__ void poll_flags(gu64* f, u64 tok) {
#pragma unroll 1
  for (unsigned spins = 0; spins < (1u << 20); ++spins) {
    if (__all(__hip_atomic_load(f, RLX_AGENT) == tok)) break;
    __builtin_amdgcn_s_sleep(POLL_SLEEP);
  }
  __builtin_amdgcn_fence(__ATOMIC_ACQUIRE, "wavefront");
}

template <int N, typename Fn, int... I>
__device__ __forceinline__ void static_for_impl(Fn&& f, std::integer_sequence<int, I...>) { (f(std::integral_constant<int, I>{}), ...); }
template <int N, typename Fn>
__device__ __forceinline__ void static_for(Fn&& f) { static_for_impl<N>(f, std::make_integer_sequence<int, N>{}); }
constexpr int x_ks(int i) { return i < 160 ? (i % 80) / 2 : i - 160; }
constexpr int x_stage(int i) { return i < 80 ? i / 8 : i < 160 ? 10 + (i - 80) / 16 : 15 + (i - 160) / 20; }
constexpr int x_uoff(int i) { return i < 80 ? 16384 + (x_ks(i) & 3) * 2048 : i < 160 ? (x_ks(i) & 7) * 2048 : (x_ks(i) % 20) * 1024; }
constexpr int x_nld(int st) { return st < 10 ? 6 : st < 15 ? 4 : st < 17 ? 5 : 0; }

constexpr int XRF_OFF = 2 << 20, SYNC_OFF = 6 << 20;
#define FS 16
constexpr int FT = 1, FX = 121;
constexpr int FA = 185 * FS;
constexpr unsigned CODE_TOUCH_MAX = XP ? 21888 : 14080;
constexpr int L_SYNC = 156160, L_DUMP = 156416, LDS_BYTES = 156672;

__global__ __launch_bounds__(512, 2) void quip(const __hip_bfloat16* __restrict__ x, const int* __restrict__ qw,
                                               const __hip_bfloat16* __restrict__ scales, const __hip_bfloat16* __restrict__ zeros,
                                               const __hip_bfloat16* __restrict__ U, const __hip_bfloat16* __restrict__ V,
                                               char* ws, __hip_bfloat16* __restrict__ y) {
  extern __shared__ __attribute__((aligned(16))) char smem_[];
  lds_char* smem = (lds_char*)smem_;
  const unsigned lb = (unsigned)(size_t)smem;
  const int wid = __builtin_amdgcn_readfirstlane(threadIdx.x >> 6), lane = threadIdx.x & 63;
  const int xcd = (XCD_PERM >> ((blockIdx.x & 7) * 4)) & 7, idx = blockIdx.x >> 3;
  {
    u64 pc;
    asm volatile("s_mov_b32 m0, %3\n\ts_cmp_gt_u32 %2, 7\n\ts_cbranch_scc1 1f\n\ts_getpc_b64 %0\n\ts_nop 0\n\tglobal_load_lds_dword %1, %0\n1:"
                 : "=&s"(pc) : "v"(min(threadIdx.x * 128, CODE_TOUCH_MAX)), "s"(blockIdx.x), "s"(lb + L_DUMP) : "memory", "scc");
    SB();
  }
  const int h = lane >> 5, r = lane & 31;
  const int blk = (lane >> 4) & 1, q4 = (lane & 15) >> 2, p4 = lane & 3;
  lds_u32* sw = (lds_u32*)(smem + L_SYNC);
  gu64* sync = (gu64*)(ws + SYNC_OFF);
  const __amdgpu_buffer_rsrc_t wsr = __builtin_amdgcn_make_buffer_rsrc(ws, 0, SYNC_OFF, 0x00020000);
  const bool is_pf = idx < 12 && xcd < 6;
  if (wid == 0) STAMP(0);

  auto get_token = [&]() {
    if (wid == 7 && lane == 0) {
      const u64 e = __hip_atomic_load(sync, RLX_AGENT) + 0x9E3779B97F4A7C15ull;
      sw[0] = (unsigned)e; sw[1] = (unsigned)(e >> 32);
      __hip_atomic_store(sw + 2, 1u, RLX_WG);
      __hip_atomic_store(sync + FA + blockIdx.x, e, RLX_AGENT);
    }
  };
  if (wid == 3 && lane < 16) sw[lane] = ((lane == 3 || lane == 8) && is_pf) ? 4u : 0u;
  int myflag = 0;

  if (is_pf) {
    if (wid < 2) {
      const int ga = xcd & 1, gb = xcd >> 1;
      const bool isq = idx < 3 || (idx >= 7 && idx < 10), far = idx < 7;
      const unsigned vo_ = isq ? (lane >> 1) * (F_ * 4) + (lane & 1) * 128 : (lane >> 3) * (F_ * 2) + (lane & 7) * 128;
      const char* gbase = isq ? (const char*)qw + (size_t)(ga * 320) * (F_ * 4) : (const char*)V + gb * 1024;
      const int nc = isq ? 3 : (far ? 4 : 2), c0 = idx - (idx < 3 ? 0 : idx < 7 ? 3 : idx < 10 ? 7 : 10);
      const int ne = isq ? (far ? 80 : 160) : 96;
      BAR();
#pragma unroll 1
      for (int e = c0 + nc * wid; e < ne; e += nc * 2) {
        if (isq) {
          int t = far ? e * 3 + gb : (e >> 1) * 3 + (gb + 1 + (e & 1)) % 3;
          const int pp = t / 10, j = t - pp * 10;
          touch(gbase + j * (32 * F_ * 4) + pp * 256, vo_);
        } else {
          touch(gbase + (size_t)(e * 2 + (far ? ga : ga ^ 1)) * (8 * F_ * 2), vo_);
        }
      }
      if (wid == 0) STAMP(7);
    } else {
      WAIT_L(0);
      BAR();
      get_token();
    }
  } else if (xcd < 6) {
    const int t = idx - 12;
    const int mt = (xcd & 1) * 5 + t % 5, nt = (xcd >> 1) * 4 + t / 5;
    myflag = FT + nt * 10 + mt;
    if (wid >= 4) {
      const int w4 = wid - 4, rr = lane >> 4, c16 = lane & 15;
      unsigned ag[4], bg[4];
#pragma unroll
      for (int i = 0; i < 4; ++i) {
        int row = i * 4 + rr;
        ag[i] = row * (F_ * 4) + ((c16 ^ row) * 16) - i * 1024;
        bg[i] = row * (F_ * 2) + ((c16 ^ (rr << 2)) * 16) - i * 1024;
      }
      const char* ap = (const char*)qw + (size_t)(mt * 64 + w4 * 16) * (F_ * 4);
      const char* bp = (const char*)V + nt * 256 + (size_t)(w4 * 16) * (F_ * 2);
      unsigned so = lb + w4 * 4096;
      auto issue = [&](int p) {
        unsigned base = so + (p & 3) * 32768;
        set_m0(base);
        glds<0>(ap, ag[0]); glds<1024>(ap, ag[1]); glds<2048>(ap, ag[2]); glds<3072>(ap, ag[3]);
        set_m0(base + 16384);
        glds<0>(bp, bg[0]); glds<1024>(bp, bg[1]); glds<2048>(bp, bg[2]); glds<3072>(bp, bg[3]);
        ap += 256; bp += 64 * F_ * 2;
      };
      issue(0);
      issue(1);
#pragma unroll 1
      for (int p = 2; p < 24; ++p) {
        WAIT_V(8);
        BAR();
        issue(p);
      }
      WAIT_V(8);
      BAR();
      WAIT_V(0);
      BAR();
      get_token();
    } else {
      const int m = wid & 1, kl = wid >> 1;
      lds_char* TB = smem + 131072 + wid * 6144;
      u32x2 sr[3], zr[3];
      {
        const unsigned o = ((mt * 2 + m) * F_ + (lane >> 3) * 64 + kl * 32 + (lane & 7) * 4) * 2;
#pragma unroll
        for (int j = 0; j < 3; ++j) {
          sr[j] = *(const u32x2*)((const char*)scales + o + j * 1024);
          zr[j] = *(const u32x2*)((const char*)zeros + o + j * 1024);
        }
      }
      auto cvt = [&](int j) {
        f32x4 sa, za;
        sa[0] = bflo(sr[j][0]); sa[1] = bfhi(sr[j][0]); sa[2] = bflo(sr[j][1]); sa[3] = bfhi(sr[j][1]);
        za[0] = -bflo(zr[j][0]) * sa[0]; za[1] = -bfhi(zr[j][0]) * sa[1]; za[2] = -bflo(zr[j][1]) * sa[2]; za[3] = -bfhi(zr[j][1]) * sa[3];
        lds_char* d = TB + j * 2048 + (lane >> 3) * 256 + (lane & 7) * 16;
        ldsw<f32x4>(d, sa); ldsw<f32x4>(d + 128, za);
      };
      int aoff[4], boff[4];
#pragma unroll
      for (int c = 0; c < 4; ++c) {
        int ch = 8 * kl + 4 * (c >> 1) + 2 * h + (c & 1);
        aoff[c] = (m * 32 + r) * 256 + ((ch ^ (r & 15)) * 16);
        boff[c] = 16384 + (32 * kl + 8 * h + q4) * 256 + ((c ^ (2 * kl) ^ q4) * 64) + 32 * (p4 & 1) + 16 * blk + 8 * (p4 >> 1);
      }
      lds_char* tp = TB + h * 32;
      f32x16 acc[4] = {};
      f32x4 ts[4], tz[4];
      u32x4 av[4];
      u32x4 af[2][2] = {}, bf[2][2][4] = {};
      auto rdtab = [&](int o, int c) {
        ts[c] = ldsr<f32x4>(tp + o + (c >> 1) * 64 + (c & 1) * 16);
        tz[c] = ldsr<f32x4>(tp + o + 128 + (c >> 1) * 64 + (c & 1) * 16);
      };
      auto step = [&](int slot) {
        const int buf = slot & 1;
        lds_char* sl = smem + slot * 32768;
        BAR();
#pragma unroll
        for (int c = 0; c < 4; ++c) av[c] = ldsr<u32x4>(sl + aoff[c]);
        SB();
#pragma unroll
        for (int u = 0; u < 8; ++u) {
          acc[u & 3] = mfma32(af[buf ^ 1][u >> 2], bf[buf ^ 1][u >> 2][u & 3], acc[u & 3]);
          SB();
          if (u < 4) {
#pragma unroll
            for (int v = 2 * u; v < 2 * u + 2; ++v)
              bf[buf][v >> 2][v & 3] = trread2(sl + boff[v & 3] + (16 * (v >> 2)) * 256, sl + boff[v & 3] + (16 * (v >> 2) + 4) * 256);
          } else {
            const int c = u - 4;
            u32x2 o = deq4(av[c], ts[c], tz[c]);
            af[buf][c >> 1][(c & 1) * 2] = o[0]; af[buf][c >> 1][(c & 1) * 2 + 1] = o[1];
            SB();
            rdtab((slot + 1) * 256, c);
          }
          SB();
        }
      };
#pragma unroll
      for (int c = 0; c < 4; ++c)
        asm volatile("" : "+v"(acc[c]), "+v"(bf[1][0][c]), "+v"(bf[1][1][c]), "+v"(aoff[c]), "+v"(boff[c]));
      asm volatile("" : "+v"(af[1][0]), "+v"(af[1][1]));
      SB();
      cvt(0);
#pragma unroll
      for (int c = 0; c < 4; ++c) rdtab(0, c);
      cvt(1); cvt(2);
      if (wid == 0) STAMP(9);
#pragma unroll 1
      for (int p = 0; p < 24; p += 4) {
        step(0); step(1); step(2); step(3);
        tp += 1024;
      }
      if (wid == 0) STAMP(1);
      lds_char* RED = smem + m * 16384 + lane * 16;
      const int n32 = nt * 4 + kl * 2;
      const unsigned oo0 = (((n32 / 3) * 40 + (mt * 2 + m) * 2) * 3 + n32 % 3) * 1024;
      const unsigned oo1 = ((((n32 + 1) / 3) * 40 + (mt * 2 + m) * 2) * 3 + (n32 + 1) % 3) * 1024;
      SB();
#pragma unroll
      for (int u = 0; u < 8; ++u) {
        const int c = (u < 4 ? 2 : 0) + ((u >> 1) & 1), s2 = u & 1;
        acc[c] = mfma32(af[1][s2], bf[1][s2][c], acc[c]);
        SB();
        if (u >= 3 && u < 7) {
#pragma unroll
          for (int j = 2 * (u - 3); j < 2 * (u - 3) + 2; ++j) {
            const f32x16& g = acc[2 + (j >> 2)];
            const int q = 4 * (j & 3);
            ldsw<f32x4>(RED + kl * 8192 + j * 1024, f32x4{g[q], g[q + 1], g[q + 2], g[q + 3]});
          }
          SB();
        }
      }
      asm volatile("" ::: "memory");
      __hip_atomic_store(sw + 9 + wid, 1u, RLX_WG);
      while (__builtin_amdgcn_readfirstlane(__hip_atomic_load(sw + 9 + (wid ^ 2), RLX_WG)) == 0) {}
      asm volatile("" ::: "memory");
      f32x4 pv[8];
#pragma unroll
      for (int j = 0; j < 8; ++j) pv[j] = ldsr<f32x4>(RED + (kl ^ 1) * 8192 + j * 1024);
      SB();
#pragma unroll
      for (int j = 0; j < 4; ++j) {
        f32x16& o = acc[j >> 1];
#pragma unroll
        for (int e = 0; e < 8; ++e) o[(j & 1) * 8 + e] += pv[2 * j + (e >> 2)][e & 3];
        __builtin_amdgcn_raw_buffer_store_b128(pack8(o, j & 1), wsr, lane * 16 + (j & 1) * 3072, j < 2 ? oo0 : oo1, 16);
      }
    }
  } else {
    const int mi = idx & 7, eb = idx >> 3;
    const int xmb = (xcd - 6) * 8 + mi;
    const int ea = eb & 1;
    myflag = FX + xmb * 4 + eb;
    if (wid >= 4) {
      const int w4 = wid - 4;
      unsigned xg[4], ug[2];
#pragma unroll
      for (int i = 0; i < 4; ++i) {
        int row = i * 8 + (lane >> 3);
        xg[i] = row * (D_ * 2) + (((lane & 7) ^ ((row >> 1) & 7)) * 16) - i * 1024;
      }
#pragma unroll
      for (int i = 0; i < 2; ++i) ug[i] = (i * 8 + (lane >> 3)) * (D_ * 2) + (((lane & 7) ^ (((lane >> 4) & 1) << 2)) * 16) - i * 1024;
      const unsigned cg = (lane >> 2) * (D_ * 2) + (lane & 3) * 16;
      const char* xb = (const char*)x + (size_t)(xmb * 128 + w4 * 32) * (D_ * 2);
      const char* ub = (const char*)U + eb * 320 + ea * 64 + (size_t)(w4 * 16) * (D_ * 2);
      int ks = mi + ((eb * 5 + 1) >> 1);
      if (ks >= 10) ks -= 10;
      auto issue = [&](auto sc) {
        constexpr int st = decltype(sc)::value;
        const unsigned base = lb + (st % 5) * 24576;
        if constexpr (st < 10) {
          const char* xp = xb + ks * 128;
          const char* up = ub + (size_t)ks * 64 * (D_ * 2);
          set_m0(base + w4 * 4096);
          glds<0>(xp, xg[0]); glds<1024>(xp, xg[1]); glds<2048>(xp, xg[2]); glds<3072>(xp, xg[3]);
          set_m0(base + 16384 + w4 * 2048);
          glds<0>(up, ug[0]); glds<1024>(up, ug[1]);
          if (++ks == 10) ks = 0;
        } else if constexpr (st < 15) {
#pragma unroll
          for (int j = 0; j < 2; ++j) {
            const char* up = ub + 128 + (size_t)ks * 64 * (D_ * 2);
            set_m0(base + j * 8192 + w4 * 2048);
            glds<0>(up, ug[0]); glds<1024>(up, ug[1]);
            if (++ks == 10) ks = 0;
          }
        } else if constexpr (st < 17) {
#pragma unroll
          for (int j = 0; j < 5; ++j) {
            set_m0(base + j * 4096 + w4 * 1024);
            glds<0>(ub + (ea ? -64 : 256) + (size_t)ks * 64 * (D_ * 2), cg);
            if (++ks == 10) ks = 0;
          }
        }
      };
      issue(std::integral_constant<int, 0>{});
      issue(std::integral_constant<int, 1>{});
      static_for<17>([&](auto sc) {
        constexpr int st = decltype(sc)::value;
        asm volatile("s_waitcnt vmcnt(%0)" ::"i"(x_nld(st + 1)) : "memory");
        BAR();
        issue(std::integral_constant<int, st + 2>{});
      });
      get_token();
    } else {
      int xoff[4], uoff[2];
#pragma unroll
      for (int s = 0; s < 4; ++s) xoff[s] = (wid * 32 + r) * 128 + (((2 * s + h) ^ ((r >> 1) & 7)) * 16);
#pragma unroll
      for (int t = 0; t < 2; ++t) uoff[t] = (8 * h + q4) * 128 + ((t ^ ((q4 >> 1) & 1)) * 64) + blk * 32 + p4 * 8;
      const int coff = (8 * h + q4) * 64 + blk * 32 + p4 * 8;
      const unsigned xo = XRF_OFF + ((xmb * 4 + wid) * 40 + eb * 10) * 1024;
      f32x16 acc[2] = {};
      u32x4 xf[40], uf[8];
      auto rd = [&](auto ic) {
        constexpr int i = decltype(ic)::value;
        if constexpr (i < 200) {
          if constexpr (i == 0 || x_stage(i) != x_stage(i - 1)) { BAR(); asm volatile("" ::: "memory"); }
          lds_char* base = smem + (x_stage(i) % 5) * 24576;
          if constexpr (i < 80 && (i & 1) == 0) xf[i / 2] = ldsr<u32x4>(base + xoff[(i / 2) & 3]);
          if constexpr (i < 160) uf[i & 7] = trread2(base + x_uoff(i) + uoff[i & 1], base + x_uoff(i) + uoff[i & 1] + 512);
          else uf[i & 7] = trread2(base + x_uoff(i) + coff, base + x_uoff(i) + coff + 256);
        }
      };
      u32x4 pk[4];
      WAIT_L(0);
      static_for<6>(rd);
      static_for<200>([&](auto ic) {
        constexpr int i = decltype(ic)::value;
        constexpr int a = i < 160 ? (i & 1) : 0;
        if constexpr (i == 80 || i == 160) {
#pragma unroll
          for (int j = 0; j < 4; ++j) pk[j] = pack8(acc[j >> 1], j & 1);
          acc[0] = f32x16{}; acc[1] = f32x16{};
          SB();
        }
        acc[a] = mfma32(uf[i & 7], xf[x_ks(i)], acc[a]);
        SB();
        rd(std::integral_constant<int, i + 6>{});
        if constexpr (i >= 80 && i % 80 < 8 && (i & 1) == 0)
          __builtin_amdgcn_raw_buffer_store_b128(pk[(i % 80) / 2], wsr, lane * 16 + ((i % 80) / 2) * 1024, xo + ea * 2048 + (i / 160) * 4096, 16);
        SB();
      });
      __builtin_amdgcn_raw_buffer_store_b128(pack8(acc[0], 0), wsr, lane * 16, xo + (ea ? 0 : 8192), 16);
      __builtin_amdgcn_raw_buffer_store_b128(pack8(acc[0], 1), wsr, lane * 16 + 1024, xo + (ea ? 0 : 8192), 16);
      if (wid == 0) STAMP(3);
    }
  }

  const int mb = YPART == 0 ? (xcd >> 1) * 4 + (idx & 3) : YPART == 1 ? (xcd >> 2) * 8 + (idx & 7) : idx & 15;
  const int nb = YPART == 0 ? (xcd & 1) * 8 + (idx >> 2) : YPART == 1 ? (xcd & 3) * 4 + (idx >> 3) : xcd * 2 + (idx >> 4);
  const unsigned vo = lane * 16;
  if (wid < 4) {
    if (!is_pf) {
      if (wid == 0) STAMP(10);
      if (lane == 0) __hip_atomic_fetch_add(sw + 8, 1u, RLX_WG);
    }
    lds_spin(sw + 2, 1);
    const u64 tok = (u64)sw[0] | ((u64)sw[1] << 32);
    if (!is_pf) {
      asm volatile("s_waitcnt vmcnt(0) lgkmcnt(0)" ::: "memory");
      if (lane == 0) {
        if (__hip_atomic_fetch_add(sw + 3, 1u, RLX_WG) == 3) __hip_atomic_store(sync + myflag * FS, tok, RLX_AGENT);
      }
      if (wid == 0) STAMP(2);
    }
    if (wid == 0) {
      lds_spin(sw + 3, 4);
      const int n0 = (nb * 3) >> 2, n1 = (nb * 3 + 2) >> 2;
      poll_flags(sync + (FT + n0 * 10 + min(lane, (n1 - n0) * 10 + 9)) * FS, tok);
      if (lane == 0) __hip_atomic_store(sw + 5, 1u, RLX_WG);
      STAMP(4);
    } else {
      lds_spin(sw + 5, 1);
    }
    const char* tsrc = ws + nb * 122880 + wid * 3072;
    unsigned tdst = lb + wid * 3072;
    auto issue = [&]() {
      asm volatile("s_mov_b32 m0, %2\n\ts_nop 0\n\tglobal_load_lds_dwordx4 %0, %1 sc1\n\tglobal_load_lds_dwordx4 %0, %1 offset:1024 sc1\n\t"
                   "global_load_lds_dwordx4 %0, %1 offset:2048 sc1" ::"v"(vo), "s"(tsrc), "s"(tdst) : "memory");
      tsrc += 12288; tdst += 12288;
    };
    issue(); issue(); issue(); issue();
#pragma unroll 1
    for (int ch = 0; ch < 6; ++ch) {
      WAIT_V(9);
      BAR();
      issue();
    }
    WAIT_V(9); BAR(); WAIT_V(6); BAR(); WAIT_V(3); BAR(); WAIT_V(0); BAR();
    if (blockIdx.x == 1 && wid == 2) {
#pragma unroll 1
      for (unsigned spins = 0; spins < (1u << 20); ++spins) {
        bool ok = true;
#pragma unroll
        for (int i = 0; i < 4; ++i) ok &= __hip_atomic_load(sync + FA + i * 64 + lane, RLX_AGENT) == tok;
        if (__all(ok)) break;
        __builtin_amdgcn_s_sleep(POLL_SLEEP);
      }
      if (lane == 0) __hip_atomic_store(sync, tok, RLX_AGENT);
    }
  } else {
    const int w4 = wid - 4;
    if (wid == 4) {
      lds_spin(sw + 2, 1);
      const u64 tok = (u64)sw[0] | ((u64)sw[1] << 32);
      poll_flags(sync + (FX + mb * 4 + (lane & 3)) * FS, tok);
      lds_spin(sw + 8, 4);
      if (lane == 0) __hip_atomic_store(sw + 4, 1u, RLX_WG);
      STAMP(8);
    } else {
      lds_spin(sw + 4, 1);
    }
    u32x4 xf[40];
    const unsigned xo = XRF_OFF + (mb * 4 + w4) * 40960;
#pragma unroll
    for (int ch = 0; ch < 10; ++ch) {
#pragma unroll
      for (int i = 0; i < 4; ++i) xf[ch * 4 + i] = __builtin_amdgcn_raw_buffer_load_b128(wsr, lane * 16 + i * 1024, xo + ch * 4096, 16);
    }
    SB();
    if (wid == 4) STAMP(11);
    f32x16 acc[3] = {};
    u32x4 tf[3][3];
    lds_char* tb = smem + lane * 16;
    BAR();
#pragma unroll
    for (int c = 0; c < 6; ++c) tf[c / 3][c % 3] = ldsr<u32x4>(tb + c * 1024);
#pragma unroll
    for (int ks = 0; ks < 40; ++ks) {
#pragma unroll
      for (int c = 0; c < 3; ++c) {
        acc[c] = mfma32(tf[ks % 3][c], xf[ks], acc[c]);
        SB();
        if (ks < 38) {
          if (c == 0 && (ks & 3) == 2) { BAR(); asm volatile("" ::: "memory"); }
          tf[(ks + 2) % 3][c] = ldsr<u32x4>(tb + ((ks + 2) * 3 + c) * 1024);
        }
        SB();
      }
    }
    if (wid == 4) STAMP(5);
    lds_char* eb = smem + 122880 + w4 * 6656;
#pragma unroll
    for (int c = 0; c < 3; ++c) {
      ldsw<u32x4>(eb + r * 208 + c * 64 + h * 32, pack8(acc[c], 0));
      ldsw<u32x4>(eb + r * 208 + c * 64 + h * 32 + 16, pack8(acc[c], 1));
    }
    const __amdgpu_buffer_rsrc_t yr = __builtin_amdgcn_make_buffer_rsrc((void*)y, 0, 2048 * F_ * 2, 0x00020000);
    const unsigned yo = ((mb * 128 + w4 * 32) * F_ + nb * 96) * 2;
#pragma unroll
    for (int i = 0; i < 6; ++i) {
      int q = i * 64 + lane, row = q / 12, chn = q % 12;
      __builtin_amdgcn_raw_buffer_store_b128(ldsr<u32x4>(eb + row * 208 + chn * 16), yr, row * (F_ * 2) + chn * 16, yo, Y_AUX);
    }
  }
}

extern "C" void kernel_launch(void* const* d_in, const int* in_sizes, int n_in, void* d_out, int out_size, void* d_ws,
                              size_t ws_size, hipStream_t stream) {
  (void)hipFuncSetAttribute((const void*)quip, hipFuncAttributeMaxDynamicSharedMemorySize, LDS_BYTES);
  quip<<<256, 512, LDS_BYTES, stream>>>((const __hip_bfloat16*)d_in[0], (const int*)d_in[1], (const __hip_bfloat16*)d_in[2],
                                        (const __hip_bfloat16*)d_in[3], (const __hip_bfloat16*)d_in[4], (const __hip_bfloat16*)d_in[5],
                                        (char*)d_ws, (__hip_bfloat16*)d_out);
}
```

```cpp
#include <hip/hip_runtime.h>
#include <hip/hip_bf16.h>
#include <utility>

typedef __attribute__((ext_vector_type(8))) __bf16 bf16x8;
typedef __attribute__((ext_vector_type(2))) __bf16 bf16x2;
typedef __attribute__((ext_vector_type(4))) short s16x4;
typedef __attribute__((ext_vector_type(16))) float f32x16;
typedef __attribute__((ext_vector_type(4))) float f32x4;
typedef __attribute__((ext_vector_type(4))) unsigned u32x4;
typedef __attribute__((ext_vector_type(2))) unsigned u32x2;
typedef __attribute__((address_space(3))) s16x4 lds_s16x4;
typedef __attribute__((address_space(3))) char lds_char;
typedef __attribute__((address_space(3))) unsigned lds_u32;
typedef __attribute__((address_space(1))) unsigned long long gu64;
typedef unsigned long long u64;

#define STAMP(i)
#define Y_AUX 2
#define XCD_PERM 0x54321706
#define YPART 0
#define XRV 0
#define POLL_SLEEP 4
constexpr int D_ = 640, F_ = 1536;

#define WAIT_V(n) asm volatile("s_waitcnt vmcnt(" #n ")" ::: "memory")
#define WAIT_L(n) asm volatile("s_waitcnt lgkmcnt(" #n ")" ::: "memory")
#define BAR() do { asm volatile("" ::: "memory"); __builtin_amdgcn_s_barrier(); asm volatile("" ::: "memory"); } while (0)
#define SB() __builtin_amdgcn_sched_barrier(0)
#define RLX_AGENT __ATOMIC_RELAXED, __HIP_MEMORY_SCOPE_AGENT
#define RLX_WG __ATOMIC_RELAXED, __HIP_MEMORY_SCOPE_WORKGROUP

__device__ __forceinline__ void set_m0(unsigned ldsaddr) { asm volatile("s_mov_b32 m0, %0\n\ts_nop 0" ::"s"(ldsaddr) : "memory"); }
template <int OFF> __device__ __forceinline__ void glds(const void* sbase, unsigned voff) {
  asm volatile("global_load_lds_dwordx4 %0, %1 offset:%2" ::"v"(voff), "s"(sbase), "i"(OFF) : "memory");
}
__device__ __forceinline__ void touch(const void* sbase, unsigned voff) {
  asm volatile("global_load_lds_dword %0, %1" ::"v"(voff), "s"(sbase) : "memory");
}
__device__ __forceinline__ unsigned pack2(float a, float b) {
  bf16x2 p; p[0] = (__bf16)a; p[1] = (__bf16)b;
  return __builtin_bit_cast(unsigned, p);
}
__device__ __forceinline__ float bflo(unsigned u) { return __builtin_bit_cast(float, u << 16); }
__device__ __forceinline__ float bfhi(unsigned u) { return __builtin_bit_cast(float, u & 0xffff0000u); }
__device__ __forceinline__ u32x4 trread2(lds_char* p0, lds_char* p1) {
  u32x2 lo = __builtin_bit_cast(u32x2, __builtin_amdgcn_ds_read_tr16_b64_v4i16((lds_s16x4*)p0));
  u32x2 hi = __builtin_bit_cast(u32x2, __builtin_amdgcn_ds_read_tr16_b64_v4i16((lds_s16x4*)p1));
  return u32x4{lo[0], lo[1], hi[0], hi[1]};
}
template <typename T> __device__ __forceinline__ T ldsr(lds_char* p) { return *(__attribute__((address_space(3))) T*)p; }
template <typename T> __device__ __forceinline__ void ldsw(lds_char* p, T v) { *(__attribute__((address_space(3))) T*)p = v; }
__device__ __forceinline__ f32x16 mfma32(u32x4 a, u32x4 b, f32x16 c) {
  return __builtin_amdgcn_mfma_f32_32x32x16_bf16(__builtin_bit_cast(bf16x8, a), __builtin_bit_cast(bf16x8, b), c, 0, 0, 0);
}
__device__ __forceinline__ u32x4 pack8(const f32x16& a, int s) {
  u32x4 r;
  r[0] = pack2(a[8 * s + 0], a[8 * s + 1]); r[1] = pack2(a[8 * s + 2], a[8 * s + 3]);
  r[2] = pack2(a[8 * s + 4], a[8 * s + 5]); r[3] = pack2(a[8 * s + 6], a[8 * s + 7]);
  return r;
}
__device__ __forceinline__ u32x2 deq4(u32x4 q, f32x4 s, f32x4 z) {
  float w0, w1, w2, w3;
  asm("v_cvt_f32_i32 %0, %4\n\tv_cvt_f32_i32 %1, %5\n\tv_cvt_f32_i32 %2, %6\n\tv_cvt_f32_i32 %3, %7\n\t"
      "v_fma_f32 %0, %0, %8, %12\n\tv_fma_f32 %1, %1, %9, %13\n\tv_fma_f32 %2, %2, %10, %14\n\tv_fma_f32 %3, %3, %11, %15\n\t"
      "v_cvt_pk_bf16_f32 %0, %0, %1\n\tv_cvt_pk_bf16_f32 %1, %2, %3"
      : "=&v"(w0), "=&v"(w1), "=&v"(w2), "=&v"(w3)
      : "v"(q[0]), "v"(q[1]), "v"(q[2]), "v"(q[3]), "v"(s[0]), "v"(s[1]), "v"(s[2]), "v"(s[3]), "v"(z[0]), "v"(z[1]), "v"(z[2]), "v"(z[3]));
  return u32x2{__builtin_bit_cast(unsigned, w0), __builtin_bit_cast(unsigned, w1)};
}
__device__ __forceinline__ void lds_spin(lds_u32* w, unsigned want) {
  while ((unsigned)__builtin_amdgcn_readfirstlane(__hip_atomic_load(w, RLX_WG)) < want) __builtin_amdgcn_s_sleep(1);
  asm volatile("" ::: "memory");
}
__device__ __forceinline__ void poll_flags(gu64* f, u64 tok) {
#pragma unroll 1
  for (unsigned spins = 0; spins < (1u << 20); ++spins) {
    if (__all(__hip_atomic_load(f, RLX_AGENT) == tok)) break;
    __builtin_amdgcn_s_sleep(POLL_SLEEP);
  }
  __builtin_amdgcn_fence(__ATOMIC_ACQUIRE, "wavefront");
}

template <int N, typename Fn, int... I>
__device__ __forceinline__ void static_for_impl(Fn&& f, std::integer_sequence<int, I...>) { (f(std::integral_constant<int, I>{}), ...); }
template <int N, typename Fn>
__device__ __forceinline__ void static_for(Fn&& f) { static_for_impl<N>(f, std::make_integer_sequence<int, N>{}); }
constexpr int x_ks(int i) { return i < 160 ? (i % 80) / 2 : i - 160; }
constexpr int x_stage(int i) { return i < 80 ? i / 8 : i < 160 ? 10 + (i - 80) / 16 : 15 + (i - 160) / 20; }
constexpr int x_uoff(int i) { return i < 80 ? 16384 + (x_ks(i) & 3) * 2048 : i < 160 ? (x_ks(i) & 7) * 2048 : (x_ks(i) % 20) * 1024; }
constexpr int x_nld(int st) { return st < 10 ? 6 : st < 15 ? 4 : st < 17 ? 5 : 0; }

constexpr int XRF_OFF = 2 << 20, SYNC_OFF = 6 << 20;
#define FS 16
constexpr int FT = 1, FX = 121;
constexpr int FA = 185 * FS;
constexpr unsigned CODE_TOUCH_MAX = 21632;
constexpr int L_SYNC = 156160, L_DUMP = 156416, LDS_BYTES = 156672;

__global__ __launch_bounds__(512, 2) void quip(const __hip_bfloat16* __restrict__ x, const int* __restrict__ qw,
                                               const __hip_bfloat16* __restrict__ scales, const __hip_bfloat16* __restrict__ zeros,
                                               const __hip_bfloat16* __restrict__ U, const __hip_bfloat16* __restrict__ V,
                                               char* ws, __hip_bfloat16* __restrict__ y) {
  extern __shared__ __attribute__((aligned(16))) char smem_[];
  lds_char* smem = (lds_char*)smem_;
  const unsigned lb = (unsigned)(size_t)smem;
  const int wid = __builtin_amdgcn_readfirstlane(threadIdx.x >> 6), lane = threadIdx.x & 63;
  const int xcd = (XCD_PERM >> ((blockIdx.x & 7) * 4)) & 7, idx = blockIdx.x >> 3;
  {
    u64 pc;
    asm volatile("s_mov_b32 m0, %3\n\ts_cmp_gt_u32 %2, 7\n\ts_cbranch_scc1 1f\n\ts_getpc_b64 %0\n\ts_nop 0\n\tglobal_load_lds_dword %1, %0\n1:"
                 : "=&s"(pc) : "v"(min(threadIdx.x * 128, CODE_TOUCH_MAX)), "s"(blockIdx.x), "s"(lb + L_DUMP) : "memory", "scc");
    SB();
  }
  const int h = lane >> 5, r = lane & 31;
  const int blk = (lane >> 4) & 1, q4 = (lane & 15) >> 2, p4 = lane & 3;
  lds_u32* sw = (lds_u32*)(smem + L_SYNC);
  gu64* sync = (gu64*)(ws + SYNC_OFF);
  const __amdgpu_buffer_rsrc_t wsr = __builtin_amdgcn_make_buffer_rsrc(ws, 0, SYNC_OFF, 0x00020000);
  const bool is_pf = idx < 12 && xcd < 6;
  if (wid == 0) STAMP(0);

  const int mb = YPART == 0 ? (xcd >> 1) * 4 + (idx & 3) : (xcd >> 2) * 8 + (idx & 7);
  const int nb = YPART == 0 ? (xcd & 1) * 8 + (idx >> 2) : (xcd & 3) * 4 + (idx >> 3);
  const unsigned fo = lane < 4 ? (FX + mb * 4 + lane) * FS * 8 : 0;
  u64 first = 0;
  auto ld_first = [&]() { asm volatile("global_load_dwordx2 %0, %1, %2 sc1" : "=v"(first) : "v"(fo), "s"(sync) : "memory"); };
  auto wait_first = [&]() { asm volatile("s_waitcnt vmcnt(0)" : "+v"(first)::"memory"); };
  if (wid == 3 && lane < 16) sw[lane] = ((lane == 8 || lane == 3) && is_pf) ? 4u : 0u;
  int myflag = 0;

  if (is_pf) {
    if (wid < 2) {
      const int ga = xcd & 1, gb = xcd >> 1;
      const bool isq = idx < 3 || (idx >= 7 && idx < 10), far = idx < 7;
      const unsigned vo_ = isq ? (lane >> 1) * (F_ * 4) + (lane & 1) * 128 : (lane >> 3) * (F_ * 2) + (lane & 7) * 128;
      const char* gbase = isq ? (const char*)qw + (size_t)(ga * 320) * (F_ * 4) : (const char*)V + gb * 1024;
      const int nc = isq ? 3 : (far ? 4 : 2), c0 = idx - (idx < 3 ? 0 : idx < 7 ? 3 : idx < 10 ? 7 : 10);
      const int ne = isq ? (far ? 80 : 160) : 96;
      BAR();
#pragma unroll 1
      for (int e = c0 + nc * wid; e < ne; e += nc * 2) {
        if (isq) {
          int t = far ? e * 3 + gb : (e >> 1) * 3 + (gb + 1 + (e & 1)) % 3;
          const int pp = t / 10, j = t - pp * 10;
          touch(gbase + j * (32 * F_ * 4) + pp * 256, vo_);
        } else {
          touch(gbase + (size_t)(e * 2 + (far ? ga : ga ^ 1)) * (8 * F_ * 2), vo_);
        }
      }
      if (wid == 0) STAMP(7);
    } else {
      WAIT_L(0);
      BAR();
      if (wid == 4) { ld_first(); wait_first(); }
    }
  } else if (xcd < 6) {
    const int t = idx - 12;
    const int mt = (xcd & 1) * 5 + t % 5, nt = (xcd >> 1) * 4 + t / 5;
    myflag = FT + nt * 10 + mt;
    if (wid >= 4) {
      const int w4 = wid - 4, rr = lane >> 4, c16 = lane & 15;
      unsigned ag[4], bg[4];
#pragma unroll
      for (int i = 0; i < 4; ++i) {
        int row = i * 4 + rr;
        ag[i] = row * (F_ * 4) + ((c16 ^ row) * 16) - i * 1024;
        bg[i] = row * (F_ * 2) + ((c16 ^ (rr << 2)) * 16) - i * 1024;
      }
      const char* ap = (const char*)qw + (size_t)(mt * 64 + w4 * 16) * (F_ * 4);
      const char* bp = (const char*)V + nt * 256 + (size_t)(w4 * 16) * (F_ * 2);
      unsigned so = lb + w4 * 4096;
      auto issue = [&](int p) {
        unsigned base = so + (p & 3) * 32768;
        set_m0(base);
        glds<0>(ap, ag[0]); glds<1024>(ap, ag[1]); glds<2048>(ap, ag[2]); glds<3072>(ap, ag[3]);
        set_m0(base + 16384);
        glds<0>(bp, bg[0]); glds<1024>(bp, bg[1]); glds<2048>(bp, bg[2]); glds<3072>(bp, bg[3]);
        ap += 256; bp += 64 * F_ * 2;
      };
      issue(0);
      issue(1);
#pragma unroll 1
      for (int p = 2; p < 24; ++p) {
        WAIT_V(8);
        BAR();
        issue(p);
      }
      if (wid == 4) {
        ld_first();
        WAIT_V(9); BAR(); WAIT_V(1); BAR();
        wait_first();
      } else {
        WAIT_V(8); BAR(); WAIT_V(0); BAR();
      }
    } else {
      const int m = wid & 1, kl = wid >> 1;
      lds_char* TB = smem + 131072 + wid * 6144;
      u32x2 sr[3], zr[3];
      {
        const unsigned o = ((mt * 2 + m) * F_ + (lane >> 3) * 64 + kl * 32 + (lane & 7) * 4) * 2;
#pragma unroll
        for (int j = 0; j < 3; ++j) {
          sr[j] = *(const u32x2*)((const char*)scales + o + j * 1024);
          zr[j] = *(const u32x2*)((const char*)zeros + o + j * 1024);
        }
      }
      auto cvt = [&](int j) {
        f32x4 sa, za;
        sa[0] = bflo(sr[j][0]); sa[1] = bfhi(sr[j][0]); sa[2] = bflo(sr[j][1]); sa[3] = bfhi(sr[j][1]);
        za[0] = -bflo(zr[j][0]) * sa[0]; za[1] = -bfhi(zr[j][0]) * sa[1]; za[2] = -bflo(zr[j][1]) * sa[2]; za[3] = -bfhi(zr[j][1]) * sa[3];
        lds_char* d = TB + j * 2048 + (lane >> 3) * 256 + (lane & 7) * 16;
        ldsw<f32x4>(d, sa); ldsw<f32x4>(d + 128, za);
      };
      int aoff[4], boff[4];
#pragma unroll
      for (int c = 0; c < 4; ++c) {
        int ch = 8 * kl + 4 * (c >> 1) + 2 * h + (c & 1);
        aoff[c] = (m * 32 + r) * 256 + ((ch ^ (r & 15)) * 16);
        boff[c] = 16384 + (32 * kl + 8 * h + q4) * 256 + ((c ^ (2 * kl) ^ q4) * 64) + 32 * (p4 & 1) + 16 * blk + 8 * (p4 >> 1);
      }
      lds_char* tp = TB + h * 32;
      f32x16 acc[4] = {};
      f32x4 ts[4], tz[4];
      u32x4 av[4];
      u32x4 af[2][2] = {}, bf[2][2][4] = {};
      auto rdtab = [&](int o, int c) {
        ts[c] = ldsr<f32x4>(tp + o + (c >> 1) * 64 + (c & 1) * 16);
        tz[c] = ldsr<f32x4>(tp + o + 128 + (c >> 1) * 64 + (c & 1) * 16);
      };
      auto step = [&](int slot) {
        const int buf = slot & 1;
        lds_char* sl = smem + slot * 32768;
        BAR();
#pragma unroll
        for (int c = 0; c < 4; ++c) av[c] = ldsr<u32x4>(sl + aoff[c]);
        SB();
#pragma unroll
        for (int u = 0; u < 8; ++u) {
          acc[u & 3] = mfma32(af[buf ^ 1][u >> 2], bf[buf ^ 1][u >> 2][u & 3], acc[u & 3]);
          SB();
          if (u < 4) {
#pragma unroll
            for (int v = 2 * u; v < 2 * u + 2; ++v)
              bf[buf][v >> 2][v & 3] = trread2(sl + boff[v & 3] + (16 * (v >> 2)) * 256, sl + boff[v & 3] + (16 * (v >> 2) + 4) * 256);
          } else {
            const int c = u - 4;
            u32x2 o = deq4(av[c], ts[c], tz[c]);
            af[buf][c >> 1][(c & 1) * 2] = o[0]; af[buf][c >> 1][(c & 1) * 2 + 1] = o[1];
            SB();
            rdtab((slot + 1) * 256, c);
          }
          SB();
        }
      };
#pragma unroll
      for (int c = 0; c < 4; ++c)
        asm volatile("" : "+v"(acc[c]), "+v"(bf[1][0][c]), "+v"(bf[1][1][c]), "+v"(aoff[c]), "+v"(boff[c]));
      asm volatile("" : "+v"(af[1][0]), "+v"(af[1][1]));
      SB();
      cvt(0);
#pragma unroll
      for (int c = 0; c < 4; ++c) rdtab(0, c);
      cvt(1); cvt(2);
      if (wid == 0) STAMP(9);
#pragma unroll 1
      for (int p = 0; p < 24; p += 4) {
        step(0); step(1); step(2); step(3);
        tp += 1024;
      }
      if (wid == 0) STAMP(1);
      lds_char* RED = smem + m * 16384 + lane * 16;
      const int n32 = nt * 4 + kl * 2;
      const unsigned oo0 = (((n32 / 3) * 40 + (mt * 2 + m) * 2) * 3 + n32 % 3) * 1024;
      const unsigned oo1 = ((((n32 + 1) / 3) * 40 + (mt * 2 + m) * 2) * 3 + (n32 + 1) % 3) * 1024;
      SB();
#pragma unroll
      for (int u = 0; u < 8; ++u) {
        const int c = (u < 4 ? 2 : 0) + ((u >> 1) & 1), s2 = u & 1;
        acc[c] = mfma32(af[1][s2], bf[1][s2][c], acc[c]);
        SB();
        if (u >= 3 && u < 7) {
#pragma unroll
          for (int j = 2 * (u - 3); j < 2 * (u - 3) + 2; ++j) {
            const f32x16& g = acc[2 + (j >> 2)];
            const int q = 4 * (j & 3);
            ldsw<f32x4>(RED + kl * 8192 + j * 1024, f32x4{g[q], g[q + 1], g[q + 2], g[q + 3]});
          }
          SB();
        }
      }
      asm volatile("" ::: "memory");
      __hip_atomic_store(sw + 9 + wid, 1u, RLX_WG);
      while (__builtin_amdgcn_readfirstlane(__hip_atomic_load(sw + 9 + (wid ^ 2), RLX_WG)) == 0) {}
      asm volatile("" ::: "memory");
      f32x4 pv[8];
#pragma unroll
      for (int j = 0; j < 8; ++j) pv[j] = ldsr<f32x4>(RED + (kl ^ 1) * 8192 + j * 1024);
      SB();
#pragma unroll
      for (int j = 0; j < 4; ++j) {
        f32x16& o = acc[j >> 1];
#pragma unroll
        for (int e = 0; e < 8; ++e) o[(j & 1) * 8 + e] += pv[2 * j + (e >> 2)][e & 3];
        __builtin_amdgcn_raw_buffer_store_b128(pack8(o, j & 1), wsr, lane * 16 + (j & 1) * 3072, j < 2 ? oo0 : oo1, 16);
      }
    }
  } else {
    const int mi = idx & 7, eb = idx >> 3;
    const int xmb = (xcd - 6) * 8 + mi;
    const int ea = eb & 1;
    myflag = FX + xmb * 4 + eb;
    if (wid >= 4) {
      const int w4 = wid - 4;
      unsigned xg[4], ug[2];
#pragma unroll
      for (int i = 0; i < 4; ++i) {
        int row = i * 8 + (lane >> 3);
        xg[i] = row * (D_ * 2) + (((lane & 7) ^ ((row >> 1) & 7)) * 16) - i * 1024;
      }
#pragma unroll
      for (int i = 0; i < 2; ++i) ug[i] = (i * 8 + (lane >> 3)) * (D_ * 2) + (((lane & 7) ^ (((lane >> 4) & 1) << 2)) * 16) - i * 1024;
      const unsigned cg = (lane >> 2) * (D_ * 2) + (lane & 3) * 16;
      const char* xb = (const char*)x + (size_t)(xmb * 128 + w4 * 32) * (D_ * 2);
      const char* ub = (const char*)U + eb * 320 + ea * 64 + (size_t)(w4 * 16) * (D_ * 2);
      int ks = mi + ((eb * 5 + 1) >> 1);
      if (ks >= 10) ks -= 10;
      auto issue = [&](auto sc) {
        constexpr int st = decltype(sc)::value;
        const unsigned base = lb + (st % 5) * 24576;
        if constexpr (st < 10) {
          const char* xp = xb + ks * 128;
          const char* up = ub + (size_t)ks * 64 * (D_ * 2);
          set_m0(base + w4 * 4096);
          glds<0>(xp, xg[0]); glds<1024>(xp, xg[1]); glds<2048>(xp, xg[2]); glds<3072>(xp, xg[3]);
          set_m0(base + 16384 + w4 * 2048);
          glds<0>(up, ug[0]); glds<1024>(up, ug[1]);
          if (++ks == 10) ks = 0;
        } else if constexpr (st < 15) {
#pragma unroll
          for (int j = 0; j < 2; ++j) {
            const char* up = ub + 128 + (size_t)ks * 64 * (D_ * 2);
            set_m0(base + j * 8192 + w4 * 2048);
            glds<0>(up, ug[0]); glds<1024>(up, ug[1]);
            if (++ks == 10) ks = 0;
          }
        } else if constexpr (st < 17) {
#pragma unroll
          for (int j = 0; j < 5; ++j) {
            set_m0(base + j * 4096 + w4 * 1024);
            glds<0>(ub + (ea ? -64 : 256) + (size_t)ks * 64 * (D_ * 2), cg);
            if (++ks == 10) ks = 0;
          }
        }
      };
      issue(std::integral_constant<int, 0>{});
      issue(std::integral_constant<int, 1>{});
      static_for<17>([&](auto sc) {
        constexpr int st = decltype(sc)::value;
        asm volatile("s_waitcnt vmcnt(%0)" ::"i"(x_nld(st + 1)) : "memory");
        BAR();
        issue(std::integral_constant<int, st + 2>{});
      });
      if (wid == 4) { ld_first(); wait_first(); }
    } else {
      int xoff[4], uoff[2];
#pragma unroll
      for (int s = 0; s < 4; ++s) xoff[s] = (wid * 32 + r) * 128 + (((2 * s + h) ^ ((r >> 1) & 7)) * 16);
#pragma unroll
      for (int t = 0; t < 2; ++t) uoff[t] = (8 * h + q4) * 128 + ((t ^ ((q4 >> 1) & 1)) * 64) + blk * 32 + p4 * 8;
      const int coff = (8 * h + q4) * 64 + blk * 32 + p4 * 8;
      const unsigned xo = XRF_OFF + ((xmb * 4 + wid) * 40 + eb * 10) * 1024;
      f32x16 acc[2] = {};
      u32x4 xf[40], uf[8];
      auto rd = [&](auto ic) {
        constexpr int i = decltype(ic)::value;
        if constexpr (i < 200) {
          if constexpr (i == 0 || x_stage(i) != x_stage(i - 1)) BAR();
          lds_char* base = smem + (x_stage(i) % 5) * 24576;
          if constexpr (i < 80 && (i & 1) == 0) xf[i / 2] = ldsr<u32x4>(base + xoff[(i / 2) & 3]);
          if constexpr (i < 160) uf[i & 7] = trread2(base + x_uoff(i) + uoff[i & 1], base + x_uoff(i) + uoff[i & 1] + 512);
          else uf[i & 7] = trread2(base + x_uoff(i) + coff, base + x_uoff(i) + coff + 256);
        }
      };
      u32x4 pk[4];
      WAIT_L(0);
      static_for<6>(rd);
      static_for<200>([&](auto ic) {
        constexpr int i = decltype(ic)::value;
        constexpr int a = i < 160 ? (i & 1) : 0;
        if constexpr (i == 80 || i == 160) {
#pragma unroll
          for (int j = 0; j < 4; ++j) pk[j] = pack8(acc[j >> 1], j & 1);
          acc[0] = f32x16{}; acc[1] = f32x16{};
          SB();
        }
        acc[a] = mfma32(uf[i & 7], xf[x_ks(i)], acc[a]);
        SB();
        rd(std::integral_constant<int, i + 6>{});
        if constexpr (i >= 80 && i % 80 < 8 && (i & 1) == 0)
          __builtin_amdgcn_raw_buffer_store_b128(pk[(i % 80) / 2], wsr, lane * 16 + ((i % 80) / 2) * 1024, xo + ea * 2048 + (i / 160) * 4096, 16);
        SB();
      });
      __builtin_amdgcn_raw_buffer_store_b128(pack8(acc[0], 0), wsr, lane * 16, xo + (ea ? 0 : 8192), 16);
      __builtin_amdgcn_raw_buffer_store_b128(pack8(acc[0], 1), wsr, lane * 16 + 1024, xo + (ea ? 0 : 8192), 16);
      if (wid == 0) STAMP(3);
    }
  }

  const unsigned vo = lane * 16;
  if (wid < 4) {
    if (!is_pf) {
      if (wid == 0) STAMP(10);
      if (lane == 0) __hip_atomic_fetch_add(sw + 8, 1u, RLX_WG);
    }
    const int n0 = (nb * 3) >> 2, n1 = (nb * 3 + 2) >> 2;
    gu64* tfl = sync + (FT + n0 * 10 + min(lane, (n1 - n0) * 10 + 9)) * FS;
    lds_spin(sw + 2, 1);
    const u64 tok = (u64)sw[0] | ((u64)sw[1] << 32);
    if (!is_pf) {
      asm volatile("s_waitcnt vmcnt(0) lgkmcnt(0)" ::: "memory");
      if (lane == 0) {
        if (__hip_atomic_fetch_add(sw + 3, 1u, RLX_WG) == 3) __hip_atomic_store(sync + myflag * FS, tok, RLX_AGENT);
      }
      if (wid == 0) STAMP(2);
    }
    if (wid == 0) {
      lds_spin(sw + 8, 4);
      poll_flags(tfl, tok);
      if (lane == 0) __hip_atomic_store(sw + 5, 1u, RLX_WG);
      STAMP(4);
    } else {
      lds_spin(sw + 5, 1);
    }
    const char* tsrc = ws + nb * 122880 + wid * 3072;
    unsigned tdst = lb + wid * 3072;
    auto issue = [&]() {
      asm volatile("s_mov_b32 m0, %2\n\ts_nop 0\n\tglobal_load_lds_dwordx4 %0, %1 sc1\n\tglobal_load_lds_dwordx4 %0, %1 offset:1024 sc1\n\t"
                   "global_load_lds_dwordx4 %0, %1 offset:2048 sc1" ::"v"(vo), "s"(tsrc), "s"(tdst) : "memory");
      tsrc += 12288; tdst += 12288;
    };
    issue(); issue(); issue(); issue();
#pragma unroll 1
    for (int ch = 0; ch < 6; ++ch) {
      WAIT_V(9);
      BAR();
      issue();
    }
    WAIT_V(9); BAR(); WAIT_V(6); BAR(); WAIT_V(3); BAR(); WAIT_V(0); BAR();
    if (blockIdx.x == 1 && wid == 2) {
#pragma unroll 1
      for (unsigned spins = 0; spins < (1u << 20); ++spins) {
        bool ok = true;
#pragma unroll
        for (int i = 0; i < 4; ++i) ok &= __hip_atomic_load(sync + FA + i * 64 + lane, RLX_AGENT) == tok;
        if (__all(ok)) break;
        __builtin_amdgcn_s_sleep(POLL_SLEEP);
      }
      if (lane == 0) __hip_atomic_store(sync, tok, RLX_AGENT);
    }
  } else {
    const int w4 = wid - 4;
    if (wid == 4) {
      const u64 tok = (((u64)__builtin_amdgcn_readlane((unsigned)(first >> 32), 4) << 32) | (unsigned)__builtin_amdgcn_readlane((unsigned)first, 4)) +
                      0x9E3779B97F4A7C15ull;
      if (lane == 0) {
        sw[0] = (unsigned)tok; sw[1] = (unsigned)(tok >> 32);
        asm volatile("" ::: "memory");
        __hip_atomic_store(sw + 2, 1u, RLX_WG);
        __hip_atomic_store(sync + FA + blockIdx.x, tok, RLX_AGENT);
      }
      if (!__all(lane >= 4 || first == tok)) poll_flags(sync + (FX + mb * 4 + (lane & 3)) * FS, tok);
      if (lane == 0) __hip_atomic_store(sw + 4, 1u, RLX_WG);
      STAMP(8);
    } else {
      lds_spin(sw + 4, 1);
    }
    lds_spin(sw + 3, 4);
    u32x4 xf[40];
    const unsigned xo = XRF_OFF + (mb * 4 + w4) * 40960;
#pragma unroll
    for (int ch = 0; ch < 10; ++ch) {
#pragma unroll
      for (int i = 0; i < 4; ++i) xf[ch * 4 + i] = __builtin_amdgcn_raw_buffer_load_b128(wsr, lane * 16 + i * 1024, xo + ch * 4096, 16);
    }
    SB();
    if (wid == 4) STAMP(11);
    f32x16 acc[3] = {};
    u32x4 tf[3][3];
    lds_char* tb = smem + lane * 16;
    BAR();
#pragma unroll
    for (int c = 0; c < 6; ++c) tf[c / 3][c % 3] = ldsr<u32x4>(tb + c * 1024);
#pragma unroll
    for (int ks = 0; ks < 40; ++ks) {
#pragma unroll
      for (int c = 0; c < 3; ++c) {
        acc[c] = mfma32(tf[ks % 3][c], xf[ks], acc[c]);
        SB();
        if (ks < 38) {
          if (c == 0 && (ks & 3) == 2) BAR();
          tf[(ks + 2) % 3][c] = ldsr<u32x4>(tb + ((ks + 2) * 3 + c) * 1024);
        }
        SB();
      }
    }
    if (wid == 4) STAMP(5);
    lds_char* eb = smem + 122880 + w4 * 6656;
#pragma unroll
    for (int c = 0; c < 3; ++c) {
      ldsw<u32x4>(eb + r * 208 + c * 64 + h * 32, pack8(acc[c], 0));
      ldsw<u32x4>(eb + r * 208 + c * 64 + h * 32 + 16, pack8(acc[c], 1));
    }
    const __amdgpu_buffer_rsrc_t yr = __builtin_amdgcn_make_buffer_rsrc((void*)y, 0, 2048 * F_ * 2, 0x00020000);
    const unsigned yo = ((mb * 128 + w4 * 32) * F_ + nb * 96) * 2;
#pragma unroll
    for (int i = 0; i < 6; ++i) {
      int q = i * 64 + lane, row = q / 12, chn = q % 12;
      __builtin_amdgcn_raw_buffer_store_b128(ldsr<u32x4>(eb + row * 208 + chn * 16), yr, row * (F_ * 2) + chn * 16, yo, Y_AUX);
    }
  }
}

extern "C" void kernel_launch(void* const* d_in, const int* in_sizes, int n_in, void* d_out, int out_size, void* d_ws,
                              size_t ws_size, hipStream_t stream) {
  (void)hipFuncSetAttribute((const void*)quip, hipFuncAttributeMaxDynamicSharedMemorySize, LDS_BYTES);
  quip<<<256, 512, LDS_BYTES, stream>>>((const __hip_bfloat16*)d_in[0], (const int*)d_in[1], (const __hip_bfloat16*)d_in[2],
                                        (const __hip_bfloat16*)d_in[3], (const __hip_bfloat16*)d_in[4], (const __hip_bfloat16*)d_in[5],
                                        (char*)d_ws, (__hip_bfloat16*)d_out);
}
```

```cpp
#include <hip/hip_runtime.h>
#include <hip/hip_bf16.h>
#include <utility>

typedef __attribute__((ext_vector_type(8))) __bf16 bf16x8;
typedef __attribute__((ext_vector_type(2))) __bf16 bf16x2;
typedef __attribute__((ext_vector_type(4))) short s16x4;
typedef __attribute__((ext_vector_type(16))) float f32x16;
typedef __attribute__((ext_vector_type(4))) float f32x4;
typedef __attribute__((ext_vector_type(4))) unsigned u32x4;
typedef __attribute__((ext_vector_type(2))) unsigned u32x2;
typedef __attribute__((address_space(3))) s16x4 lds_s16x4;
typedef __attribute__((address_space(3))) char lds_char;
typedef __attribute__((address_space(3))) unsigned lds_u32;
typedef __attribute__((address_space(1))) unsigned long long gu64;
typedef unsigned long long u64;

constexpr unsigned XCD_PERM = 0x54321706;
constexpr int POLL_SLEEP = 4;
constexpr int D_ = 640, F_ = 1536;

#define WAIT_V(n) asm volatile("s_waitcnt vmcnt(" #n ")" ::: "memory")
#define WAIT_L(n) asm volatile("s_waitcnt lgkmcnt(" #n ")" ::: "memory")
#define BAR() do { asm volatile("" ::: "memory"); __builtin_amdgcn_s_barrier(); asm volatile("" ::: "memory"); } while (0)
#define SB() __builtin_amdgcn_sched_barrier(0)
#define RLX_AGENT __ATOMIC_RELAXED, __HIP_MEMORY_SCOPE_AGENT
#define RLX_WG __ATOMIC_RELAXED, __HIP_MEMORY_SCOPE_WORKGROUP

__device__ __forceinline__ void set_m0(unsigned ldsaddr) { asm volatile("s_mov_b32 m0, %0\n\ts_nop 0" ::"s"(ldsaddr) : "memory"); }
template <int OFF> __device__ __forceinline__ void glds(const void* sbase, unsigned voff) {
  asm volatile("global_load_lds_dwordx4 %0, %1 offset:%2" ::"v"(voff), "s"(sbase), "i"(OFF) : "memory");
}
__device__ __forceinline__ void touch(const void* sbase, unsigned voff) {
  asm volatile("global_load_lds_dword %0, %1" ::"v"(voff), "s"(sbase) : "memory");
}
__device__ __forceinline__ unsigned pack2(float a, float b) {
  bf16x2 p; p[0] = (__bf16)a; p[1] = (__bf16)b;
  return __builtin_bit_cast(unsigned, p);
}
__device__ __forceinline__ float bflo(unsigned u) { return __builtin_bit_cast(float, u << 16); }
__device__ __forceinline__ float bfhi(unsigned u) { return __builtin_bit_cast(float, u & 0xffff0000u); }
__device__ __forceinline__ u32x4 trread2(lds_char* p0, lds_char* p1) {
  u32x2 lo = __builtin_bit_cast(u32x2, __builtin_amdgcn_ds_read_tr16_b64_v4i16((lds_s16x4*)p0));
  u32x2 hi = __builtin_bit_cast(u32x2, __builtin_amdgcn_ds_read_tr16_b64_v4i16((lds_s16x4*)p1));
  return u32x4{lo[0], lo[1], hi[0], hi[1]};
}
template <typename T> __device__ __forceinline__ T ldsr(lds_char* p) { return *(__attribute__((address_space(3))) T*)p; }
template <typename T> __device__ __forceinline__ void ldsw(lds_char* p, T v) { *(__attribute__((address_space(3))) T*)p = v; }
__device__ __forceinline__ f32x16 mfma32(u32x4 a, u32x4 b, f32x16 c) {
  return __builtin_amdgcn_mfma_f32_32x32x16_bf16(__builtin_bit_cast(bf16x8, a), __builtin_bit_cast(bf16x8, b), c, 0, 0, 0);
}
__device__ __forceinline__ u32x4 pack8(const f32x16& a, int s) {
  u32x4 r;
  r[0] = pack2(a[8 * s + 0], a[8 * s + 1]); r[1] = pack2(a[8 * s + 2], a[8 * s + 3]);
  r[2] = pack2(a[8 * s + 4], a[8 * s + 5]); r[3] = pack2(a[8 * s + 6], a[8 * s + 7]);
  return r;
}
__device__ __forceinline__ u32x2 deq4(u32x4 q, f32x4 s, f32x4 z) {
  float w0, w1, w2, w3;
  asm("v_cvt_f32_i32 %0, %4\n\tv_cvt_f32_i32 %1, %5\n\tv_cvt_f32_i32 %2, %6\n\tv_cvt_f32_i32 %3, %7\n\t"
      "v_fma_f32 %0, %0, %8, %12\n\tv_fma_f32 %1, %1, %9, %13\n\tv_fma_f32 %2, %2, %10, %14\n\tv_fma_f32 %3, %3, %11, %15\n\t"
      "v_cvt_pk_bf16_f32 %0, %0, %1\n\tv_cvt_pk_bf16_f32 %1, %2, %3"
      : "=&v"(w0), "=&v"(w1), "=&v"(w2), "=&v"(w3)
      : "v"(q[0]), "v"(q[1]), "v"(q[2]), "v"(q[3]), "v"(s[0]), "v"(s[1]), "v"(s[2]), "v"(s[3]), "v"(z[0]), "v"(z[1]), "v"(z[2]), "v"(z[3]));
  return u32x2{__builtin_bit_cast(unsigned, w0), __builtin_bit_cast(unsigned, w1)};
}
__device__ __forceinline__ void lds_spin(lds_u32* w, unsigned want) {
  while ((unsigned)__builtin_amdgcn_readfirstlane(__hip_atomic_load(w, RLX_WG)) < want) __builtin_amdgcn_s_sleep(1);
  asm volatile("" ::: "memory");
}
__device__ __forceinline__ void poll_flags(gu64* f, u64 tok) {
#pragma unroll 1
  for (unsigned spins = 0; spins < (1u << 20); ++spins) {
    if (__all(__hip_atomic_load(f, RLX_AGENT) == tok)) break;
    __builtin_amdgcn_s_sleep(POLL_SLEEP);
  }
  __builtin_amdgcn_fence(__ATOMIC_ACQUIRE, "wavefront");
}

template <int N, typename Fn, int... I>
__device__ __forceinline__ void static_for_impl(Fn&& f, std::integer_sequence<int, I...>) { (f(std::integral_constant<int, I>{}), ...); }
template <int N, typename Fn>
__device__ __forceinline__ void static_for(Fn&& f) { static_for_impl<N>(f, std::make_integer_sequence<int, N>{}); }
constexpr int x_ks(int i) { return i < 160 ? (i % 80) / 2 : i - 160; }
constexpr int x_stage(int i) { return i < 80 ? i / 8 : i < 160 ? 10 + (i - 80) / 16 : 15 + (i - 160) / 20; }
constexpr int x_uoff(int i) { return i < 80 ? 16384 + (x_ks(i) & 3) * 2048 : i < 160 ? (x_ks(i) & 7) * 2048 : (x_ks(i) % 20) * 1024; }
constexpr int x_nld(int st) { return st < 10 ? 6 : st < 15 ? 4 : st < 17 ? 5 : 0; }

constexpr int XRF_OFF = 2 << 20, SYNC_OFF = 6 << 20;
constexpr int FS = 16;
constexpr int FT = 1, FX = 121;
constexpr int FA = 185 * FS;
constexpr unsigned CODE_TOUCH_MAX = 20736;
constexpr int L_SYNC = 156160, L_DUMP = 156416, LDS_BYTES = 156672;

__global__ __launch_bounds__(512, 2) void quip(const __hip_bfloat16* __restrict__ x, const int* __restrict__ qw,
                                               const __hip_bfloat16* __restrict__ scales, const __hip_bfloat16* __restrict__ zeros,
                                               const __hip_bfloat16* __restrict__ U, const __hip_bfloat16* __restrict__ V,
                                               char* ws, __hip_bfloat16* __restrict__ y) {
  extern __shared__ __attribute__((aligned(16))) char smem_[];
  lds_char* smem = (lds_char*)smem_;
  const unsigned lb = (unsigned)(size_t)smem;
  const int wid = __builtin_amdgcn_readfirstlane(threadIdx.x >> 6), lane = threadIdx.x & 63;
  const int xcd = (XCD_PERM >> ((blockIdx.x & 7) * 4)) & 7, idx = blockIdx.x >> 3;
  {
    u64 pc;
    asm volatile("s_mov_b32 m0, %3\n\ts_cmp_gt_u32 %2, 7\n\ts_cbranch_scc1 1f\n\ts_getpc_b64 %0\n\ts_nop 0\n\tglobal_load_lds_dword %1, %0\n1:"
                 : "=&s"(pc) : "v"(min(threadIdx.x * 128, CODE_TOUCH_MAX)), "s"(blockIdx.x), "s"(lb + L_DUMP) : "memory", "scc");
    SB();
  }
  const int h = lane >> 5, r = lane & 31;
  const int blk = (lane >> 4) & 1, q4 = (lane & 15) >> 2, p4 = lane & 3;
  lds_u32* sw = (lds_u32*)(smem + L_SYNC);
  gu64* sync = (gu64*)(ws + SYNC_OFF);
  const __amdgpu_buffer_rsrc_t wsr = __builtin_amdgcn_make_buffer_rsrc(ws, 0, SYNC_OFF, 0x00020000);
  const bool is_pf = idx < 12 && xcd < 6;

  const int mb = (xcd >> 1) * 4 + (idx & 3);
  const int nb = (xcd & 1) * 8 + (idx >> 2);
  const unsigned fo = lane < 4 ? (FX + mb * 4 + lane) * FS * 8 : 0;
  u64 first = 0;
  auto ld_first = [&]() { asm volatile("global_load_dwordx2 %0, %1, %2 sc1" : "=v"(first) : "v"(fo), "s"(sync) : "memory"); };
  auto wait_first = [&]() { asm volatile("s_waitcnt vmcnt(0)" : "+v"(first)::"memory"); };
  if (wid == 3 && lane < 16) sw[lane] = ((lane == 8 || lane == 3) && is_pf) ? 4u : 0u;
  int myflag = 0;

  if (is_pf) {
    const int ga = xcd & 1, gb = xcd >> 1;
    if (wid < 2) {
      const bool isq = idx < 3 || (idx >= 7 && idx < 10), far = idx < 7;
      const unsigned vo_ = isq ? (lane >> 1) * (F_ * 4) + (lane & 1) * 128 : (lane >> 3) * (F_ * 2) + (lane & 7) * 128;
      const char* gbase = isq ? (const char*)qw + (size_t)(ga * 320) * (F_ * 4) : (const char*)V + gb * 1024;
      const int nc = isq ? 3 : (far ? 4 : 2), c0 = idx - (idx < 3 ? 0 : idx < 7 ? 3 : idx < 10 ? 7 : 10);
      const int ne = isq ? (far ? 80 : 160) : 96;
      BAR();
#pragma unroll 1
      for (int e = c0 + nc * wid; e < ne; e += nc * 2) {
        if (isq) {
          int t = far ? e * 3 + gb : (e >> 1) * 3 + (gb + 1 + (e & 1)) % 3;
          const int pp = t / 10, j = t - pp * 10;
          touch(gbase + j * (32 * F_ * 4) + pp * 256, vo_);
        } else {
          touch(gbase + (size_t)(e * 2 + (far ? ga : ga ^ 1)) * (8 * F_ * 2), vo_);
        }
      }
    } else {
      WAIT_L(0);
      BAR();
      if (wid == 4) { ld_first(); wait_first(); }
    }
  } else if (xcd < 6) {
    const int t = idx - 12;
    const int mt = (xcd & 1) * 5 + t % 5, nt = (xcd >> 1) * 4 + t / 5;
    myflag = FT + nt * 10 + mt;
    if (wid >= 4) {
      const int w4 = wid - 4, rr = lane >> 4, c16 = lane & 15;
      unsigned ag[4], bg[4];
#pragma unroll
      for (int i = 0; i < 4; ++i) {
        int row = i * 4 + rr;
        ag[i] = row * (F_ * 4) + ((c16 ^ row) * 16) - i * 1024;
        bg[i] = row * (F_ * 2) + ((c16 ^ (rr << 2)) * 16) - i * 1024;
      }
      const char* ap = (const char*)qw + (size_t)(mt * 64 + w4 * 16) * (F_ * 4);
      const char* bp = (const char*)V + nt * 256 + (size_t)(w4 * 16) * (F_ * 2);
      unsigned so = lb + w4 * 4096;
      auto issue = [&](int p) {
        unsigned base = so + (p & 3) * 32768;
        set_m0(base);
        glds<0>(ap, ag[0]); glds<1024>(ap, ag[1]); glds<2048>(ap, ag[2]); glds<3072>(ap, ag[3]);
        set_m0(base + 16384);
        glds<0>(bp, bg[0]); glds<1024>(bp, bg[1]); glds<2048>(bp, bg[2]); glds<3072>(bp, bg[3]);
        ap += 256; bp += 64 * F_ * 2;
      };
      issue(0);
      issue(1);
#pragma unroll 1
      for (int p = 2; p < 24; ++p) {
        WAIT_V(8);
        BAR();
        issue(p);
      }
      if (wid == 4) {
        ld_first();
        WAIT_V(9); BAR(); WAIT_V(1); BAR();
        wait_first();
      } else {
        WAIT_V(8); BAR(); WAIT_V(0); BAR();
      }
    } else {
      const int m = wid & 1, kl = wid >> 1;
      lds_char* TB = smem + 131072 + wid * 6144;
      u32x2 sr[3], zr[3];
      {
        const unsigned o = ((mt * 2 + m) * F_ + (lane >> 3) * 64 + kl * 32 + (lane & 7) * 4) * 2;
#pragma unroll
        for (int j = 0; j < 3; ++j) {
          sr[j] = *(const u32x2*)((const char*)scales + o + j * 1024);
          zr[j] = *(const u32x2*)((const char*)zeros + o + j * 1024);
        }
      }
      auto cvt = [&](int j) {
        f32x4 sa, za;
        sa[0] = bflo(sr[j][0]); sa[1] = bfhi(sr[j][0]); sa[2] = bflo(sr[j][1]); sa[3] = bfhi(sr[j][1]);
        za[0] = -bflo(zr[j][0]) * sa[0]; za[1] = -bfhi(zr[j][0]) * sa[1]; za[2] = -bflo(zr[j][1]) * sa[2]; za[3] = -bfhi(zr[j][1]) * sa[3];
        lds_char* d = TB + j * 2048 + (lane >> 3) * 256 + (lane & 7) * 16;
        ldsw<f32x4>(d, sa); ldsw<f32x4>(d + 128, za);
      };
      lds_char *ap[2][4], *bp[2][4];
#pragma unroll
      for (int c = 0; c < 4; ++c) {
        int ch = 8 * kl + 4 * (c >> 1) + 2 * h + (c & 1);
        ap[0][c] = smem + (m * 32 + r) * 256 + ((ch ^ (r & 15)) * 16);
        bp[0][c] = smem + 16384 + (32 * kl + 8 * h + q4) * 256 + ((c ^ (2 * kl) ^ q4) * 64) + 32 * (p4 & 1) + 16 * blk + 8 * (p4 >> 1);
        ap[1][c] = ap[0][c] + 65536; bp[1][c] = bp[0][c] + 65536;
      }
      lds_char* tp = TB + h * 32;
      f32x16 acc[4] = {};
      f32x4 ts[4], tz[4];
      u32x4 av[4];
      u32x4 af[2][2] = {}, bf[2][2][4] = {};
      auto rdtab = [&](int o, int c) {
        ts[c] = ldsr<f32x4>(tp + o + (c >> 1) * 64 + (c & 1) * 16);
        tz[c] = ldsr<f32x4>(tp + o + 128 + (c >> 1) * 64 + (c & 1) * 16);
      };
      auto step = [&](int slot) {
        const int buf = slot & 1;
        BAR();
#pragma unroll
        for (int c = 0; c < 4; ++c) av[c] = ldsr<u32x4>(ap[slot >> 1][c] + buf * 32768);
        SB();
#pragma unroll
        for (int u = 0; u < 8; ++u) {
          acc[u & 3] = mfma32(af[buf ^ 1][u >> 2], bf[buf ^ 1][u >> 2][u & 3], acc[u & 3]);
          SB();
          if (u < 4) {
#pragma unroll
            for (int v = 2 * u; v < 2 * u + 2; ++v)
              bf[buf][v >> 2][v & 3] = trread2(bp[slot >> 1][v & 3] + buf * 32768 + (v >> 2) * 4096, bp[slot >> 1][v & 3] + buf * 32768 + (v >> 2) * 4096 + 1024);
          } else {
            const int c = u - 4;
            u32x2 o = deq4(av[c], ts[c], tz[c]);
            af[buf][c >> 1][(c & 1) * 2] = o[0]; af[buf][c >> 1][(c & 1) * 2 + 1] = o[1];
            SB();
            rdtab((slot + 1) * 256, c);
          }
          SB();
        }
      };
#pragma unroll
      for (int c = 0; c < 4; ++c)
        asm volatile("" : "+v"(acc[c]), "+v"(bf[1][0][c]), "+v"(bf[1][1][c]), "+v"(ap[0][c]), "+v"(bp[0][c]), "+v"(ap[1][c]), "+v"(bp[1][c]));
      asm volatile("" : "+v"(af[1][0]), "+v"(af[1][1]));
      SB();
      cvt(0);
#pragma unroll
      for (int c = 0; c < 4; ++c) rdtab(0, c);
      cvt(1); cvt(2);
#pragma unroll 1
      for (int p = 0; p < 24; p += 4) {
        step(0); step(1); step(2); step(3);
        tp += 1024;
      }
      lds_char* RED = smem + m * 16384 + lane * 16;
      const int n32 = nt * 4 + kl * 2;
      const unsigned oo0 = (((n32 / 3) * 40 + (mt * 2 + m) * 2) * 3 + n32 % 3) * 1024;
      const unsigned oo1 = ((((n32 + 1) / 3) * 40 + (mt * 2 + m) * 2) * 3 + (n32 + 1) % 3) * 1024;
      SB();
#pragma unroll
      for (int u = 0; u < 8; ++u) {
        const int c = (u < 4 ? 2 : 0) + ((u >> 1) & 1), s2 = u & 1;
        acc[c] = mfma32(af[1][s2], bf[1][s2][c], acc[c]);
        SB();
        if (u >= 3 && u < 7) {
#pragma unroll
          for (int j = 2 * (u - 3); j < 2 * (u - 3) + 2; ++j) {
            const f32x16& g = acc[2 + (j >> 2)];
            const int q = 4 * (j & 3);
            ldsw<f32x4>(RED + kl * 8192 + j * 1024, f32x4{g[q], g[q + 1], g[q + 2], g[q + 3]});
          }
          SB();
        }
      }
      asm volatile("" ::: "memory");
      __hip_atomic_store(sw + 9 + wid, 1u, RLX_WG);
      while (__builtin_amdgcn_readfirstlane(__hip_atomic_load(sw + 9 + (wid ^ 2), RLX_WG)) == 0) {}
      asm volatile("" ::: "memory");
      f32x4 pv[8];
#pragma unroll
      for (int j = 0; j < 8; ++j) pv[j] = ldsr<f32x4>(RED + (kl ^ 1) * 8192 + j * 1024);
      SB();
#pragma unroll
      for (int j = 0; j < 4; ++j) {
        f32x16& o = acc[j >> 1];
#pragma unroll
        for (int e = 0; e < 8; ++e) o[(j & 1) * 8 + e] += pv[2 * j + (e >> 2)][e & 3];
        __builtin_amdgcn_raw_buffer_store_b128(pack8(o, j & 1), wsr, lane * 16 + (j & 1) * 3072, j < 2 ? oo0 : oo1, 16);
      }
    }
  } else {
    const int mi = idx & 7, eb = idx >> 3;
    const int xmb = (xcd - 6) * 8 + mi;
    const int ea = eb & 1;
    myflag = FX + xmb * 4 + eb;
    if (wid >= 4) {
      const int w4 = wid - 4;
      unsigned xg[4], ug[2];
#pragma unroll
      for (int i = 0; i < 4; ++i) {
        int row = i * 8 + (lane >> 3);
        xg[i] = row * (D_ * 2) + (((lane & 7) ^ ((row >> 1) & 7)) * 16) - i * 1024;
      }
#pragma unroll
      for (int i = 0; i < 2; ++i) ug[i] = (i * 8 + (lane >> 3)) * (D_ * 2) + (((lane & 7) ^ (((lane >> 4) & 1) << 2)) * 16) - i * 1024;
      const unsigned cg = (lane >> 2) * (D_ * 2) + (lane & 3) * 16;
      const char* xb = (const char*)x + (size_t)(xmb * 128 + w4 * 32) * (D_ * 2);
      const char* ub = (const char*)U + eb * 320 + ea * 64 + (size_t)(w4 * 16) * (D_ * 2);
      int ks = mi + ((eb * 5 + 1) >> 1);
      if (ks >= 10) ks -= 10;
      auto issue = [&](auto sc) {
        constexpr int st = decltype(sc)::value;
        const unsigned base = lb + (st % 5) * 24576;
        if constexpr (st < 10) {
          const char* xp = xb + ks * 128;
          const char* up = ub + (size_t)ks * 64 * (D_ * 2);
          set_m0(base + w4 * 4096);
          glds<0>(xp, xg[0]); glds<1024>(xp, xg[1]); glds<2048>(xp, xg[2]); glds<3072>(xp, xg[3]);
          set_m0(base + 16384 + w4 * 2048);
          glds<0>(up, ug[0]); glds<1024>(up, ug[1]);
          if (++ks == 10) ks = 0;
        } else if constexpr (st < 15) {
#pragma unroll
          for (int j = 0; j < 2; ++j) {
            const char* up = ub + 128 + (size_t)ks * 64 * (D_ * 2);
            set_m0(base + j * 8192 + w4 * 2048);
            glds<0>(up, ug[0]); glds<1024>(up, ug[1]);
            if (++ks == 10) ks = 0;
          }
        } else if constexpr (st < 17) {
#pragma unroll
          for (int j = 0; j < 5; ++j) {
            set_m0(base + j * 4096 + w4 * 1024);
            glds<0>(ub + (ea ? -64 : 256) + (size_t)ks * 64 * (D_ * 2), cg);
            if (++ks == 10) ks = 0;
          }
        }
      };
      issue(std::integral_constant<int, 0>{});
      issue(std::integral_constant<int, 1>{});
      static_for<17>([&](auto sc) {
        constexpr int st = decltype(sc)::value;
        asm volatile("s_waitcnt vmcnt(%0)" ::"i"(x_nld(st + 1)) : "memory");
        BAR();
        issue(std::integral_constant<int, st + 2>{});
      });
      if (wid == 4) { ld_first(); wait_first(); }
    } else {
      lds_char *xp[2][4], *up[2][2];
#pragma unroll
      for (int s = 0; s < 4; ++s) {
        xp[0][s] = smem + (wid * 32 + r) * 128 + (((2 * s + h) ^ ((r >> 1) & 7)) * 16);
        xp[1][s] = xp[0][s] + 61440;
        asm volatile("" : "+v"(xp[0][s]), "+v"(xp[1][s]));
      }
#pragma unroll
      for (int t = 0; t < 2; ++t) {
        up[0][t] = smem + (8 * h + q4) * 128 + ((t ^ ((q4 >> 1) & 1)) * 64) + blk * 32 + p4 * 8;
        up[1][t] = up[0][t] + 61440;
        asm volatile("" : "+v"(up[0][t]), "+v"(up[1][t]));
      }
      lds_char* cp = smem + (8 * h + q4) * 64 + blk * 32 + p4 * 8;
      const unsigned xo = XRF_OFF + ((xmb * 4 + wid) * 40 + eb * 10) * 1024;
      f32x16 acc[2] = {};
      u32x4 xf[40], uf[8];
      auto rd = [&](auto ic) {
        constexpr int i = decltype(ic)::value;
        if constexpr (i < 200) {
          if constexpr (i == 0 || x_stage(i) != x_stage(i - 1)) BAR();
          constexpr int so = (x_stage(i) % 5) * 24576, uo = so + x_uoff(i);
          if constexpr (i < 80 && (i & 1) == 0) xf[i / 2] = ldsr<u32x4>(xp[so / 61440][(i / 2) & 3] + so % 61440);
          if constexpr (i < 160) uf[i & 7] = trread2(up[uo / 61440][i & 1] + uo % 61440, up[uo / 61440][i & 1] + uo % 61440 + 512);
          else uf[i & 7] = trread2(cp + uo, cp + uo + 256);
        }
      };
      u32x4 pk[4];
      WAIT_L(0);
      static_for<6>(rd);
      static_for<200>([&](auto ic) {
        constexpr int i = decltype(ic)::value;
        constexpr int a = i < 160 ? (i & 1) : 0;
        if constexpr (i == 80 || i == 160) {
#pragma unroll
          for (int j = 0; j < 4; ++j) pk[j] = pack8(acc[j >> 1], j & 1);
          acc[0] = f32x16{}; acc[1] = f32x16{};
          SB();
        }
        acc[a] = mfma32(uf[i & 7], xf[x_ks(i)], acc[a]);
        SB();
        rd(std::integral_constant<int, i + 6>{});
        if constexpr (i >= 80 && i % 80 < 8 && (i & 1) == 0)
          __builtin_amdgcn_raw_buffer_store_b128(pk[(i % 80) / 2], wsr, lane * 16 + ((i % 80) / 2) * 1024, xo + ea * 2048 + (i / 160) * 4096, 16);
        SB();
      });
      __builtin_amdgcn_raw_buffer_store_b128(pack8(acc[0], 0), wsr, lane * 16, xo + (ea ? 0 : 8192), 16);
      __builtin_amdgcn_raw_buffer_store_b128(pack8(acc[0], 1), wsr, lane * 16 + 1024, xo + (ea ? 0 : 8192), 16);
    }
  }

  const unsigned vo = lane * 16;
  if (wid < 4) {
    if (!is_pf && lane == 0) __hip_atomic_fetch_add(sw + 8, 1u, RLX_WG);
    const int n0 = (nb * 3) >> 2, n1 = (nb * 3 + 2) >> 2;
    gu64* tfl = sync + (FT + n0 * 10 + min(lane, (n1 - n0) * 10 + 9)) * FS;
    lds_spin(sw + 2, 1);
    const u64 tok = (u64)sw[0] | ((u64)sw[1] << 32);
    if (!is_pf) {
      asm volatile("s_waitcnt vmcnt(0) lgkmcnt(0)" ::: "memory");
      if (lane == 0) {
        if (__hip_atomic_fetch_add(sw + 3, 1u, RLX_WG) == 3) __hip_atomic_store(sync + myflag * FS, tok, RLX_AGENT);
      }
    }
    if (wid == 0) {
      lds_spin(sw + 8, 4);
      poll_flags(tfl, tok);
      if (lane == 0) __hip_atomic_store(sw + 5, 1u, RLX_WG);
    } else {
      lds_spin(sw + 5, 1);
    }
    const char* tsrc = ws + nb * 122880 + wid * 3072;
    unsigned tdst = lb + wid * 3072;
    auto issue = [&]() {
      asm volatile("s_mov_b32 m0, %2\n\ts_nop 0\n\tglobal_load_lds_dwordx4 %0, %1 sc1\n\tglobal_load_lds_dwordx4 %0, %1 offset:1024 sc1\n\t"
                   "global_load_lds_dwordx4 %0, %1 offset:2048 sc1" ::"v"(vo), "s"(tsrc), "s"(tdst) : "memory");
      tsrc += 12288; tdst += 12288;
    };
    issue(); issue(); issue(); issue();
#pragma unroll 1
    for (int ch = 0; ch < 6; ++ch) {
      WAIT_V(9);
      BAR();
      issue();
    }
    WAIT_V(9); BAR(); WAIT_V(6); BAR(); WAIT_V(3); BAR(); WAIT_V(0); BAR();
    if (blockIdx.x == 1 && wid == 2) {
#pragma unroll 1
      for (unsigned spins = 0; spins < (1u << 20); ++spins) {
        bool ok = true;
#pragma unroll
        for (int i = 0; i < 4; ++i) ok &= __hip_atomic_load(sync + FA + i * 64 + lane, RLX_AGENT) == tok;
        if (__all(ok)) break;
        __builtin_amdgcn_s_sleep(POLL_SLEEP);
      }
      if (lane == 0) __hip_atomic_store(sync, tok, RLX_AGENT);
    }
  } else {
    const int w4 = wid - 4;
    if (wid == 4) {
      const u64 tok = (((u64)__builtin_amdgcn_readlane((unsigned)(first >> 32), 4) << 32) | (unsigned)__builtin_amdgcn_readlane((unsigned)first, 4)) +
                      0x9E3779B97F4A7C15ull;
      if (lane == 0) {
        sw[0] = (unsigned)tok; sw[1] = (unsigned)(tok >> 32);
        asm volatile("" ::: "memory");
        __hip_atomic_store(sw + 2, 1u, RLX_WG);
        __hip_atomic_store(sync + FA + blockIdx.x, tok, RLX_AGENT);
      }
      if (!__all(lane >= 4 || first == tok)) poll_flags(sync + (FX + mb * 4 + (lane & 3)) * FS, tok);
      if (lane == 0) __hip_atomic_store(sw + 4, 1u, RLX_WG);
    } else {
      lds_spin(sw + 4, 1);
    }
    lds_spin(sw + 3, 4);
    u32x4 xf[40];
    const unsigned xo = XRF_OFF + (mb * 4 + w4) * 40960;
#pragma unroll
    for (int ch = 0; ch < 10; ++ch) {
#pragma unroll
      for (int i = 0; i < 4; ++i) xf[ch * 4 + i] = __builtin_amdgcn_raw_buffer_load_b128(wsr, lane * 16 + i * 1024, xo + ch * 4096, 16);
    }
    SB();
    f32x16 acc[3] = {};
    u32x4 tf[3][3];
    lds_char* tb = smem + lane * 16;
    lds_char* tb2 = tb + 61440;
    asm volatile("" : "+v"(tb), "+v"(tb2));
    BAR();
#pragma unroll
    for (int c = 0; c < 6; ++c) tf[c / 3][c % 3] = ldsr<u32x4>(tb + c * 1024);
#pragma unroll
    for (int ks = 0; ks < 40; ++ks) {
#pragma unroll
      for (int c = 0; c < 3; ++c) {
        acc[c] = mfma32(tf[ks % 3][c], xf[ks], acc[c]);
        SB();
        if (ks < 38) {
          if (c == 0 && (ks & 3) == 2) BAR();
          tf[(ks + 2) % 3][c] = ldsr<u32x4>((ks + 2 < 20 ? tb : tb2) + (((ks + 2) % 20) * 3 + c) * 1024);
        }
        SB();
      }
    }
    lds_char* eb = smem + 122880 + w4 * 6656;
#pragma unroll
    for (int c = 0; c < 3; ++c) {
      ldsw<u32x4>(eb + r * 208 + c * 64 + h * 32, pack8(acc[c], 0));
      ldsw<u32x4>(eb + r * 208 + c * 64 + h * 32 + 16, pack8(acc[c], 1));
    }
    const __amdgpu_buffer_rsrc_t yr = __builtin_amdgcn_make_buffer_rsrc((void*)y, 0, 2048 * F_ * 2, 0x00020000);
    const unsigned yo = ((mb * 128 + w4 * 32) * F_ + nb * 96) * 2;
#pragma unroll
    for (int i = 0; i < 6; ++i) {
      int q = i * 64 + lane, row = q / 12, chn = q % 12;
      __builtin_amdgcn_raw_buffer_store_b128(ldsr<u32x4>(eb + row * 208 + chn * 16), yr, row * (F_ * 2) + chn * 16, yo, 2  );
    }
  }
}

extern "C" void kernel_launch(void* const* d_in, const int* in_sizes, int n_in, void* d_out, int out_size, void* d_ws,
                              size_t ws_size, hipStream_t stream) {
  (void)hipFuncSetAttribute((const void*)quip, hipFuncAttributeMaxDynamicSharedMemorySize, LDS_BYTES);
  quip<<<256, 512, LDS_BYTES, stream>>>((const __hip_bfloat16*)d_in[0], (const int*)d_in[1], (const __hip_bfloat16*)d_in[2],
                                        (const __hip_bfloat16*)d_in[3], (const __hip_bfloat16*)d_in[4], (const __hip_bfloat16*)d_in[5],
                                        (char*)d_ws, (__hip_bfloat16*)d_out);
}
```

```cpp
#define AMP 0
#define AMP_BLK 125
#define AMPF 3
#include <hip/hip_runtime.h>
#include <hip/hip_bf16.h>
#include <utility>

typedef __attribute__((ext_vector_type(8))) __bf16 bf16x8;
typedef __attribute__((ext_vector_type(2))) __bf16 bf16x2;
typedef __attribute__((ext_vector_type(4))) short s16x4;
typedef __attribute__((ext_vector_type(16))) float f32x16;
typedef __attribute__((ext_vector_type(4))) float f32x4;
typedef __attribute__((ext_vector_type(4))) unsigned u32x4;
typedef __attribute__((ext_vector_type(2))) unsigned u32x2;
typedef __attribute__((address_space(3))) s16x4 lds_s16x4;
typedef __attribute__((address_space(3))) char lds_char;
typedef __attribute__((address_space(3))) unsigned lds_u32;
typedef __attribute__((address_space(1))) unsigned long long gu64;
typedef unsigned long long u64;

#define STAMP(i) do { if ((threadIdx.x & 63) == 0) sw[16 + (i)] = (unsigned)__builtin_amdgcn_s_memrealtime(); } while (0)
#define Y_AUX 2
#define XCD_PERM 0x54321706
#define YPART 1
#define XRV 0
#define POLL_SLEEP 4
constexpr int XPRE = 24, XOFF = 15;
constexpr int D_ = 640, F_ = 1536;

#define WAIT_V(n) asm volatile("s_waitcnt vmcnt(" #n ")" ::: "memory")
#define WAIT_L(n) asm volatile("s_waitcnt lgkmcnt(" #n ")" ::: "memory")
#define BAR() do { asm volatile("" ::: "memory"); __builtin_amdgcn_s_barrier(); asm volatile("" ::: "memory"); } while (0)
#define SB() __builtin_amdgcn_sched_barrier(0)
#define RLX_AGENT __ATOMIC_RELAXED, __HIP_MEMORY_SCOPE_AGENT
#define RLX_WG __ATOMIC_RELAXED, __HIP_MEMORY_SCOPE_WORKGROUP

__device__ __forceinline__ void set_m0(unsigned ldsaddr) { asm volatile("s_mov_b32 m0, %0\n\ts_nop 0" ::"s"(ldsaddr) : "memory"); }
template <int OFF> __device__ __forceinline__ void glds(const void* sbase, unsigned voff) {
  asm volatile("global_load_lds_dwordx4 %0, %1 offset:%2" ::"v"(voff), "s"(sbase), "i"(OFF) : "memory");
}
__device__ __forceinline__ void touch(const void* sbase, unsigned voff) {
  asm volatile("global_load_lds_dword %0, %1" ::"v"(voff), "s"(sbase) : "memory");
}
__device__ __forceinline__ unsigned pack2(float a, float b) {
  bf16x2 p; p[0] = (__bf16)a; p[1] = (__bf16)b;
  return __builtin_bit_cast(unsigned, p);
}
__device__ __forceinline__ float bflo(unsigned u) { return __builtin_bit_cast(float, u << 16); }
__device__ __forceinline__ float bfhi(unsigned u) { return __builtin_bit_cast(float, u & 0xffff0000u); }
__device__ __forceinline__ u32x4 trread2(lds_char* p0, lds_char* p1) {
  u32x2 lo = __builtin_bit_cast(u32x2, __builtin_amdgcn_ds_read_tr16_b64_v4i16((lds_s16x4*)p0));
  u32x2 hi = __builtin_bit_cast(u32x2, __builtin_amdgcn_ds_read_tr16_b64_v4i16((lds_s16x4*)p1));
  return u32x4{lo[0], lo[1], hi[0], hi[1]};
}
template <typename T> __device__ __forceinline__ T ldsr(lds_char* p) { return *(__attribute__((address_space(3))) T*)p; }
template <typename T> __device__ __forceinline__ void ldsw(lds_char* p, T v) { *(__attribute__((address_space(3))) T*)p = v; }
__device__ __forceinline__ f32x16 mfma32(u32x4 a, u32x4 b, f32x16 c) {
  return __builtin_amdgcn_mfma_f32_32x32x16_bf16(__builtin_bit_cast(bf16x8, a), __builtin_bit_cast(bf16x8, b), c, 0, 0, 0);
}
__device__ __forceinline__ u32x4 pack8(const f32x16& a, int s) {
  u32x4 r;
  r[0] = pack2(a[8 * s + 0], a[8 * s + 1]); r[1] = pack2(a[8 * s + 2], a[8 * s + 3]);
  r[2] = pack2(a[8 * s + 4], a[8 * s + 5]); r[3] = pack2(a[8 * s + 6], a[8 * s + 7]);
  return r;
}
__device__ __forceinline__ u32x2 deq4(u32x4 q, f32x4 s, f32x4 z) {
  float w0, w1, w2, w3;
  asm("v_cvt_f32_i32 %0, %4\n\tv_cvt_f32_i32 %1, %5\n\tv_cvt_f32_i32 %2, %6\n\tv_cvt_f32_i32 %3, %7\n\t"
      "v_fma_f32 %0, %0, %8, %12\n\tv_fma_f32 %1, %1, %9, %13\n\tv_fma_f32 %2, %2, %10, %14\n\tv_fma_f32 %3, %3, %11, %15\n\t"
      "v_cvt_pk_bf16_f32 %0, %0, %1\n\tv_cvt_pk_bf16_f32 %1, %2, %3"
      : "=&v"(w0), "=&v"(w1), "=&v"(w2), "=&v"(w3)
      : "v"(q[0]), "v"(q[1]), "v"(q[2]), "v"(q[3]), "v"(s[0]), "v"(s[1]), "v"(s[2]), "v"(s[3]), "v"(z[0]), "v"(z[1]), "v"(z[2]), "v"(z[3]));
  return u32x2{__builtin_bit_cast(unsigned, w0), __builtin_bit_cast(unsigned, w1)};
}
__device__ __forceinline__ void lds_spin(lds_u32* w, unsigned want) {
  while ((unsigned)__builtin_amdgcn_readfirstlane(__hip_atomic_load(w, RLX_WG)) < want) __builtin_amdgcn_s_sleep(1);
  asm volatile("" ::: "memory");
}
__device__ __forceinline__ void poll_flags(gu64* f, u64 tok) {
#pragma unroll 1
  for (unsigned spins = 0; spins < (1u << 20); ++spins) {
    if (__all(__hip_atomic_load(f, RLX_AGENT) == tok)) break;
    __builtin_amdgcn_s_sleep(POLL_SLEEP);
  }
  __builtin_amdgcn_fence(__ATOMIC_ACQUIRE, "wavefront");
}

template <int N, typename Fn, int... I>
__device__ __forceinline__ void static_for_impl(Fn&& f, std::integer_sequence<int, I...>) { (f(std::integral_constant<int, I>{}), ...); }
template <int N, typename Fn>
__device__ __forceinline__ void static_for(Fn&& f) { static_for_impl<N>(f, std::make_integer_sequence<int, N>{}); }
constexpr int x_ks(int i) { return i < 160 ? (i % 80) / 2 : i - 160; }
constexpr int x_stage(int i) { return i < 80 ? i / 8 : i < 160 ? 10 + (i - 80) / 16 : 15 + (i - 160) / 20; }
constexpr int x_uoff(int i) { return i < 80 ? 16384 + (x_ks(i) & 3) * 2048 : i < 160 ? (x_ks(i) & 7) * 2048 : (x_ks(i) % 20) * 1024; }
constexpr int x_nld(int st) { return st < 10 ? 6 : st < 15 ? 4 : st < 17 ? 5 : 0; }

constexpr int XRF_OFF = 2 << 20, SYNC_OFF = 6 << 20;
#define FS 16
constexpr int FT = 1, FX = 121;
constexpr int FA = 185 * FS;
constexpr unsigned CODE_TOUCH_MAX = 20736;
constexpr int L_SYNC = 156160, L_DUMP = 156416, LDS_BYTES = 156672;

__global__ __launch_bounds__(512, 2) void quip(const __hip_bfloat16* __restrict__ x, const int* __restrict__ qw,
                                               const __hip_bfloat16* __restrict__ scales, const __hip_bfloat16* __restrict__ zeros,
                                               const __hip_bfloat16* __restrict__ U, const __hip_bfloat16* __restrict__ V,
                                               char* ws, __hip_bfloat16* __restrict__ y) {
  extern __shared__ __attribute__((aligned(16))) char smem_[];
  lds_char* smem = (lds_char*)smem_;
  const unsigned lb = (unsigned)(size_t)smem;
  const int wid = __builtin_amdgcn_readfirstlane(threadIdx.x >> 6), lane = threadIdx.x & 63;
  const int xcd = (XCD_PERM >> ((blockIdx.x & 7) * 4)) & 7, idx = blockIdx.x >> 3;
  {
    u64 pc;
    asm volatile("s_mov_b32 m0, %3\n\ts_cmp_gt_u32 %2, 7\n\ts_cbranch_scc1 1f\n\ts_getpc_b64 %0\n\ts_nop 0\n\tglobal_load_lds_dword %1, %0\n1:"
                 : "=&s"(pc) : "v"(min(threadIdx.x * 128, CODE_TOUCH_MAX)), "s"(blockIdx.x), "s"(lb + L_DUMP) : "memory", "scc");
    SB();
  }
  const int h = lane >> 5, r = lane & 31;
  const int blk = (lane >> 4) & 1, q4 = (lane & 15) >> 2, p4 = lane & 3;
  lds_u32* sw = (lds_u32*)(smem + L_SYNC);
  gu64* sync = (gu64*)(ws + SYNC_OFF);
  const __amdgpu_buffer_rsrc_t wsr = __builtin_amdgcn_make_buffer_rsrc(ws, 0, SYNC_OFF, 0x00020000);
  const bool is_pf = idx < 12 && xcd < 6;
  if (wid == 0) STAMP(0);

  const int mb = YPART == 0 ? (xcd >> 1) * 4 + (idx & 3) : (xcd >> 2) * 8 + (idx & 7);
  const int nb = YPART == 0 ? (xcd & 1) * 8 + (idx >> 2) : (xcd & 3) * 4 + (idx >> 3);
  const unsigned fo = lane < 4 ? (FX + mb * 4 + lane) * FS * 8 : 0;
  u64 first = 0;
  auto ld_first = [&]() { asm volatile("global_load_dwordx2 %0, %1, %2 sc1" : "=v"(first) : "v"(fo), "s"(sync) : "memory"); };
  auto wait_first = [&]() { asm volatile("s_waitcnt vmcnt(0)" : "+v"(first)::"memory"); };
  if (wid == 3 && lane < 16) sw[lane] = ((lane == 8 || lane == 3) && is_pf) ? 4u : 0u;
  int myflag = 0;

  if (is_pf) {
    const int ga = xcd & 1, gb = xcd >> 1;
    if (wid < 2) {
      const bool isq = idx < 3 || (idx >= 7 && idx < 10), far = idx < 7;
      const unsigned vo_ = isq ? (lane >> 1) * (F_ * 4) + (lane & 1) * 128 : (lane >> 3) * (F_ * 2) + (lane & 7) * 128;
      const char* gbase = isq ? (const char*)qw + (size_t)(ga * 320) * (F_ * 4) : (const char*)V + gb * 1024;
      const int nc = isq ? 3 : (far ? 4 : 2), c0 = idx - (idx < 3 ? 0 : idx < 7 ? 3 : idx < 10 ? 7 : 10);
      const int ne = isq ? (far ? 80 : 160) : 96;
      BAR();
#pragma unroll 1
      for (int e = c0 + nc * wid; e < ne; e += nc * 2) {
        if (isq) {
          int t = far ? e * 3 + gb : (e >> 1) * 3 + (gb + 1 + (e & 1)) % 3;
          const int pp = t / 10, j = t - pp * 10;
          touch(gbase + j * (32 * F_ * 4) + pp * 256, vo_);
        } else {
          touch(gbase + (size_t)(e * 2 + (far ? ga : ga ^ 1)) * (8 * F_ * 2), vo_);
        }
      }
      if (wid == 0) STAMP(7);
    } else {
      WAIT_L(0);
      BAR();
      if (wid == 4) { ld_first(); wait_first(); }
    }
  } else if (xcd < 6) {
    const int t = idx - 12;
    const int mt = (xcd & 1) * 5 + t % 5, nt = (xcd >> 1) * 4 + t / 5;
    myflag = FT + nt * 10 + mt;
    if (wid >= 4) {
      const int w4 = wid - 4, rr = lane >> 4, c16 = lane & 15;
      unsigned ag[4], bg[4];
#pragma unroll
      for (int i = 0; i < 4; ++i) {
        int row = i * 4 + rr;
        ag[i] = row * (F_ * 4) + ((c16 ^ row) * 16) - i * 1024;
        bg[i] = row * (F_ * 2) + ((c16 ^ (rr << 2)) * 16) - i * 1024;
      }
      const char* ap = (const char*)qw + (size_t)(mt * 64 + w4 * 16) * (F_ * 4);
      const char* bp = (const char*)V + nt * 256 + (size_t)(w4 * 16) * (F_ * 2);
      unsigned so = lb + w4 * 4096;
      auto issue = [&](int p) {
        unsigned base = so + (p & 3) * 32768;
        set_m0(base);
        glds<0>(ap, ag[0]); glds<1024>(ap, ag[1]); glds<2048>(ap, ag[2]); glds<3072>(ap, ag[3]);
        set_m0(base + 16384);
        glds<0>(bp, bg[0]); glds<1024>(bp, bg[1]); glds<2048>(bp, bg[2]); glds<3072>(bp, bg[3]);
        ap += 256; bp += 64 * F_ * 2;
      };
      issue(0);
      issue(1);
#pragma unroll 1
      for (int p = 2; p < 24; ++p) {
        WAIT_V(8);
        BAR();
        issue(p);
      }
      if (wid == 4) {
        ld_first();
        WAIT_V(9); BAR(); WAIT_V(1); BAR();
        wait_first();
      } else {
        WAIT_V(8); BAR(); WAIT_V(0); BAR();
      }
    } else {
      const int m = wid & 1, kl = wid >> 1;
      lds_char* TB = smem + 131072 + wid * 6144;
      u32x2 sr[3], zr[3];
      {
        const unsigned o = ((mt * 2 + m) * F_ + (lane >> 3) * 64 + kl * 32 + (lane & 7) * 4) * 2;
#pragma unroll
        for (int j = 0; j < 3; ++j) {
          sr[j] = *(const u32x2*)((const char*)scales + o + j * 1024);
          zr[j] = *(const u32x2*)((const char*)zeros + o + j * 1024);
        }
      }
      auto cvt = [&](int j) {
        f32x4 sa, za;
        sa[0] = bflo(sr[j][0]); sa[1] = bfhi(sr[j][0]); sa[2] = bflo(sr[j][1]); sa[3] = bfhi(sr[j][1]);
        za[0] = -bflo(zr[j][0]) * sa[0]; za[1] = -bfhi(zr[j][0]) * sa[1]; za[2] = -bflo(zr[j][1]) * sa[2]; za[3] = -bfhi(zr[j][1]) * sa[3];
        lds_char* d = TB + j * 2048 + (lane >> 3) * 256 + (lane & 7) * 16;
        ldsw<f32x4>(d, sa); ldsw<f32x4>(d + 128, za);
      };
      lds_char *ap[2][4], *bp[2][4];
#pragma unroll
      for (int c = 0; c < 4; ++c) {
        int ch = 8 * kl + 4 * (c >> 1) + 2 * h + (c & 1);
        ap[0][c] = smem + (m * 32 + r) * 256 + ((ch ^ (r & 15)) * 16);
        bp[0][c] = smem + 16384 + (32 * kl + 8 * h + q4) * 256 + ((c ^ (2 * kl) ^ q4) * 64) + 32 * (p4 & 1) + 16 * blk + 8 * (p4 >> 1);
        ap[1][c] = ap[0][c] + 65536; bp[1][c] = bp[0][c] + 65536;
      }
      lds_char* tp = TB + h * 32;
      f32x16 acc[4] = {};
      f32x4 ts[4], tz[4];
      u32x4 av[4];
      u32x4 af[2][2] = {}, bf[2][2][4] = {};
      auto rdtab = [&](int o, int c) {
        ts[c] = ldsr<f32x4>(tp + o + (c >> 1) * 64 + (c & 1) * 16);
        tz[c] = ldsr<f32x4>(tp + o + 128 + (c >> 1) * 64 + (c & 1) * 16);
      };
      auto step = [&](int slot) {
        const int buf = slot & 1;
        BAR();
#pragma unroll
        for (int c = 0; c < 4; ++c) av[c] = ldsr<u32x4>(ap[slot >> 1][c] + buf * 32768);
        SB();
#pragma unroll
        for (int u = 0; u < 8; ++u) {
          acc[u & 3] = mfma32(af[buf ^ 1][u >> 2], bf[buf ^ 1][u >> 2][u & 3], acc[u & 3]);
          SB();
          if (u < 4) {
#pragma unroll
            for (int v = 2 * u; v < 2 * u + 2; ++v)
              bf[buf][v >> 2][v & 3] = trread2(bp[slot >> 1][v & 3] + buf * 32768 + (v >> 2) * 4096, bp[slot >> 1][v & 3] + buf * 32768 + (v >> 2) * 4096 + 1024);
          } else {
            const int c = u - 4;
            u32x2 o = deq4(av[c], ts[c], tz[c]);
            af[buf][c >> 1][(c & 1) * 2] = o[0]; af[buf][c >> 1][(c & 1) * 2 + 1] = o[1];
            SB();
            rdtab((slot + 1) * 256, c);
          }
          SB();
        }
      };
#pragma unroll
      for (int c = 0; c < 4; ++c)
        asm volatile("" : "+v"(acc[c]), "+v"(bf[1][0][c]), "+v"(bf[1][1][c]), "+v"(ap[0][c]), "+v"(bp[0][c]), "+v"(ap[1][c]), "+v"(bp[1][c]));
      asm volatile("" : "+v"(af[1][0]), "+v"(af[1][1]));
      SB();
      cvt(0);
#pragma unroll
      for (int c = 0; c < 4; ++c) rdtab(0, c);
      cvt(1); cvt(2);
      if (wid == 0) STAMP(9);
#pragma unroll 1
      for (int p = 0; p < 24; p += 4) {
        step(0); step(1); step(2); step(3);
        tp += 1024;
      }
      if (wid == 0) STAMP(1);
      lds_char* RED = smem + m * 16384 + lane * 16;
      const int n32 = nt * 4 + kl * 2;
      const unsigned oo0 = (((n32 / 3) * 40 + (mt * 2 + m) * 2) * 3 + n32 % 3) * 1024;
      const unsigned oo1 = ((((n32 + 1) / 3) * 40 + (mt * 2 + m) * 2) * 3 + (n32 + 1) % 3) * 1024;
      SB();
#pragma unroll
      for (int u = 0; u < 8; ++u) {
        const int c = (u < 4 ? 2 : 0) + ((u >> 1) & 1), s2 = u & 1;
        acc[c] = mfma32(af[1][s2], bf[1][s2][c], acc[c]);
        SB();
        if (u >= 3 && u < 7) {
#pragma unroll
          for (int j = 2 * (u - 3); j < 2 * (u - 3) + 2; ++j) {
            const f32x16& g = acc[2 + (j >> 2)];
            const int q = 4 * (j & 3);
            ldsw<f32x4>(RED + kl * 8192 + j * 1024, f32x4{g[q], g[q + 1], g[q + 2], g[q + 3]});
          }
          SB();
        }
      }
      asm volatile("" ::: "memory");
      __hip_atomic_store(sw + 9 + wid, 1u, RLX_WG);
      while (__builtin_amdgcn_readfirstlane(__hip_atomic_load(sw + 9 + (wid ^ 2), RLX_WG)) == 0) {}
      asm volatile("" ::: "memory");
      f32x4 pv[8];
#pragma unroll
      for (int j = 0; j < 8; ++j) pv[j] = ldsr<f32x4>(RED + (kl ^ 1) * 8192 + j * 1024);
      SB();
#pragma unroll
      for (int j = 0; j < 4; ++j) {
        f32x16& o = acc[j >> 1];
#pragma unroll
        for (int e = 0; e < 8; ++e) o[(j & 1) * 8 + e] += pv[2 * j + (e >> 2)][e & 3];
        __builtin_amdgcn_raw_buffer_store_b128(pack8(o, j & 1), wsr, lane * 16 + (j & 1) * 3072, j < 2 ? oo0 : oo1, 16);
      }
    }
  } else {
    const int mi = idx & 7, eb = idx >> 3;
    const int xmb = (xcd - 6) * 8 + mi;
    const int ea = eb & 1;
    myflag = FX + xmb * 4 + eb;
    if (wid >= 4) {
      const int w4 = wid - 4;
      unsigned xg[4], ug[2];
#pragma unroll
      for (int i = 0; i < 4; ++i) {
        int row = i * 8 + (lane >> 3);
        xg[i] = row * (D_ * 2) + (((lane & 7) ^ ((row >> 1) & 7)) * 16) - i * 1024;
      }
#pragma unroll
      for (int i = 0; i < 2; ++i) ug[i] = (i * 8 + (lane >> 3)) * (D_ * 2) + (((lane & 7) ^ (((lane >> 4) & 1) << 2)) * 16) - i * 1024;
      const unsigned cg = (lane >> 2) * (D_ * 2) + (lane & 3) * 16;
      const char* xb = (const char*)x + (size_t)(xmb * 128 + w4 * 32) * (D_ * 2);
      const char* ub = (const char*)U + eb * 320 + ea * 64 + (size_t)(w4 * 16) * (D_ * 2);
      int ks = mi + ((eb * 5 + 1) >> 1);
      if (ks >= 10) ks -= 10;
      auto issue = [&](auto sc) {
        constexpr int st = decltype(sc)::value;
        const unsigned base = lb + (st % 5) * 24576;
        if constexpr (st < 10) {
          const char* xp = xb + ks * 128;
          const char* up = ub + (size_t)ks * 64 * (D_ * 2);
          set_m0(base + w4 * 4096);
          glds<0>(xp, xg[0]); glds<1024>(xp, xg[1]); glds<2048>(xp, xg[2]); glds<3072>(xp, xg[3]);
          set_m0(base + 16384 + w4 * 2048);
          glds<0>(up, ug[0]); glds<1024>(up, ug[1]);
          if (++ks == 10) ks = 0;
        } else if constexpr (st < 15) {
#pragma unroll
          for (int j = 0; j < 2; ++j) {
            const char* up = ub + 128 + (size_t)ks * 64 * (D_ * 2);
            set_m0(base + j * 8192 + w4 * 2048);
            glds<0>(up, ug[0]); glds<1024>(up, ug[1]);
            if (++ks == 10) ks = 0;
          }
        } else if constexpr (st < 17) {
#pragma unroll
          for (int j = 0; j < 5; ++j) {
            set_m0(base + j * 4096 + w4 * 1024);
            glds<0>(ub + (ea ? -64 : 256) + (size_t)ks * 64 * (D_ * 2), cg);
            if (++ks == 10) ks = 0;
          }
        }
      };
      issue(std::integral_constant<int, 0>{});
      issue(std::integral_constant<int, 1>{});
      static_for<17>([&](auto sc) {
        constexpr int st = decltype(sc)::value;
        asm volatile("s_waitcnt vmcnt(%0)" ::"i"(x_nld(st + 1)) : "memory");
        BAR();
        issue(std::integral_constant<int, st + 2>{});
      });
      if (wid == 4) { ld_first(); wait_first(); }
    } else {
      lds_char *xp[2][4], *up[2][2];
#pragma unroll
      for (int s = 0; s < 4; ++s) {
        xp[0][s] = smem + (wid * 32 + r) * 128 + (((2 * s + h) ^ ((r >> 1) & 7)) * 16);
        xp[1][s] = xp[0][s] + 61440;
        asm volatile("" : "+v"(xp[0][s]), "+v"(xp[1][s]));
      }
#pragma unroll
      for (int t = 0; t < 2; ++t) {
        up[0][t] = smem + (8 * h + q4) * 128 + ((t ^ ((q4 >> 1) & 1)) * 64) + blk * 32 + p4 * 8;
        up[1][t] = up[0][t] + 61440;
        asm volatile("" : "+v"(up[0][t]), "+v"(up[1][t]));
      }
      lds_char* cp = smem + (8 * h + q4) * 64 + blk * 32 + p4 * 8;
      const unsigned xo = XRF_OFF + ((xmb * 4 + wid) * 40 + eb * 10) * 1024;
      f32x16 acc[2] = {};
      u32x4 xf[40], uf[8];
      auto rd = [&](auto ic) {
        constexpr int i = decltype(ic)::value;
        if constexpr (i < 200) {
          if constexpr (i == 0 || x_stage(i) != x_stage(i - 1)) BAR();
          constexpr int so = (x_stage(i) % 5) * 24576, uo = so + x_uoff(i);
          if constexpr (i < 80 && (i & 1) == 0) xf[i / 2] = ldsr<u32x4>(xp[so / 61440][(i / 2) & 3] + so % 61440);
          if constexpr (i < 160) uf[i & 7] = trread2(up[uo / 61440][i & 1] + uo % 61440, up[uo / 61440][i & 1] + uo % 61440 + 512);
          else uf[i & 7] = trread2(cp + uo, cp + uo + 256);
        }
      };
      u32x4 pk[4];
      if (idx * 2 + wid < 60 && wid < 2) {
        const int t = idx * 2 + wid, rb = t / 6, j = t - rb * 6;
        touch((const char*)U + (size_t)rb * 64 * (D_ * 2) + ((0x976421 >> (4 * j)) & 15) * 128, lane * (D_ * 2));
      }
      WAIT_L(0);
      static_for<6>(rd);
      static_for<200>([&](auto ic) {
        constexpr int i = decltype(ic)::value;
        constexpr int a = i < 160 ? (i & 1) : 0;
        if constexpr (i == 80 || i == 160) {
#pragma unroll
          for (int j = 0; j < 4; ++j) pk[j] = pack8(acc[j >> 1], j & 1);
          acc[0] = f32x16{}; acc[1] = f32x16{};
          SB();
        }
        acc[a] = mfma32(uf[i & 7], xf[x_ks(i)], acc[a]);
        SB();
        rd(std::integral_constant<int, i + 6>{});
        if constexpr (i >= 80 && i % 80 < 8 && (i & 1) == 0)
          __builtin_amdgcn_raw_buffer_store_b128(pk[(i % 80) / 2], wsr, lane * 16 + ((i % 80) / 2) * 1024, xo + ea * 2048 + (i / 160) * 4096, 16);
        SB();
      });
      __builtin_amdgcn_raw_buffer_store_b128(pack8(acc[0], 0), wsr, lane * 16, xo + (ea ? 0 : 8192), 16);
      __builtin_amdgcn_raw_buffer_store_b128(pack8(acc[0], 1), wsr, lane * 16 + 1024, xo + (ea ? 0 : 8192), 16);
      if (wid == 0) STAMP(3);
    }
  }

  const unsigned vo = lane * 16;
  if (wid < 4) {
    if (!is_pf) {
      if (wid == 0) STAMP(10);
      if (lane == 0) __hip_atomic_fetch_add(sw + 8, 1u, RLX_WG);
    }
    const int n0 = (nb * 3) >> 2, n1 = (nb * 3 + 2) >> 2;
    gu64* tfl = sync + (FT + n0 * 10 + min(lane, (n1 - n0) * 10 + 9)) * FS;
    lds_spin(sw + 2, 1);
    const u64 tok = (u64)sw[0] | ((u64)sw[1] << 32);
    if (!is_pf) {
      asm volatile("s_waitcnt vmcnt(0) lgkmcnt(0)" ::: "memory");
      if (lane == 0) {
        if (__hip_atomic_fetch_add(sw + 3, 1u, RLX_WG) == 3) __hip_atomic_store(sync + myflag * FS, tok, RLX_AGENT);
      }
      if (wid == 0) STAMP(2);
    }
    if (wid == 0) {
      lds_spin(sw + 8, 4);
      poll_flags(tfl, tok);
      if (lane == 0) __hip_atomic_store(sw + 5, 1u, RLX_WG);
      STAMP(4);
    } else {
      lds_spin(sw + 5, 1);
    }
    const char* tsrc = ws + nb * 122880 + wid * 3072;
    unsigned tdst = lb + wid * 3072;
    auto issue = [&]() {
      asm volatile("s_mov_b32 m0, %2\n\ts_nop 0\n\tglobal_load_lds_dwordx4 %0, %1 sc1\n\tglobal_load_lds_dwordx4 %0, %1 offset:1024 sc1\n\t"
                   "global_load_lds_dwordx4 %0, %1 offset:2048 sc1" ::"v"(vo), "s"(tsrc), "s"(tdst) : "memory");
      tsrc += 12288; tdst += 12288;
    };
    issue(); issue(); issue(); issue();
#pragma unroll 1
    for (int ch = 0; ch < 6; ++ch) {
      WAIT_V(9);
      BAR();
      issue();
    }
    WAIT_V(9); BAR(); WAIT_V(6); BAR(); WAIT_V(3); BAR(); WAIT_V(0); BAR();
    if (blockIdx.x == 1 && wid == 2) {
#pragma unroll 1
      for (unsigned spins = 0; spins < (1u << 20); ++spins) {
        bool ok = true;
#pragma unroll
        for (int i = 0; i < 4; ++i) ok &= __hip_atomic_load(sync + FA + i * 64 + lane, RLX_AGENT) == tok;
        if (__all(ok)) break;
        __builtin_amdgcn_s_sleep(POLL_SLEEP);
      }
      if (lane == 0) __hip_atomic_store(sync, tok, RLX_AGENT);
    }
  } else {
    const int w4 = wid - 4;
    if (wid == 4) {
      const u64 tok = (((u64)__builtin_amdgcn_readlane((unsigned)(first >> 32), 4) << 32) | (unsigned)__builtin_amdgcn_readlane((unsigned)first, 4)) +
                      0x9E3779B97F4A7C15ull;
      if (lane == 0) {
        sw[0] = (unsigned)tok; sw[1] = (unsigned)(tok >> 32);
        asm volatile("" ::: "memory");
        __hip_atomic_store(sw + 2, 1u, RLX_WG);
        __hip_atomic_store(sync + FA + blockIdx.x, tok, RLX_AGENT);
      }
      if (!__all(lane >= 4 || first == tok)) poll_flags(sync + (FX + mb * 4 + (lane & 3)) * FS, tok);
      if (lane == 0) __hip_atomic_store(sw + 4, 1u, RLX_WG);
      STAMP(8);
    } else {
      lds_spin(sw + 4, 1);
    }
    lds_spin(sw + 3, 4);
    u32x4 xf[40];
    const unsigned xo = XRF_OFF + (mb * 4 + w4) * 40960;
#pragma unroll
    for (int ch = 0; ch < XPRE / 4; ++ch) {
#pragma unroll
      for (int i = 0; i < 4; ++i) xf[ch * 4 + i] = __builtin_amdgcn_raw_buffer_load_b128(wsr, lane * 16 + i * 1024, xo + ch * 4096, 16);
    }
    SB();
    if (wid == 4) STAMP(11);
    f32x16 acc[3] = {};
    u32x4 tf[3][3];
    lds_char* tb = smem + lane * 16;
    lds_char* tb2 = tb + 61440;
    asm volatile("" : "+v"(tb), "+v"(tb2));
    BAR();
#pragma unroll
    for (int c = 0; c < 6; ++c) tf[c / 3][c % 3] = ldsr<u32x4>(tb + c * 1024);
#pragma unroll
    for (int ks = 0; ks < 40; ++ks) {
#pragma unroll
      for (int c = 0; c < 3; ++c) {
        acc[c] = mfma32(tf[ks % 3][c], xf[ks], acc[c]);
        SB();
        if (ks < 38) {
          if (c == 0 && (ks & 3) == 2) BAR();
          tf[(ks + 2) % 3][c] = ldsr<u32x4>((ks + 2 < 20 ? tb : tb2) + (((ks + 2) % 20) * 3 + c) * 1024);
        }
        if (c == 1 && ks >= XOFF && ks - XOFF + XPRE < 40) {
          const int i = ks - XOFF + XPRE;
          xf[i] = __builtin_amdgcn_raw_buffer_load_b128(wsr, lane * 16 + (i & 3) * 1024, xo + (i >> 2) * 4096, 16);
        }
        SB();
      }
    }
    if (wid == 4) STAMP(5);
    lds_char* eb = smem + 122880 + w4 * 6656;
#pragma unroll
    for (int c = 0; c < 3; ++c) {
      ldsw<u32x4>(eb + r * 208 + c * 64 + h * 32, pack8(acc[c], 0));
      ldsw<u32x4>(eb + r * 208 + c * 64 + h * 32 + 16, pack8(acc[c], 1));
    }
    const __amdgpu_buffer_rsrc_t yr = __builtin_amdgcn_make_buffer_rsrc((void*)y, 0, 2048 * F_ * 2, 0x00020000);
    const unsigned yo = ((mb * 128 + w4 * 32) * F_ + nb * 96) * 2;
#pragma unroll
    for (int i = 0; i < 6; ++i) {
      int q = i * 64 + lane, row = q / 12, chn = q % 12;
      __builtin_amdgcn_raw_buffer_store_b128(ldsr<u32x4>(eb + row * 208 + chn * 16), yr, row * (F_ * 2) + chn * 16, yo, Y_AUX);
    }
    WAIT_V(0);
    if (wid == 4) STAMP(6);
    if (wid == 4 && blockIdx.x == AMP_BLK) {
      WAIT_L(0);
      unsigned d = __builtin_amdgcn_readfirstlane(sw[16 + AMP] - sw[16]);
      unsigned t0 = (unsigned)__builtin_amdgcn_s_memrealtime();
      while ((unsigned)__builtin_amdgcn_s_memrealtime() - t0 < AMPF * d + 300) __builtin_amdgcn_s_sleep(1);
    }
  }
}

extern "C" void kernel_launch(void* const* d_in, const int* in_sizes, int n_in, void* d_out, int out_size, void* d_ws,
                              size_t ws_size, hipStream_t stream) {
  (void)hipFuncSetAttribute((const void*)quip, hipFuncAttributeMaxDynamicSharedMemorySize, LDS_BYTES);
  quip<<<256, 512, LDS_BYTES, stream>>>((const __hip_bfloat16*)d_in[0], (const int*)d_in[1], (const __hip_bfloat16*)d_in[2],
                                        (const __hip_bfloat16*)d_in[3], (const __hip_bfloat16*)d_in[4], (const __hip_bfloat16*)d_in[5],
                                        (char*)d_ws, (__hip_bfloat16*)d_out);
}
```

```cpp
#include <hip/hip_runtime.h>
#include <hip/hip_bf16.h>
#include <utility>

typedef __attribute__((ext_vector_type(8))) __bf16 bf16x8;
typedef __attribute__((ext_vector_type(2))) __bf16 bf16x2;
typedef __attribute__((ext_vector_type(4))) short s16x4;
typedef __attribute__((ext_vector_type(16))) float f32x16;
typedef __attribute__((ext_vector_type(4))) float f32x4;
typedef __attribute__((ext_vector_type(4))) unsigned u32x4;
typedef __attribute__((ext_vector_type(2))) unsigned u32x2;
typedef __attribute__((address_space(3))) s16x4 lds_s16x4;
typedef __attribute__((address_space(3))) char lds_char;
typedef __attribute__((address_space(3))) unsigned lds_u32;
typedef __attribute__((address_space(1))) unsigned long long gu64;
typedef unsigned long long u64;

constexpr unsigned XCD_PERM = 0x54321706;
constexpr int POLL_SLEEP = 4;
constexpr int XPRE = 24, XOFF = 15;
constexpr int D_ = 640, F_ = 1536;

#define WAIT_V(n) asm volatile("s_waitcnt vmcnt(" #n ")" ::: "memory")
#define WAIT_L(n) asm volatile("s_waitcnt lgkmcnt(" #n ")" ::: "memory")
#define BAR() do { asm volatile("" ::: "memory"); __builtin_amdgcn_s_barrier(); asm volatile("" ::: "memory"); } while (0)
#define SB() __builtin_amdgcn_sched_barrier(0)
#define RLX_AGENT __ATOMIC_RELAXED, __HIP_MEMORY_SCOPE_AGENT
#define RLX_WG __ATOMIC_RELAXED, __HIP_MEMORY_SCOPE_WORKGROUP

__device__ __forceinline__ void set_m0(unsigned ldsaddr) { asm volatile("s_mov_b32 m0, %0\n\ts_nop 0" ::"s"(ldsaddr) : "memory"); }
template <int OFF> __device__ __forceinline__ void glds(const void* sbase, unsigned voff) {
  asm volatile("global_load_lds_dwordx4 %0, %1 offset:%2" ::"v"(voff), "s"(sbase), "i"(OFF) : "memory");
}
__device__ __forceinline__ void touch(const void* sbase, unsigned voff) {
  asm volatile("global_load_lds_dword %0, %1" ::"v"(voff), "s"(sbase) : "memory");
}
__device__ __forceinline__ unsigned pack2(float a, float b) {
  bf16x2 p; p[0] = (__bf16)a; p[1] = (__bf16)b;
  return __builtin_bit_cast(unsigned, p);
}
__device__ __forceinline__ float bflo(unsigned u) { return __builtin_bit_cast(float, u << 16); }
__device__ __forceinline__ float bfhi(unsigned u) { return __builtin_bit_cast(float, u & 0xffff0000u); }
__device__ __forceinline__ u32x4 trread2(lds_char* p0, lds_char* p1) {
  u32x2 lo = __builtin_bit_cast(u32x2, __builtin_amdgcn_ds_read_tr16_b64_v4i16((lds_s16x4*)p0));
  u32x2 hi = __builtin_bit_cast(u32x2, __builtin_amdgcn_ds_read_tr16_b64_v4i16((lds_s16x4*)p1));
  return u32x4{lo[0], lo[1], hi[0], hi[1]};
}
template <typename T> __device__ __forceinline__ T ldsr(lds_char* p) { return *(__attribute__((address_space(3))) T*)p; }
template <typename T> __device__ __forceinline__ void ldsw(lds_char* p, T v) { *(__attribute__((address_space(3))) T*)p = v; }
__device__ __forceinline__ f32x16 mfma32(u32x4 a, u32x4 b, f32x16 c) {
  return __builtin_amdgcn_mfma_f32_32x32x16_bf16(__builtin_bit_cast(bf16x8, a), __builtin_bit_cast(bf16x8, b), c, 0, 0, 0);
}
__device__ __forceinline__ u32x4 pack8(const f32x16& a, int s) {
  u32x4 r;
  r[0] = pack2(a[8 * s + 0], a[8 * s + 1]); r[1] = pack2(a[8 * s + 2], a[8 * s + 3]);
  r[2] = pack2(a[8 * s + 4], a[8 * s + 5]); r[3] = pack2(a[8 * s + 6], a[8 * s + 7]);
  return r;
}
__device__ __forceinline__ u32x2 deq4(u32x4 q, f32x4 s, f32x4 z) {
  float w0, w1, w2, w3;
  asm("v_cvt_f32_i32 %0, %4\n\tv_cvt_f32_i32 %1, %5\n\tv_cvt_f32_i32 %2, %6\n\tv_cvt_f32_i32 %3, %7\n\t"
      "v_fma_f32 %0, %0, %8, %12\n\tv_fma_f32 %1, %1, %9, %13\n\tv_fma_f32 %2, %2, %10, %14\n\tv_fma_f32 %3, %3, %11, %15\n\t"
      "v_cvt_pk_bf16_f32 %0, %0, %1\n\tv_cvt_pk_bf16_f32 %1, %2, %3"
      : "=&v"(w0), "=&v"(w1), "=&v"(w2), "=&v"(w3)
      : "v"(q[0]), "v"(q[1]), "v"(q[2]), "v"(q[3]), "v"(s[0]), "v"(s[1]), "v"(s[2]), "v"(s[3]), "v"(z[0]), "v"(z[1]), "v"(z[2]), "v"(z[3]));
  return u32x2{__builtin_bit_cast(unsigned, w0), __builtin_bit_cast(unsigned, w1)};
}
__device__ __forceinline__ void lds_spin(lds_u32* w, unsigned want) {
  while ((unsigned)__builtin_amdgcn_readfirstlane(__hip_atomic_load(w, RLX_WG)) < want) __builtin_amdgcn_s_sleep(1);
  asm volatile("" ::: "memory");
}
__device__ __forceinline__ void poll_flags(gu64* f, u64 tok) {
#pragma unroll 1
  for (unsigned spins = 0; spins < (1u << 20); ++spins) {
    if (__all(__hip_atomic_load(f, RLX_AGENT) == tok)) break;
    __builtin_amdgcn_s_sleep(POLL_SLEEP);
  }
  __builtin_amdgcn_fence(__ATOMIC_ACQUIRE, "wavefront");
}

template <int N, typename Fn, int... I>
__device__ __forceinline__ void static_for_impl(Fn&& f, std::integer_sequence<int, I...>) { (f(std::integral_constant<int, I>{}), ...); }
template <int N, typename Fn>
__device__ __forceinline__ void static_for(Fn&& f) { static_for_impl<N>(f, std::make_integer_sequence<int, N>{}); }
constexpr int x_ks(int i) { return i < 160 ? (i % 80) / 2 : i - 160; }
constexpr int x_stage(int i) { return i < 80 ? i / 8 : i < 160 ? 10 + (i - 80) / 16 : 15 + (i - 160) / 20; }
constexpr int x_uoff(int i) { return i < 80 ? 16384 + (x_ks(i) & 3) * 2048 : i < 160 ? (x_ks(i) & 7) * 2048 : (x_ks(i) % 20) * 1024; }
constexpr int x_nld(int st) { return st < 10 ? 6 : st < 15 ? 4 : st < 17 ? 5 : 0; }

constexpr int XRF_OFF = 2 << 20, SYNC_OFF = 6 << 20;
constexpr int FS = 16;
constexpr int FT = 1, FX = 121;
constexpr int FA = 185 * FS;
constexpr unsigned CODE_TOUCH_MAX = 20864;
constexpr int L_SYNC = 156160, L_DUMP = 156416, LDS_BYTES = 156672;

__global__ __launch_bounds__(512, 2) void quip(const __hip_bfloat16* __restrict__ x, const int* __restrict__ qw,
                                               const __hip_bfloat16* __restrict__ scales, const __hip_bfloat16* __restrict__ zeros,
                                               const __hip_bfloat16* __restrict__ U, const __hip_bfloat16* __restrict__ V,
                                               char* ws, __hip_bfloat16* __restrict__ y) {
  extern __shared__ __attribute__((aligned(16))) char smem_[];
  lds_char* smem = (lds_char*)smem_;
  const unsigned lb = (unsigned)(size_t)smem;
  const int wid = __builtin_amdgcn_readfirstlane(threadIdx.x >> 6), lane = threadIdx.x & 63;
  const int xcd = (XCD_PERM >> ((blockIdx.x & 7) * 4)) & 7, idx = blockIdx.x >> 3;
  {
    u64 pc;
    asm volatile("s_mov_b32 m0, %3\n\ts_cmp_gt_u32 %2, 7\n\ts_cbranch_scc1 1f\n\ts_getpc_b64 %0\n\ts_nop 0\n\tglobal_load_lds_dword %1, %0\n1:"
                 : "=&s"(pc) : "v"(min(threadIdx.x * 128, CODE_TOUCH_MAX)), "s"(blockIdx.x), "s"(lb + L_DUMP) : "memory", "scc");
    SB();
  }
  const int h = lane >> 5, r = lane & 31;
  const int blk = (lane >> 4) & 1, q4 = (lane & 15) >> 2, p4 = lane & 3;
  lds_u32* sw = (lds_u32*)(smem + L_SYNC);
  gu64* sync = (gu64*)(ws + SYNC_OFF);
  const __amdgpu_buffer_rsrc_t wsr = __builtin_amdgcn_make_buffer_rsrc(ws, 0, SYNC_OFF, 0x00020000);
  const bool is_pf = idx < 12 && xcd < 6;

  const int mb = (xcd >> 2) * 8 + (idx & 7);
  const int nb = (xcd & 3) * 4 + (idx >> 3);
  const unsigned fo = lane < 4 ? (FX + mb * 4 + lane) * FS * 8 : 0;
  u64 first = 0;
  auto ld_first = [&]() { asm volatile("global_load_dwordx2 %0, %1, %2 sc1" : "=v"(first) : "v"(fo), "s"(sync) : "memory"); };
  auto wait_first = [&]() { asm volatile("s_waitcnt vmcnt(0)" : "+v"(first)::"memory"); };
  if (wid == 3 && lane < 16) sw[lane] = ((lane == 8 || lane == 3) && is_pf) ? 4u : 0u;
  int myflag = 0;

  if (is_pf) {
    const int ga = xcd & 1, gb = xcd >> 1;
    if (wid < 2) {
      const bool isq = idx < 3 || (idx >= 7 && idx < 10), far = idx < 7;
      const unsigned vo_ = isq ? (lane >> 1) * (F_ * 4) + (lane & 1) * 128 : (lane >> 3) * (F_ * 2) + (lane & 7) * 128;
      const char* gbase = isq ? (const char*)qw + (size_t)(ga * 320) * (F_ * 4) : (const char*)V + gb * 1024;
      const int nc = isq ? 3 : (far ? 4 : 2), c0 = idx - (idx < 3 ? 0 : idx < 7 ? 3 : idx < 10 ? 7 : 10);
      const int ne = isq ? (far ? 80 : 160) : 96;
      BAR();
#pragma unroll 1
      for (int e = c0 + nc * wid; e < ne; e += nc * 2) {
        if (isq) {
          int t = far ? e * 3 + gb : (e >> 1) * 3 + (gb + 1 + (e & 1)) % 3;
          const int pp = t / 10, j = t - pp * 10;
          touch(gbase + j * (32 * F_ * 4) + pp * 256, vo_);
        } else {
          touch(gbase + (size_t)(e * 2 + (far ? ga : ga ^ 1)) * (8 * F_ * 2), vo_);
        }
      }
    } else {
      WAIT_L(0);
      BAR();
      if (wid == 4) { ld_first(); wait_first(); }
    }
  } else if (xcd < 6) {
    const int t = idx - 12;
    const int mt = (xcd & 1) * 5 + t % 5, nt = (xcd >> 1) * 4 + t / 5;
    myflag = FT + nt * 10 + mt;
    if (wid >= 4) {
      const int w4 = wid - 4, rr = lane >> 4, c16 = lane & 15;
      unsigned ag[4], bg[4];
#pragma unroll
      for (int i = 0; i < 4; ++i) {
        int row = i * 4 + rr;
        ag[i] = row * (F_ * 4) + ((c16 ^ row) * 16) - i * 1024;
        bg[i] = row * (F_ * 2) + ((c16 ^ (rr << 2)) * 16) - i * 1024;
      }
      const char* ap = (const char*)qw + (size_t)(mt * 64 + w4 * 16) * (F_ * 4);
      const char* bp = (const char*)V + nt * 256 + (size_t)(w4 * 16) * (F_ * 2);
      unsigned so = lb + w4 * 4096;
      auto issue = [&](int p) {
        unsigned base = so + (p & 3) * 32768;
        set_m0(base);
        glds<0>(ap, ag[0]); glds<1024>(ap, ag[1]); glds<2048>(ap, ag[2]); glds<3072>(ap, ag[3]);
        set_m0(base + 16384);
        glds<0>(bp, bg[0]); glds<1024>(bp, bg[1]); glds<2048>(bp, bg[2]); glds<3072>(bp, bg[3]);
        ap += 256; bp += 64 * F_ * 2;
      };
      issue(0);
      issue(1);
#pragma unroll 1
      for (int p = 2; p < 24; ++p) {
        WAIT_V(8);
        BAR();
        issue(p);
      }
      if (wid == 4) {
        ld_first();
        WAIT_V(9); BAR(); WAIT_V(1); BAR();
        wait_first();
      } else {
        WAIT_V(8); BAR(); WAIT_V(0); BAR();
      }
    } else {
      const int m = wid & 1, kl = wid >> 1;
      lds_char* TB = smem + 131072 + wid * 6144;
      u32x2 sr[3], zr[3];
      {
        const unsigned o = ((mt * 2 + m) * F_ + (lane >> 3) * 64 + kl * 32 + (lane & 7) * 4) * 2;
#pragma unroll
        for (int j = 0; j < 3; ++j) {
          sr[j] = *(const u32x2*)((const char*)scales + o + j * 1024);
          zr[j] = *(const u32x2*)((const char*)zeros + o + j * 1024);
        }
      }
      auto cvt = [&](int j) {
        f32x4 sa, za;
        sa[0] = bflo(sr[j][0]); sa[1] = bfhi(sr[j][0]); sa[2] = bflo(sr[j][1]); sa[3] = bfhi(sr[j][1]);
        za[0] = -bflo(zr[j][0]) * sa[0]; za[1] = -bfhi(zr[j][0]) * sa[1]; za[2] = -bflo(zr[j][1]) * sa[2]; za[3] = -bfhi(zr[j][1]) * sa[3];
        lds_char* d = TB + j * 2048 + (lane >> 3) * 256 + (lane & 7) * 16;
        ldsw<f32x4>(d, sa); ldsw<f32x4>(d + 128, za);
      };
      lds_char *ap[2][4], *bp[2][4];
#pragma unroll
      for (int c = 0; c < 4; ++c) {
        int ch = 8 * kl + 4 * (c >> 1) + 2 * h + (c & 1);
        ap[0][c] = smem + (m * 32 + r) * 256 + ((ch ^ (r & 15)) * 16);
        bp[0][c] = smem + 16384 + (32 * kl + 8 * h + q4) * 256 + ((c ^ (2 * kl) ^ q4) * 64) + 32 * (p4 & 1) + 16 * blk + 8 * (p4 >> 1);
        ap[1][c] = ap[0][c] + 65536; bp[1][c] = bp[0][c] + 65536;
      }
      lds_char* tp = TB + h * 32;
      f32x16 acc[4] = {};
      f32x4 ts[4], tz[4];
      u32x4 av[4];
      u32x4 af[2][2] = {}, bf[2][2][4] = {};
      auto rdtab = [&](int o, int c) {
        ts[c] = ldsr<f32x4>(tp + o + (c >> 1) * 64 + (c & 1) * 16);
        tz[c] = ldsr<f32x4>(tp + o + 128 + (c >> 1) * 64 + (c & 1) * 16);
      };
      auto step = [&](int slot) {
        const int buf = slot & 1;
        BAR();
#pragma unroll
        for (int c = 0; c < 4; ++c) av[c] = ldsr<u32x4>(ap[slot >> 1][c] + buf * 32768);
        SB();
#pragma unroll
        for (int u = 0; u < 8; ++u) {
          acc[u & 3] = mfma32(af[buf ^ 1][u >> 2], bf[buf ^ 1][u >> 2][u & 3], acc[u & 3]);
          SB();
          if (u < 4) {
#pragma unroll
            for (int v = 2 * u; v < 2 * u + 2; ++v)
              bf[buf][v >> 2][v & 3] = trread2(bp[slot >> 1][v & 3] + buf * 32768 + (v >> 2) * 4096, bp[slot >> 1][v & 3] + buf * 32768 + (v >> 2) * 4096 + 1024);
          } else {
            const int c = u - 4;
            u32x2 o = deq4(av[c], ts[c], tz[c]);
            af[buf][c >> 1][(c & 1) * 2] = o[0]; af[buf][c >> 1][(c & 1) * 2 + 1] = o[1];
            SB();
            rdtab((slot + 1) * 256, c);
          }
          SB();
        }
      };
#pragma unroll
      for (int c = 0; c < 4; ++c)
        asm volatile("" : "+v"(acc[c]), "+v"(bf[1][0][c]), "+v"(bf[1][1][c]), "+v"(ap[0][c]), "+v"(bp[0][c]), "+v"(ap[1][c]), "+v"(bp[1][c]));
      asm volatile("" : "+v"(af[1][0]), "+v"(af[1][1]));
      SB();
      cvt(0);
#pragma unroll
      for (int c = 0; c < 4; ++c) rdtab(0, c);
      cvt(1); cvt(2);
#pragma unroll 1
      for (int p = 0; p < 24; p += 4) {
        step(0); step(1); step(2); step(3);
        tp += 1024;
      }
      lds_char* RED = smem + m * 16384 + lane * 16;
      const int n32 = nt * 4 + kl * 2;
      const unsigned oo0 = (((n32 / 3) * 40 + (mt * 2 + m) * 2) * 3 + n32 % 3) * 1024;
      const unsigned oo1 = ((((n32 + 1) / 3) * 40 + (mt * 2 + m) * 2) * 3 + (n32 + 1) % 3) * 1024;
      SB();
#pragma unroll
      for (int u = 0; u < 8; ++u) {
        const int c = (u < 4 ? 2 : 0) + ((u >> 1) & 1), s2 = u & 1;
        acc[c] = mfma32(af[1][s2], bf[1][s2][c], acc[c]);
        SB();
        if (u >= 3 && u < 7) {
#pragma unroll
          for (int j = 2 * (u - 3); j < 2 * (u - 3) + 2; ++j) {
            const f32x16& g = acc[2 + (j >> 2)];
            const int q = 4 * (j & 3);
            ldsw<f32x4>(RED + kl * 8192 + j * 1024, f32x4{g[q], g[q + 1], g[q + 2], g[q + 3]});
          }
          SB();
        }
      }
      asm volatile("" ::: "memory");
      __hip_atomic_store(sw + 9 + wid, 1u, RLX_WG);
      while (__builtin_amdgcn_readfirstlane(__hip_atomic_load(sw + 9 + (wid ^ 2), RLX_WG)) == 0) {}
      asm volatile("" ::: "memory");
      f32x4 pv[8];
#pragma unroll
      for (int j = 0; j < 8; ++j) pv[j] = ldsr<f32x4>(RED + (kl ^ 1) * 8192 + j * 1024);
      SB();
#pragma unroll
      for (int j = 0; j < 4; ++j) {
        f32x16& o = acc[j >> 1];
#pragma unroll
        for (int e = 0; e < 8; ++e) o[(j & 1) * 8 + e] += pv[2 * j + (e >> 2)][e & 3];
        __builtin_amdgcn_raw_buffer_store_b128(pack8(o, j & 1), wsr, lane * 16 + (j & 1) * 3072, j < 2 ? oo0 : oo1, 16);
      }
    }
  } else {
    const int mi = idx & 7, eb = idx >> 3;
    const int xmb = (xcd - 6) * 8 + mi;
    const int ea = eb & 1;
    myflag = FX + xmb * 4 + eb;
    if (wid >= 4) {
      const int w4 = wid - 4;
      unsigned xg[4], ug[2];
#pragma unroll
      for (int i = 0; i < 4; ++i) {
        int row = i * 8 + (lane >> 3);
        xg[i] = row * (D_ * 2) + (((lane & 7) ^ ((row >> 1) & 7)) * 16) - i * 1024;
      }
#pragma unroll
      for (int i = 0; i < 2; ++i) ug[i] = (i * 8 + (lane >> 3)) * (D_ * 2) + (((lane & 7) ^ (((lane >> 4) & 1) << 2)) * 16) - i * 1024;
      const unsigned cg = (lane >> 2) * (D_ * 2) + (lane & 3) * 16;
      const char* xb = (const char*)x + (size_t)(xmb * 128 + w4 * 32) * (D_ * 2);
      const char* ub = (const char*)U + eb * 320 + ea * 64 + (size_t)(w4 * 16) * (D_ * 2);
      int ks = mi + ((eb * 5 + 1) >> 1);
      if (ks >= 10) ks -= 10;
      auto issue = [&](auto sc) {
        constexpr int st = decltype(sc)::value;
        const unsigned base = lb + (st % 5) * 24576;
        if constexpr (st < 10) {
          const char* xp = xb + ks * 128;
          const char* up = ub + (size_t)ks * 64 * (D_ * 2);
          set_m0(base + w4 * 4096);
          glds<0>(xp, xg[0]); glds<1024>(xp, xg[1]); glds<2048>(xp, xg[2]); glds<3072>(xp, xg[3]);
          set_m0(base + 16384 + w4 * 2048);
          glds<0>(up, ug[0]); glds<1024>(up, ug[1]);
          if (++ks == 10) ks = 0;
        } else if constexpr (st < 15) {
#pragma unroll
          for (int j = 0; j < 2; ++j) {
            const char* up = ub + 128 + (size_t)ks * 64 * (D_ * 2);
            set_m0(base + j * 8192 + w4 * 2048);
            glds<0>(up, ug[0]); glds<1024>(up, ug[1]);
            if (++ks == 10) ks = 0;
          }
        } else if constexpr (st < 17) {
#pragma unroll
          for (int j = 0; j < 5; ++j) {
            set_m0(base + j * 4096 + w4 * 1024);
            glds<0>(ub + (ea ? -64 : 256) + (size_t)ks * 64 * (D_ * 2), cg);
            if (++ks == 10) ks = 0;
          }
        }
      };
      issue(std::integral_constant<int, 0>{});
      issue(std::integral_constant<int, 1>{});
      static_for<17>([&](auto sc) {
        constexpr int st = decltype(sc)::value;
        asm volatile("s_waitcnt vmcnt(%0)" ::"i"(x_nld(st + 1)) : "memory");
        BAR();
        issue(std::integral_constant<int, st + 2>{});
      });
      if (wid == 4) { ld_first(); wait_first(); }
    } else {
      lds_char *xp[2][4], *up[2][2];
#pragma unroll
      for (int s = 0; s < 4; ++s) {
        xp[0][s] = smem + (wid * 32 + r) * 128 + (((2 * s + h) ^ ((r >> 1) & 7)) * 16);
        xp[1][s] = xp[0][s] + 61440;
        asm volatile("" : "+v"(xp[0][s]), "+v"(xp[1][s]));
      }
#pragma unroll
      for (int t = 0; t < 2; ++t) {
        up[0][t] = smem + (8 * h + q4) * 128 + ((t ^ ((q4 >> 1) & 1)) * 64) + blk * 32 + p4 * 8;
        up[1][t] = up[0][t] + 61440;
        asm volatile("" : "+v"(up[0][t]), "+v"(up[1][t]));
      }
      lds_char* cp = smem + (8 * h + q4) * 64 + blk * 32 + p4 * 8;
      const unsigned xo = XRF_OFF + ((xmb * 4 + wid) * 40 + eb * 10) * 1024;
      f32x16 acc[2] = {};
      u32x4 xf[40], uf[8];
      auto rd = [&](auto ic) {
        constexpr int i = decltype(ic)::value;
        if constexpr (i < 200) {
          if constexpr (i == 0 || x_stage(i) != x_stage(i - 1)) BAR();
          constexpr int so = (x_stage(i) % 5) * 24576, uo = so + x_uoff(i);
          if constexpr (i < 80 && (i & 1) == 0) xf[i / 2] = ldsr<u32x4>(xp[so / 61440][(i / 2) & 3] + so % 61440);
          if constexpr (i < 160) uf[i & 7] = trread2(up[uo / 61440][i & 1] + uo % 61440, up[uo / 61440][i & 1] + uo % 61440 + 512);
          else uf[i & 7] = trread2(cp + uo, cp + uo + 256);
        }
      };
      u32x4 pk[4];
      if (idx * 2 + wid < 60 && wid < 2) {
        const int t = idx * 2 + wid, rb = t / 6, j = t - rb * 6;
        touch((const char*)U + (size_t)rb * 64 * (D_ * 2) + ((0x976421 >> (4 * j)) & 15) * 128, lane * (D_ * 2));
      }
      WAIT_L(0);
      static_for<6>(rd);
      static_for<200>([&](auto ic) {
        constexpr int i = decltype(ic)::value;
        constexpr int a = i < 160 ? (i & 1) : 0;
        if constexpr (i == 80 || i == 160) {
#pragma unroll
          for (int j = 0; j < 4; ++j) pk[j] = pack8(acc[j >> 1], j & 1);
          acc[0] = f32x16{}; acc[1] = f32x16{};
          SB();
        }
        acc[a] = mfma32(uf[i & 7], xf[x_ks(i)], acc[a]);
        SB();
        rd(std::integral_constant<int, i + 6>{});
        if constexpr (i >= 80 && i % 80 < 8 && (i & 1) == 0)
          __builtin_amdgcn_raw_buffer_store_b128(pk[(i % 80) / 2], wsr, lane * 16 + ((i % 80) / 2) * 1024, xo + ea * 2048 + (i / 160) * 4096, 16);
        SB();
      });
      __builtin_amdgcn_raw_buffer_store_b128(pack8(acc[0], 0), wsr, lane * 16, xo + (ea ? 0 : 8192), 16);
      __builtin_amdgcn_raw_buffer_store_b128(pack8(acc[0], 1), wsr, lane * 16 + 1024, xo + (ea ? 0 : 8192), 16);
    }
  }

  const unsigned vo = lane * 16;
  if (wid < 4) {
    if (!is_pf && lane == 0) __hip_atomic_fetch_add(sw + 8, 1u, RLX_WG);
    const int n0 = (nb * 3) >> 2, n1 = (nb * 3 + 2) >> 2;
    gu64* tfl = sync + (FT + n0 * 10 + min(lane, (n1 - n0) * 10 + 9)) * FS;
    lds_spin(sw + 2, 1);
    const u64 tok = (u64)sw[0] | ((u64)sw[1] << 32);
    if (!is_pf) {
      asm volatile("s_waitcnt vmcnt(0) lgkmcnt(0)" ::: "memory");
      if (lane == 0) {
        if (__hip_atomic_fetch_add(sw + 3, 1u, RLX_WG) == 3) __hip_atomic_store(sync + myflag * FS, tok, RLX_AGENT);
      }
    }
    if (wid == 0) {
      lds_spin(sw + 8, 4);
      poll_flags(tfl, tok);
      if (lane == 0) __hip_atomic_store(sw + 5, 1u, RLX_WG);
    } else {
      lds_spin(sw + 5, 1);
    }
    const char* tsrc = ws + nb * 122880 + wid * 3072;
    unsigned tdst = lb + wid * 3072;
    auto issue = [&]() {
      asm volatile("s_mov_b32 m0, %2\n\ts_nop 0\n\tglobal_load_lds_dwordx4 %0, %1 sc1\n\tglobal_load_lds_dwordx4 %0, %1 offset:1024 sc1\n\t"
                   "global_load_lds_dwordx4 %0, %1 offset:2048 sc1" ::"v"(vo), "s"(tsrc), "s"(tdst) : "memory");
      tsrc += 12288; tdst += 12288;
    };
    issue(); issue(); issue(); issue();
#pragma unroll 1
    for (int ch = 0; ch < 6; ++ch) {
      WAIT_V(9);
      BAR();
      issue();
    }
    WAIT_V(9); BAR(); WAIT_V(6); BAR(); WAIT_V(3); BAR(); WAIT_V(0); BAR();
    if (blockIdx.x == 1 && wid == 2) {
#pragma unroll 1
      for (unsigned spins = 0; spins < (1u << 20); ++spins) {
        bool ok = true;
#pragma unroll
        for (int i = 0; i < 4; ++i) ok &= __hip_atomic_load(sync + FA + i * 64 + lane, RLX_AGENT) == tok;
        if (__all(ok)) break;
        __builtin_amdgcn_s_sleep(POLL_SLEEP);
      }
      if (lane == 0) __hip_atomic_store(sync, tok, RLX_AGENT);
    }
  } else {
    const int w4 = wid - 4;
    if (wid == 4) {
      const u64 tok = (((u64)__builtin_amdgcn_readlane((unsigned)(first >> 32), 4) << 32) | (unsigned)__builtin_amdgcn_readlane((unsigned)first, 4)) +
                      0x9E3779B97F4A7C15ull;
      if (lane == 0) {
        sw[0] = (unsigned)tok; sw[1] = (unsigned)(tok >> 32);
        asm volatile("" ::: "memory");
        __hip_atomic_store(sw + 2, 1u, RLX_WG);
        __hip_atomic_store(sync + FA + blockIdx.x, tok, RLX_AGENT);
      }
      if (!__all(lane >= 4 || first == tok)) poll_flags(sync + (FX + mb * 4 + (lane & 3)) * FS, tok);
      if (lane == 0) __hip_atomic_store(sw + 4, 1u, RLX_WG);
    } else {
      lds_spin(sw + 4, 1);
    }
    lds_spin(sw + 3, 4);
    u32x4 xf[40];
    const unsigned xo = XRF_OFF + (mb * 4 + w4) * 40960;
#pragma unroll
    for (int ch = 0; ch < XPRE / 4; ++ch) {
#pragma unroll
      for (int i = 0; i < 4; ++i) xf[ch * 4 + i] = __builtin_amdgcn_raw_buffer_load_b128(wsr, lane * 16 + i * 1024, xo + ch * 4096, 16);
    }
    SB();
    f32x16 acc[3] = {};
    u32x4 tf[3][3];
    lds_char* tb = smem + lane * 16;
    lds_char* tb2 = tb + 61440;
    asm volatile("" : "+v"(tb), "+v"(tb2));
    BAR();
#pragma unroll
    for (int c = 0; c < 6; ++c) tf[c / 3][c % 3] = ldsr<u32x4>(tb + c * 1024);
#pragma unroll
    for (int ks = 0; ks < 40; ++ks) {
#pragma unroll
      for (int c = 0; c < 3; ++c) {
        acc[c] = mfma32(tf[ks % 3][c], xf[ks], acc[c]);
        SB();
        if (ks < 38) {
          if (c == 0 && (ks & 3) == 2) BAR();
          tf[(ks + 2) % 3][c] = ldsr<u32x4>((ks + 2 < 20 ? tb : tb2) + (((ks + 2) % 20) * 3 + c) * 1024);
        }
        if (c == 1 && ks >= XOFF && ks - XOFF + XPRE < 40) {
          const int i = ks - XOFF + XPRE;
          xf[i] = __builtin_amdgcn_raw_buffer_load_b128(wsr, lane * 16 + (i & 3) * 1024, xo + (i >> 2) * 4096, 16);
        }
        SB();
      }
    }
    lds_char* eb = smem + 122880 + w4 * 6656;
#pragma unroll
    for (int c = 0; c < 3; ++c) {
      ldsw<u32x4>(eb + r * 208 + c * 64 + h * 32, pack8(acc[c], 0));
      ldsw<u32x4>(eb + r * 208 + c * 64 + h * 32 + 16, pack8(acc[c], 1));
    }
    const __amdgpu_buffer_rsrc_t yr = __builtin_amdgcn_make_buffer_rsrc((void*)y, 0, 2048 * F_ * 2, 0x00020000);
    const unsigned yo = ((mb * 128 + w4 * 32) * F_ + nb * 96) * 2;
#pragma unroll
    for (int i = 0; i < 6; ++i) {
      int q = i * 64 + lane, row = q / 12, chn = q % 12;
      __builtin_amdgcn_raw_buffer_store_b128(ldsr<u32x4>(eb + row * 208 + chn * 16), yr, row * (F_ * 2) + chn * 16, yo, 2  );
    }
  }
}

extern "C" void kernel_launch(void* const* d_in, const int* in_sizes, int n_in, void* d_out, int out_size, void* d_ws,
                              size_t ws_size, hipStream_t stream) {
  (void)hipFuncSetAttribute((const void*)quip, hipFuncAttributeMaxDynamicSharedMemorySize, LDS_BYTES);
  quip<<<256, 512, LDS_BYTES, stream>>>((const __hip_bfloat16*)d_in[0], (const int*)d_in[1], (const __hip_bfloat16*)d_in[2],
                                        (const __hip_bfloat16*)d_in[3], (const __hip_bfloat16*)d_in[4], (const __hip_bfloat16*)d_in[5],
                                        (char*)d_ws, (__hip_bfloat16*)d_out);
}
```
